```python
import torch

def _softmax(x, axis=-1):
    return torch.softmax(x, dim=axis)
def _gelu(x):
    return torch.nn.functional.gelu(x, approximate="tanh")
def _silu(x):
    return torch.nn.functional.silu(x)
def _sigmoid(x):
    return torch.sigmoid(x)
def _layernorm(x, axis=-1, eps=1e-5):
    m = x.mean(dim=axis, keepdim=True)
    v = x.var(dim=axis, keepdim=True, unbiased=False)
    return (x - m) / torch.sqrt(v + eps)
def _scatter_add(tgt, idx, src, axis=0):
    return tgt.clone().index_add_(axis, idx, src)
def _segment_sum(src, seg, n):
    out = torch.zeros((n,) + src.shape[1:], dtype=src.dtype)
    return out.index_add_(0, seg, src)
def _pad(x, pw):
    flat = tuple(v for pair in reversed(pw) for v in pair)
    return torch.nn.functional.pad(x, flat)

kernel_name = 'parallel_sum__spectral_gfnet__dense_activation_gelu__prompt'

def setup_inputs():

    _w = (lambda a: (lambda t: t.to(torch.bfloat16) if t.is_floating_point() else t)(torch.as_tensor(a)))
    def _A_setup():
        B, S, D = 1, 2048, 576
        Df = D // 2 + 1
        return {
            "x": _w((lambda *s: torch.randn(*s, dtype=torch.bfloat16) * 0.05)(B, S, D)),
            "w_r": _w((lambda *s: torch.randn(*s, dtype=torch.bfloat16) * 0.8)(S, Df) * 0.02),
            "w_i": _w((lambda *s: torch.randn(*s, dtype=torch.bfloat16) * 0.8)(S, Df) * 0.02),
        }
    _A_in = _A_setup()

    def _B_setup():
        B, S, D, F = 1, 2048, 576, 4608
        return {
            "x": _w((lambda *s: torch.randn(*s, dtype=torch.bfloat16) * 0.05)(B, S, D)),
            "w1": _w((lambda *s: torch.randn(*s, dtype=torch.bfloat16) * 0.4)(D, F) * (D ** -0.5)),
            "b1": _w((lambda *s: torch.randn(*s, dtype=torch.bfloat16) * 12.8)(F) * 0.01),
            "w2": _w((lambda *s: torch.randn(*s, dtype=torch.bfloat16) * 0.2)(F, D) * (F ** -0.5)),
            "b2": _w((lambda *s: torch.randn(*s, dtype=torch.bfloat16) * 1.6)(D) * 0.01),
        }
    _B_in = _B_setup()

    return {
        "A_x": _A_in["x"],
        "A_w_r": _A_in["w_r"],
        "A_w_i": _A_in["w_i"],
        "B_x": _B_in["x"],
        "B_w1": _B_in["w1"],
        "B_b1": _B_in["b1"],
        "B_w2": _B_in["w2"],
        "B_b2": _B_in["b2"],
    }

def reference(A_x, A_w_r, A_w_i, B_x, B_w1, B_b1, B_w2, B_b2):
    def _A_ref(x, w_r, w_i):
        B, S, D = x.shape
        X = (lambda x, axes=(-2,-1): torch.fft.rfft2(x.float(), dim=axes))(x, axes=(-2, -1))
        W = w_r + 1j * w_i
        X = X * W
        y = (lambda x, s, axes=(-2,-1): torch.fft.irfft2(x, s=s, dim=axes).to(torch.bfloat16))(X, (S, D), axes=(-2, -1))
        return y

    def _B_ref(x, w1, b1, w2, b2):
        h = torch.einsum("bsd,df->bsf", x, w1) + b1
        h = _gelu(h)
        return torch.einsum("bsf,fd->bsd", h, w2) + b2

    x = A_x
    xn = _layernorm(x, axis=-1)
    return x + _A_ref(xn, A_w_r, A_w_i) + _B_ref(xn, B_w1, B_b1, B_w2, B_b2)
```

```cpp
#include <hip/hip_runtime.h>
#include <hip/hip_bf16.h>
typedef __hip_bfloat16 bf16;
#define S_ 2048
#define D_ 576
#define DF_ 289
#define F_ 4608
__device__ __forceinline__ float rbf(float x){ return __bfloat162float(__float2bfloat16(x)); }
__global__ void ln_k(const bf16* x, float* xn){
  int s = blockIdx.x; int t = threadIdx.x;
  float v[9]; float sum=0;
  for(int i=0;i<9;i++){ v[i]=__bfloat162float(x[s*D_+t+64*i]); sum+=v[i]; }
  for(int o=32;o>0;o>>=1) sum+=__shfl_xor(sum,o);
  float mean=sum/D_; float sq=0;
  for(int i=0;i<9;i++){ float d=v[i]-mean; sq+=d*d; }
  for(int o=32;o>0;o>>=1) sq+=__shfl_xor(sq,o);
  float var=rbf(sq/D_); float m=rbf(mean);
  float sd=rbf(sqrtf(rbf(var+1e-5f)));
  for(int i=0;i<9;i++) xn[s*D_+t+64*i]=rbf(rbf(v[i]-m)/sd);
}
__global__ void dftd_k(const float* xn, float2* Xd){
  __shared__ float row[D_];
  int s=blockIdx.x; for(int i=threadIdx.x;i<D_;i+=blockDim.x) row[i]=xn[s*D_+i];
  __syncthreads();
  for(int k=threadIdx.x;k<DF_;k+=blockDim.x){
    float re=0,im=0; int idx=0;
    for(int d=0;d<D_;d++){ float sn,cs; sincospif(2.0f*idx/D_,&sn,&cs); re+=row[d]*cs; im-=row[d]*sn; idx+=k; if(idx>=D_) idx-=D_; }
    Xd[s*DF_+k]=make_float2(re,im);
  }
}
__global__ void tw_k(float2* tw){ int i=blockIdx.x*blockDim.x+threadIdx.x; if(i<S_){ float sn,cs; sincospif(2.0f*i/S_,&sn,&cs); tw[i]=make_float2(cs,sn);} }
__global__ void dfts_k(const float2* in, float2* out, const float2* tw, const bf16* wr, const bf16* wi, int fwd){
  __shared__ float2 t[S_];
  for(int i=threadIdx.x;i<S_;i+=blockDim.x) t[i]=tw[i];
  __syncthreads();
  int sp=blockIdx.x;
  for(int k=threadIdx.x;k<DF_;k+=blockDim.x){
    float re=0,im=0; int idx=0;
    for(int s=0;s<S_;s++){ float2 a=in[s*DF_+k]; float2 w=t[idx]; float ws=fwd?-w.y:w.y; re+=a.x*w.x-a.y*ws; im+=a.x*ws+a.y*w.x; idx=(idx+sp)&(S_-1);}
    if(fwd){ float a=__bfloat162float(wr[sp*DF_+k]), b=__bfloat162float(wi[sp*DF_+k]); float r2=re*a-im*b, i2=re*b+im*a; re=r2; im=i2; }
    out[sp*DF_+k]=make_float2(re,im);
  }
}
__global__ void idftd_k(const float2* Z, float* y){
  __shared__ float2 row[DF_];
  int s=blockIdx.x; for(int i=threadIdx.x;i<DF_;i+=blockDim.x) row[i]=Z[s*DF_+i];
  __syncthreads();
  for(int d=threadIdx.x;d<D_;d+=blockDim.x){
    float acc=0; int idx=0;
    for(int k=0;k<DF_;k++){ float sn,cs; sincospif(2.0f*idx/D_,&sn,&cs); float c=(k==0||k==288)?1.f:2.f; float im=(k==0||k==288)?0.f:row[k].y; acc+=c*(row[k].x*cs-im*sn); idx+=d; if(idx>=D_) idx-=D_; }
    y[s*D_+d]=rbf(acc/(float)(S_*D_));
  }
}
__global__ void ffn1_k(const float* xn, const bf16* w1, const bf16* b1, float* h){
  __shared__ float row[4][D_];
  int s0=blockIdx.x*4; for(int i=threadIdx.x;i<4*D_;i+=blockDim.x) row[i/D_][i%D_]=xn[s0*D_+i];
  __syncthreads();
  for(int f=threadIdx.x;f<F_;f+=blockDim.x){
    float a[4]={0,0,0,0};
    for(int d=0;d<D_;d++){ float w=__bfloat162float(w1[d*F_+f]); for(int r=0;r<4;r++) a[r]+=row[r][d]*w; }
    for(int r=0;r<4;r++){ float v=rbf(rbf(a[r])+__bfloat162float(b1[f])); float u=0.7978845608f*(v+0.044715f*v*v*v); h[(s0+r)*F_+f]=rbf(0.5f*v*(1.f+tanhf(u))); }
  }
}
__global__ void ffn2_k(const float* h, const bf16* w2, const bf16* b2, const bf16* x, const float* y, bf16* out){
  __shared__ float row[2][F_];
  int s0=blockIdx.x*2; for(int i=threadIdx.x;i<2*F_;i+=blockDim.x) row[i/F_][i%F_]=h[s0*F_+i];
  __syncthreads();
  for(int d=threadIdx.x;d<D_;d+=blockDim.x){
    float a[2]={0,0};
    for(int f=0;f<F_;f++){ float w=__bfloat162float(w2[f*D_+d]); a[0]+=row[0][f]*w; a[1]+=row[1][f]*w; }
    for(int r=0;r<2;r++){ int i=(s0+r)*D_+d; float v=rbf(rbf(a[r])+__bfloat162float(b2[d])); float t=rbf(__bfloat162float(x[i])+y[i]); out[i]=__float2bfloat16(t+v);}
  }
}
extern "C" void kernel_launch(void* const* d_in, const int* in_sizes, int n_in, void* d_out, int out_size, void* d_ws, size_t ws_size, hipStream_t stream) {
  const bf16* x=(const bf16*)d_in[0]; const bf16* wr=(const bf16*)d_in[1]; const bf16* wi=(const bf16*)d_in[2];
  const bf16* w1=(const bf16*)d_in[4]; const bf16* b1=(const bf16*)d_in[5]; const bf16* w2=(const bf16*)d_in[6]; const bf16* b2=(const bf16*)d_in[7];
  char* ws=(char*)d_ws;
  float* xn=(float*)ws; ws+=S_*D_*4;
  float2* Xd=(float2*)ws; ws+=S_*DF_*8;
  float2* Y=(float2*)ws; ws+=S_*DF_*8;
  float2* Z=(float2*)ws; ws+=S_*DF_*8;
  float* y=(float*)ws; ws+=S_*D_*4;
  float2* tw=(float2*)ws; ws+=S_*8;
  float* h=(float*)ws;
  ln_k<<<S_,64,0,stream>>>(x,xn);
  tw_k<<<8,256,0,stream>>>(tw);
  dftd_k<<<S_,320,0,stream>>>(xn,Xd);
  dfts_k<<<S_,320,0,stream>>>(Xd,Y,tw,wr,wi,1);
  dfts_k<<<S_,320,0,stream>>>(Y,Z,tw,wr,wi,0);
  idftd_k<<<S_,576,0,stream>>>(Z,y);
  ffn1_k<<<S_/4,256,0,stream>>>(xn,w1,b1,h);
  ffn2_k<<<S_/2,576,0,stream>>>(h,w2,b2,x,y,(bf16*)d_out);
}
```

```cpp
#include <hip/hip_runtime.h>
#include <hip/hip_bf16.h>
#include <utility>

typedef unsigned short u16;
typedef __attribute__((ext_vector_type(8))) __bf16 bf16x8;
typedef __attribute__((ext_vector_type(2))) __bf16 bf16x2;
typedef __attribute__((ext_vector_type(4))) short s16x4;
typedef __attribute__((ext_vector_type(8))) short s16x8;
typedef __attribute__((ext_vector_type(4))) float f32x4;
typedef __attribute__((ext_vector_type(16))) float f32x16;
typedef __attribute__((ext_vector_type(4))) unsigned u32x4;
typedef __attribute__((ext_vector_type(2))) unsigned u32x2;
#define LAS __attribute__((address_space(3)))
#define GAS __attribute__((address_space(1)))
#define DI __device__ __forceinline__

namespace tab {
constexpr double PI_ = 3.14159265358979323846;
constexpr double tsin(double x) { double t = x, s = x; for (int i = 1; i < 12; i++) { t *= -x * x / ((2 * i) * (2 * i + 1)); s += t; } return s; }
constexpr double tcos(double x) { double t = 1, s = 1; for (int i = 1; i < 12; i++) { t *= -x * x / ((2 * i - 1) * (2 * i)); s += t; } return s; }
constexpr double ccos(int n, int N) {
  n %= N; if (n < 0) n += N;
  if (2 * n > N) n = N - n;
  bool neg = false;
  if (4 * n > N) { n = N / 2 - n; neg = true; }
  double v = (8 * n > N) ? tsin(2 * PI_ * (N / 4 - n) / N) : tcos(2 * PI_ * n / N);
  return neg ? -v : v;
}
constexpr double csin(int n, int N) { return ccos(n - N / 4, N); }
constexpr u16 f2bf(double d) { float f = (float)d; unsigned u = __builtin_bit_cast(unsigned, f); u += 0x7fffu + ((u >> 16) & 1u); return (u16)(u >> 16); }
struct C576 { u16 c[576]; u16 g1[576]; u16 g2[576]; };
constexpr C576 mk576() { C576 r{}; for (int i = 0; i < 576; i++) { double c = ccos(i, 576); r.c[i] = f2bf(c); r.g1[i] = f2bf(c / 576.0); r.g2[i] = f2bf(2.0 * c / 576.0); } return r; }
constexpr C576 c576 = mk576();
struct Blk { u16 v[9216]; };
constexpr int fidx(int bc, int d) {
  int n = bc < 144 ? bc * d : (bc == 144 ? ((d & 1) == 0 ? 144 * d : 144 * d + 144) : (bc - 144) * d + 144);
  return n % 576;
}
constexpr Blk mkF(int idx) {
  Blk r{}; int at = idx >> 1, hf = idx & 1;
  for (int st = 0; st < 18; st++) for (int hh = 0; hh < 2; hh++) for (int j = 0; j < 8; j++) {
    int d = 288 * hf + 32 * (st >> 1) + 16 * (j >> 2) + 8 * hh + 2 * (j & 3) + (st & 1);
    for (int cc = 0; cc < 32; cc++) r.v[((st * 2 + hh) * 8 + j) * 32 + cc] = c576.c[fidx(32 * at + cc, d)];
  }
  return r;
}
template <int I> constexpr Blk fblk = mkF(I);
struct FTab { Blk b[18]; };
template <int... I> constexpr FTab mkFT(std::integer_sequence<int, I...>) { return FTab{{fblk<I>...}}; }
constexpr double bf2d(u16 b) { return (double)__builtin_bit_cast(float, (unsigned)b << 16); }
struct FCB { unsigned v[64]; };
constexpr FCB mkFC(int at) {
  FCB r{};
  for (int e = 0; e < 2; e++) for (int cc = 0; cc < 32; cc++) {
    double s = 0;
    for (int d = e; d < 576; d += 2) s += bf2d(c576.c[fidx(32 * at + cc, d)]);
    u16 hi = f2bf(s), lo = f2bf(s - bf2d(hi));
    r.v[e * 32 + cc] = (unsigned)hi | ((unsigned)lo << 16);
  }
  return r;
}
template <int I> constexpr FCB fcb = mkFC(I);
struct FCT { FCB b[9]; };
template <int... I> constexpr FCT mkFCT(std::integer_sequence<int, I...>) { return FCT{{fcb<I>...}}; }
struct GBlk { u16 v[4608]; };
constexpr GBlk mkG(int idx) {
  GBlk r{}; int step = idx % 5, q = (idx / 5) & 3, b = idx / 20;
  for (int row = 0; row < 32; row++) {
    int rho = 32 * step + row;
    if (rho >= 144) continue;
    int kc = 72 * b + (rho >> 1), ri = rho & 1;
    for (int c = 0; c < 144; c++) {
      int d = 144 * q + c;
      r.v[row * 144 + c] = kc == 0 ? c576.g1[(ri * 288 * d) % 576] : c576.g2[(kc * d + ri * 144) % 576];
    }
  }
  return r;
}
template <int I> constexpr GBlk gblk = mkG(I);
struct GTab { GBlk b[80]; };
template <int... I> constexpr GTab mkGT(std::integer_sequence<int, I...>) { return GTab{{gblk<I>...}}; }
struct C2048 { float c[2048]; };
constexpr C2048 mk2048() { C2048 r{}; for (int i = 0; i < 2048; i++) r.c[i] = (float)ccos(i, 2048); return r; }
constexpr C2048 c2048 = mk2048();
constexpr u16 trig(int n2048, int type) {
  int n = type == 0 ? n2048 : (type == 1 ? n2048 + 1536 : n2048 + 512);
  return f2bf(c2048.c[n & 2047]);
}
struct F64T { u16 v[4][2][3][64][8]; };
constexpr F64T mkF64(int which) {
  F64T r{};
  for (int j = 0; j < 4; j++) for (int t = 0; t < 2; t++) for (int ty = 0; ty < 3; ty++) for (int l = 0; l < 64; l++) for (int jj = 0; jj < 8; jj++) {
    int g = l >> 4, o = 16 * j + (l & 15);
    int k = which == 0 ? 32 * t + 16 * (g >> 1) + 8 * (jj >> 2) + 4 * (g & 1) + (jj & 3) : 32 * t + 16 * (jj >> 2) + 4 * g + (jj & 3);
    r.v[j][t][ty][l][jj] = trig(o * k * 32, ty);
  }
  return r;
}
struct F32T { u16 v[2][3][64][8]; };
constexpr F32T mkF32() {
  F32T r{};
  for (int ct = 0; ct < 2; ct++) for (int ty = 0; ty < 3; ty++) for (int l = 0; l < 64; l++) for (int jj = 0; jj < 8; jj++) {
    int g = l >> 4, o = 16 * ct + (l & 15), k = 16 * (jj >> 2) + 4 * g + (jj & 3);
    r.v[ct][ty][l][jj] = trig(o * k * 64, ty);
  }
  return r;
}
struct TWT { float v[4][4][64][4]; };
constexpr TWT mkTW(int which) {
  TWT r{};
  for (int j = 0; j < 4; j++) for (int n = 0; n < 4; n++) for (int l = 0; l < 64; l++) for (int i = 0; i < 4; i++) {
    int g = l >> 4, at = n >> 1;
    int a = which == 0 ? 16 * at + 4 * g + i : 16 * at + (l & 15);
    int d = which == 0 ? 16 * j + (l & 15) : 16 * j + 4 * g + i;
    float v = c2048.c[(a * d + ((n & 1) ? 1536 : 0)) & 2047];
    r.v[j][n][l][i] = which == 0 ? v : v * (1.0f / 2048.0f);
  }
  return r;
}
constexpr F64T f64a = mkF64(0), f64b = mkF64(1);
constexpr F32T f32t = mkF32();
constexpr TWT tw1 = mkTW(0), tw2 = mkTW(1);
struct Small { F64T a, b; F32T f; TWT t1, t2; FCT fc; };
}
#if defined(__HIP_DEVICE_COMPILE__)
__device__ __attribute__((aligned(128))) static const tab::FTab FT = tab::mkFT(std::make_integer_sequence<int, 18>{});
__device__ __attribute__((aligned(128))) static const tab::GTab GT = tab::mkGT(std::make_integer_sequence<int, 80>{});
__device__ __attribute__((aligned(128))) static const tab::Small ST = {tab::f64a, tab::f64b, tab::f32t, tab::tw1, tab::tw2, tab::mkFCT(std::make_integer_sequence<int, 9>{})};
#else
__device__ static const tab::FTab FT = {};
__device__ static const tab::GTab GT = {};
__device__ static const tab::Small ST = {};
#endif
typedef unsigned long long u64;
DI float bfr(float x) { return (float)(__bf16)x; }
DI unsigned pk2(float a, float b) { bf16x2 c; c[0] = (__bf16)a; c[1] = (__bf16)b; return __builtin_bit_cast(unsigned, c); }
DI float blo(unsigned u) { return __builtin_bit_cast(float, u << 16); }
DI float bhi(unsigned u) { return __builtin_bit_cast(float, u & 0xffff0000u); }
DI f32x16 mfma32(u32x4 a, u32x4 b, f32x16 c) { return __builtin_amdgcn_mfma_f32_32x32x16_bf16(__builtin_bit_cast(bf16x8, a), __builtin_bit_cast(bf16x8, b), c, 0, 0, 0); }
DI f32x4 mfma16(u32x4 a, u32x4 b, f32x4 c) { return __builtin_amdgcn_mfma_f32_16x16x32_bf16(__builtin_bit_cast(bf16x8, a), __builtin_bit_cast(bf16x8, b), c, 0, 0, 0); }
DI u32x2 trd(LAS char* p) { return __builtin_bit_cast(u32x2, __builtin_amdgcn_ds_read_tr16_b64_v4i16((LAS s16x4*)p)); }
DI u32x4 trd2(LAS char* p, int off2) { u32x2 a = trd(p), b = trd(p + off2); u32x4 r; r[0] = a[0]; r[1] = a[1]; r[2] = b[0]; r[3] = b[1]; return r; }
template <int C> DI float dppadd(float v) { return v + __builtin_bit_cast(float, __builtin_amdgcn_mov_dpp(__builtin_bit_cast(int, v), C, 0xf, 0xf, true)); }
DI float rl(float v, int l) { return __builtin_bit_cast(float, __builtin_amdgcn_readlane(__builtin_bit_cast(int, v), l)); }
DI float wave_sum(float v) {
  v = dppadd<0xB1>(v); v = dppadd<0x4E>(v); v = dppadd<0x141>(v); v = dppadd<0x140>(v);
  return (rl(v, 0) + rl(v, 16)) + (rl(v, 32) + rl(v, 48));
}
DI u32x4 mkdesc(const void* p, unsigned bytes) { u64 a = (u64)p; return u32x4{(unsigned)a, (unsigned)(a >> 32) & 0xffffu, bytes, 0x00020000u}; }
typedef __amdgpu_buffer_rsrc_t rsrc_t;
DI rsrc_t mkrsrc(const void* p, unsigned bytes) { return __builtin_amdgcn_make_buffer_rsrc((void*)p, 0, bytes, 0x00020000); }
DI void st16(rsrc_t r, unsigned voff, unsigned soff, u32x4 v) {
  __builtin_amdgcn_raw_buffer_store_b128(v, r, voff, soff, 16);
  asm volatile("s_nop 1" ::"v"(v) : "memory");
}
DI u32x4 ld16(rsrc_t r, unsigned voff, unsigned soff) { return __builtin_amdgcn_raw_buffer_load_b128(r, voff, soff, 16); }
#define GLDS(d, voff, sbase, simm, lbase, limm) do { unsigned t_; \
  asm volatile("s_add_i32 m0, %1, %2\n\ts_add_i32 %0, %3, %4\n\tbuffer_load_dwordx4 %5, %6, %0 offen lds" \
               : "=&s"(t_) : "s"(lbase), "i"(limm), "s"(sbase), "i"(simm), "v"(voff), "s"(d) : "memory", "scc"); } while (0)
#define WAITV(n) asm volatile("s_waitcnt vmcnt(%0)" ::"i"(n) : "memory")
#define WAITL(n) asm volatile("s_waitcnt lgkmcnt(%0)" ::"i"(n) : "memory")
#define PIN(x) asm volatile("" : "+v"(x))
#define BARX() asm volatile("s_barrier" ::: "memory")
#define SB() __builtin_amdgcn_sched_barrier(0)
#define AI __attribute__((always_inline))
#if defined(__HIP_DEVICE_COMPILE__)
#define NOPK __attribute__((target("no-packed-fp32-ops")))
#else
#define NOPK
#endif
template <int N> struct IC { static constexpr int v = N; };
template <class F, int... I> DI void sfor_(F&& f, std::integer_sequence<int, I...>) { (f(IC<I>{}), ...); }
template <int N, class F> DI void sfor(F&& f) { sfor_(f, std::make_integer_sequence<int, N>{}); }

#define BLD(op, dst, d, voff, soff, pol) asm volatile("buffer_load_" op " %0, %1, %2, %3 offen" pol : "=v"(dst) : "v"(voff), "s"(d), "s"(soff) : "memory")
DI float asf(unsigned u) { return __builtin_bit_cast(float, u); }
DI unsigned asu(float f) { return __builtin_bit_cast(unsigned, f); }
DI float swadd32(float a, float b) { auto s = __builtin_amdgcn_permlane32_swap(asu(a), asu(b), false, false); const unsigned x = s[0], y = s[1]; return asf(x) + asf(y); }
DI float swadd16(float a, float b) { auto s = __builtin_amdgcn_permlane16_swap(asu(a), asu(b), false, false); const unsigned x = s[0], y = s[1]; return asf(x) + asf(y); }

constexpr int SLOT = 36864, SP = 4 * SLOT, LDSZ = 163840;
constexpr int SP_T = SP, SP_CS = SP + 4608, SP_RS = SP + 6656;
constexpr unsigned WS_E = 0, WS_F1 = 1024, WS_FH = 2048, WS_FZ = 3072, WS_FP = 4096, WS_ERR = 5120, WS_ST = 8192;
constexpr unsigned WS_CS = 32768;
constexpr unsigned WS_H = WS_CS + 3 * 36864;
constexpr unsigned WS_XS = WS_H + 64u * 144 * 2048;
constexpr unsigned WS_Z = WS_XS + 576u * 4096;
constexpr unsigned WS_WT = WS_Z + 576u * 4096;
constexpr unsigned WS_P = WS_WT + 289u * 8192;
constexpr unsigned WS_END = WS_P + 4096u * 5120;
constexpr int SPIN = 200000;
constexpr unsigned OOB = 0x80000000u;
DI void put_flag(rsrc_t rw, bool on, unsigned off, unsigned tag) { __builtin_amdgcn_raw_buffer_store_b32(tag, rw, on ? 0u : OOB, off, 16); }
DI bool bad4(u32x4 v, unsigned tag) { return __builtin_amdgcn_ballot_w64(v[0] != tag || v[1] != tag || v[2] != tag || v[3] != tag) != 0; }
DI void poll4(rsrc_t rw, u32x4 dws, u32x4 v, unsigned voff, unsigned soff, unsigned tag) {
  for (int it = 0; bad4(v, tag); it++) {
    if (it >= SPIN) { put_flag(rw, true, WS_ERR, 1); break; }
    __builtin_amdgcn_s_sleep(1);
    BLD("dwordx4", v, dws, voff, soff, " sc1");
    asm volatile("s_waitcnt vmcnt(0)" : "+v"(v)::"memory");
  }
}
#define DIST1 4
#define RING1 5
#define DIST2 4
#define RING2 5
__global__ __launch_bounds__(512, 2) NOPK void kmain(const u16* __restrict__ x, const u16* __restrict__ wr, const u16* __restrict__ wi,
                                                     const u16* __restrict__ w1, const u16* __restrict__ b1, const u16* __restrict__ w2,
                                                     const u16* __restrict__ b2, u16* __restrict__ out, char* __restrict__ ws) {
  extern __shared__ __attribute__((aligned(16))) char smem_[];
  LAS char* smem = (LAS char*)smem_;
  const int tid = threadIdx.x, lane = tid & 63;
  const int wv = __builtin_amdgcn_readfirstlane(tid >> 6);
  auto body = [&](auto SETC) AI {
  constexpr int SET = decltype(SETC)::v;
  const int id = blockIdx.x, xcd = id & 7, slot = id >> 3;
  const int rt = (xcd >> 2) * 8 + (slot >> 2), bb = xcd & 3, q = slot & 3, nt = bb * 4 + q, lid = rt * 16 + nt;
  const int aq = rt >> 1, bh = rt & 1, c = wv & 3;
  const int fl0 = (nt & 1) * 32, flh = (nt & 1) ? 0 : 256;
  const bool has_dft = nt < 9;
  const rsrc_t rw = mkrsrc(ws, WS_END);
  const u32x4 dws = mkdesc(ws, WS_END), dx = mkdesc(x, 2048 * 1152), dw1 = mkdesc(w1, 576 * 9216), dw2 = mkdesc(w2, 4608 * 1152);
  const int r = lane & 31, hh = lane >> 5;
  unsigned ev, xs[5];
  {
    asm volatile("global_load_dword %0, %1, %2 sc1" : "=v"(ev) : "v"(0), "s"(ws) : "memory");
    const int tr = 8 * nt + wv;
    const unsigned ro = (4 * aq + (tr >> 5) + 32 * (32 * bh + (tr & 31))) * 1152u;
#pragma unroll
    for (int i = 0; i < 5; i++) asm volatile("global_load_dword %0, %1, %2 offset:%3" : "=v"(xs[i]) : "v"(ro + (i < 4 ? lane : r) * 4), "s"(x), "i"(256 * i) : "memory");
  }
  const unsigned xvo = (4 * aq + (wv >> 1) + 32 * (32 * bh + 16 * (wv & 1) + (lane >> 2))) * 1152u + (((lane & 3) ^ (lane >> 4)) * 16);
  const unsigned xlb = 2 * SLOT + wv * 9216;
#pragma unroll
  for (int n = 0; n < 9; n++) GLDS(dx, xvo, 0, 64 * n, xlb, 1024 * n);
  const unsigned wvo = (8 * c + ((lane >> 2) & 7)) * 9216u + hh * 64 + (lane & 3) * 16;
  const unsigned wso = (288 * nt + fl0) * 2, wlb = c * 1024;
  const u32x4 dft = mkdesc(&FT, has_dft ? sizeof(FT) : 0);
  const u32x4 dm = c < 2 ? dw1 : dft;
  const unsigned mvo = c < 2 ? (144 * c + (lane >> 2)) * 9216u + (lane & 3) * 16 : lane * 16;
  const unsigned mso = c < 2 ? (288 * nt + flh) * 2 : nt * 36864 + (c & 1) * 9216;
  const unsigned mstep = c < 2 ? 16 * 9216 : 1024, mhf = c < 2 ? 288 * 9216 : 18432, mlb = c * 9216;
  if (SET == 0) {
#pragma unroll
    for (int n = 0; n < 9; n++) GLDS(dm, mvo, mso + n * mstep, 0, mlb, 1024 * n);
  }
  asm volatile("s_waitcnt vmcnt(%6)" : "+v"(ev), "+v"(xs[0]), "+v"(xs[1]), "+v"(xs[2]), "+v"(xs[3]), "+v"(xs[4]) : "i"(SET == 0 ? 18 : 9) : "memory");
  const unsigned tag = __builtin_amdgcn_readfirstlane(ev) * 0x9E3779B1u + 0x7F4A7C15u;
  {
    if (hh) xs[4] = 0;
    float s = 0.f;
#pragma unroll
    for (int i = 0; i < 5; i++) s += blo(xs[i]) + bhi(xs[i]);
    const float mean = wave_sum(s) * (1.0f / 576.0f);
    float v = 0.f;
#pragma unroll
    for (int i = 0; i < 5; i++) { float d0 = blo(xs[i]) - mean, d1 = bhi(xs[i]) - mean; float t = d0 * d0 + d1 * d1; v += (i == 4 && hh) ? 0.f : t; }
    const float var = wave_sum(v) * (1.0f / 576.0f);
    const unsigned ms = pk2(mean, __builtin_sqrtf(bfr(bfr(var) + 1e-5f)));
    __builtin_amdgcn_raw_buffer_store_b64(u32x2{ms, tag}, rw, lane == 0 ? 0u : OOB, WS_ST + (rt * 128 + 8 * nt + wv) * 8, 16);
  }
  if (SET == 1) {
#pragma unroll
    for (int n = 0; n < 9; n++) GLDS(dw1, wvo, wso, n * 32 * 9216, wlb, SLOT + 4096 * n);
  }
  const int cu = (xcd >> 2) * 32 + slot, cg = 18 * bb + cu / 3, g3 = cu % 3;
  u32x4 uu[3];
#pragma unroll
  for (int n = 0; n < 3; n++) BLD("dwordx4", uu[n], dw1, cu < 54 ? (lane >> 3) * 9216u + (lane & 7) * 16 : OOB, (192 * g3 + 24 * wv + 8 * n) * 9216 + cg * 128, "");
  const int q4 = (lane & 15) >> 2, p4 = lane & 3, g = lane >> 4, l15 = lane & 15;
  const unsigned hoff = (r >> 4) * 1024 + hh * 256 + (r & 15) * 16;
  const unsigned hrow = WS_H + (4 * rt + c) * 144 * 2048;
  const unsigned hso = hrow + 9 * nt * 2048;
  unsigned w2o[9];
  auto ftile = [&](int ks) AI {
    const int qq = (q + (ks < 20 ? ks / 5 : (ks - 20) >> 2)) & 3, e = ks < 20 ? ks % 5 : 5 + ((ks - 20) & 3);
    return 36 * bb + 9 * qq + ((qq & 1) ? e : (e == 0 ? 8 : e - 1));
  };
  auto w2stage = [&](unsigned so, int j, int n) AI { GLDS(dw2, w2o[n], so, 0, mlb, (j & 3) * SLOT + 1024 * n); };
  int cnt = 0, mark[12];
  u32x4 hb[12][2];
  int mh[36];
  auto hload = [&](int ks) AI {
    const unsigned so = hrow + ftile(ks) * 2048;
    BLD("dwordx4", hb[ks % 12][0], dws, lane * 16, so, " sc1");
    BLD("dwordx4", hb[ks % 12][1], dws, lane * 16, so, " offset:1024 sc1");
    cnt += 2; mh[ks] = cnt;
  };
  u32x4 pf;
  int mpf, mst;

  {
    constexpr int D = DIST1, R = RING1;
    const unsigned tq = q4 * 64 + (g & 1) * 32 + p4 * 8;
    LAS char* const tbP = smem + hh * 1024 + tq + SET * 512;
    LAS char* const tbM = smem + hh * 512 + tq + SET * 18432;
    LAS char* const xe = smem + 2 * SLOT + (2 * c + (r >> 4)) * 9216 + (r & 15) * 64 + ((hh ^ ((r >> 2) & 3)) * 16);
    LAS char* const xo = smem + 2 * SLOT + (2 * c + (r >> 4)) * 9216 + (r & 15) * 64 + (((2 + hh) ^ ((r >> 2) & 3)) * 16);
    u32x4 xf[36], ring[R], pm[2], sf, tf, xq[4];
    u32x2 sg, cq[3];
    unsigned bia, fc[2], wl[8], w2s;
    f32x16 acc[6];
    f32x4 cr[2];
    float cs[8], gt[3][8], ge[3][8], k0, k1, sdf, rstd;
    u32x4 go[3];
    LAS char* bs[11];
    int mw;
    const f32x16 zero = {0, 0, 0, 0, 0, 0, 0, 0, 0, 0, 0, 0, 0, 0, 0, 0};
    constexpr auto isM = [](int k) constexpr { return k == 0 || k == 2; };
    constexpr auto tile = [](int k) constexpr { return k < 4 ? (k & 1) : (k >> 1); };
    constexpr auto half = [](int k) constexpr { return k < 4 ? (k >> 1) : (k & 1); };
    constexpr auto slotof = [](int k) constexpr { return k < 2 ? k : (k + 2) & 3; };
    auto fetch = [&](int F) AI { const int k = F / 18, st = F % 18; ring[F % R] = trd2(bs[k] + st * (isM(k) ? 1024 : 2048), 256); };
    auto xread = [&](int i) AI { const int st = i % 18; xf[i] = *(LAS u32x4*)((st & 1 ? xo : xe) + (st >> 1) * 1024); };
    auto perm = [&](int T) AI {
      const int st = T % 18, b = 18 * half(T / 18) + (st & ~1); const unsigned sel = (st & 1) ? 0x07060302u : 0x05040100u;
      u32x4 o;
      o[0] = __builtin_amdgcn_perm(xf[b][1], xf[b][0], sel); o[1] = __builtin_amdgcn_perm(xf[b][3], xf[b][2], sel);
      o[2] = __builtin_amdgcn_perm(xf[b + 1][1], xf[b + 1][0], sel); o[3] = __builtin_amdgcn_perm(xf[b + 1][3], xf[b + 1][2], sel);
      PIN(o); pm[st & 1] = o;
    };
    auto gstep = [&](int t, int u, int s, int z) AI {
      float* T_ = gt[z]; float* E_ = ge[z]; const int i = s >> 1;
      auto v = [&](int n) AI { return acc[t][8 * u + n]; };
      if (s == 0) { T_[0] = v(0) * v(0); PIN(T_[0]); }
      if ((s & 1) == 0) {
        if (i < 8) { T_[i] = v(i) * __builtin_fmaf(T_[i], k1, k0); PIN(T_[i]); }
        if (i >= 1) { E_[i - 1] = __builtin_amdgcn_rcpf(__builtin_fmaf(E_[i - 1], sdf, sdf)); PIN(E_[i - 1]); }
      } else {
        if (i < 8) { E_[i] = __builtin_amdgcn_exp2f(T_[i]); PIN(E_[i]); }
        if (i >= 1) { T_[i - 1] = v(i - 1) * E_[i - 1]; if ((i & 1) == 0) { unsigned w_ = pk2(T_[i - 2], T_[i - 1]); PIN(w_); go[z][(i >> 1) - 1] = w_; } else PIN(T_[i - 1]); }
        if (i + 1 < 8) { T_[i + 1] = v(i + 1) * v(i + 1); PIN(T_[i + 1]); }
      }
      if (s == 17) { st16(rw, hoff, hso + (t == 0 ? flh : fl0 + 64 * (t - 1) + 32 * SET) * 64 + u * 512, go[z]); cnt++; if (t == 2) mst = cnt; }
    };
    const int gl = 36 * wv + (lane < 35 ? lane : 35);
    auto gather = [&]() AI {
      BLD("dwordx2", sg, dws, r * 8, WS_ST + (rt * 128 + c * 32) * 8, " sc1");
#pragma unroll
      for (int i = 0; i < 3; i++) BLD("dwordx2", cq[i], dws, gl * 8, WS_CS + i * 36864 + nt * 2304, " sc1");
    };
    WAITV(SET == 0 ? 4 : 13); BARX();
    bs[0] = tbM; PIN(bs[0]);
    xread(0); xread(1);
#pragma unroll
    for (int F = 0; F < D; F++) fetch(F);
    xread(2);
    if (SET == 1) perm(0);
    SB();
    sfor<180>([&](auto TC_) AI {
      constexpr int T = decltype(TC_)::v, k = T / 18, st = T % 18, F = T + D, t = tile(k);
      if constexpr (T == 6) { WAITL(2); BARX(); }
      if constexpr (T == 26) { WAITV(SET == 0 ? 9 : 1); BARX(); }
      if constexpr (T == 63) { if (SET == 1) WAITL(4); BARX(); }
      if constexpr (F % 18 == 0) {
        constexpr int kb = F / 18;
        if (kb == 10) WAITV(cnt - (SET == 1 ? mark[10] : mst));
        else if (kb >= 2 && (kb & 1) == SET) WAITV(cnt - mark[kb]);
        if (kb <= 2 || kb == 10) WAITL(0); else if (kb == 3) WAITL(4);
        BARX();
        if (kb == 10 && SET == 0) { put_flag(rw, tid == 0, WS_F1 + lid * 4, tag); cnt++; }
        if constexpr (kb < 10) { bs[kb] = (isM(kb) ? tbM : tbP) + slotof(kb) * SLOT; PIN(bs[kb]); }
      }
      if constexpr (F < 180) fetch(F);
      if constexpr (SET == 1 && isM(k)) {
        const int a = (st & 1) ? 5 : 0;
        acc[a] = mfma32(pm[st & 1], ring[T % R], (k == 0 && st < 2) ? zero : acc[a]);
        if (st < 17) perm(T + 1);
      } else {
        acc[t] = mfma32(ring[T % R], xf[18 * half(k) + st], (half(k) == 0 && st == 0) ? zero : acc[t]);
      }
      if (SET == 1 && T == 35) perm(36);
      if constexpr (T < 5) { xread(3 + 3 * T); xread(4 + 3 * T); xread(5 + 3 * T); }
      if constexpr (T >= 26 && T < 32) { xread(18 + 3 * (T - 26)); xread(19 + 3 * (T - 26)); xread(20 + 3 * (T - 26)); }
      if constexpr (T >= 6 && T < 9) {
#pragma unroll
        for (int n = 3 * (T - 6); n < 3 * (T - 6) + 3; n++) GLDS(dx, xvo, 0, 576 + 64 * n, xlb, 1024 * n);
      }
      if constexpr (F >= 18 && F % 18 < 9) {
        constexpr int kb = F / 18, n = F % 18;
        constexpr int ks = kb == 1 ? (SET == 0 ? 2 : -1) : kb == 2 ? (SET == 0 ? 4 : 3) : (kb + 2 <= 9 ? ((kb & 1) == SET ? kb + 2 : -1) : (SET == 1 ? kb + 2 : -1));
        if constexpr (ks == 2) GLDS(dm, mvo, mso + mhf + n * mstep, 0, mlb, 1024 * n);
        else if constexpr (ks >= 3 && ks <= 9) GLDS(dw1, wvo, wso, half(ks) * 288 * 9216 + (tile(ks) - 1) * 128 + n * 32 * 9216, wlb, slotof(ks) * SLOT + 4096 * n);
        else if constexpr (ks >= 10 && ks <= 11) {
          if (ks == 10) { const int e = 64 * n + lane; w2o[n] = (e / 18) * 1152 + (e % 18) * 16; }
          if (n == 0) w2s = ftile(4 * (ks - 10) + c) * 36864 + q * 288;
          w2stage(w2s, ks - 10, n);
        }
        if constexpr (ks >= 2 && ks <= 11) mark[ks] = ++cnt;
      }
      if constexpr (T == 10) asm volatile("s_waitcnt vmcnt(9)" : "+v"(uu[0]), "+v"(uu[1]), "+v"(uu[2])::"memory");
      if constexpr (T == 10 || T == 11) {
#pragma unroll
        for (int i = 2 * (T - 10); i < 2 * (T - 10) + 2; i++) {
          cs[2 * i] = blo(uu[0][i]) + blo(uu[1][i]) + blo(uu[2][i]); cs[2 * i + 1] = bhi(uu[0][i]) + bhi(uu[1][i]) + bhi(uu[2][i]);
          PIN(cs[2 * i]); PIN(cs[2 * i + 1]);
        }
      }
      if constexpr (T == 12) {
#pragma unroll
        for (int i = 0; i < 4; i++) { cs[i] = swadd32(cs[i], cs[i + 4]); PIN(cs[i]); }
      }
      if constexpr (T == 13) {
        const float a = dppadd<0x128>(swadd16(cs[0], cs[2])), b = dppadd<0x128>(swadd16(cs[1], cs[3]));
        *(LAS float*)(smem + SP_CS + (lane & 7) * 256 + (lane >> 3) * 32 + wv * 4) = (lane & 8) ? b : a;
      }
      if constexpr (SET == 1 && T == 15) { cr[0] = *(LAS f32x4*)(smem + SP_CS + lane * 32); cr[1] = *(LAS f32x4*)(smem + SP_CS + lane * 32 + 16); }
      if constexpr (SET == 1 && T == 17) {
        const float s = ((cr[0][0] + cr[0][1]) + (cr[0][2] + cr[0][3])) + ((cr[1][0] + cr[1][1]) + (cr[1][2] + cr[1][3]));
        __builtin_amdgcn_raw_buffer_store_b64(u32x2{pk2(s, s - bfr(s)), tag}, rw, (c == 0 && cu < 54) ? lane * 8u : OOB, WS_CS + g3 * 36864 + cg * 512, 16);
        cnt++;
      }
      if constexpr (T == 31) {
        gather();
        asm volatile("global_load_ushort %0, %1, %2" : "=v"(bia) : "v"((288 * nt + gl) * 2), "s"(b1) : "memory");
        cnt += 5;
        if (SET == 1) {
#pragma unroll
          for (int e = 0; e < 2; e++) asm volatile("global_load_dword %0, %1, %2 offset:%3" : "=v"(fc[e]) : "v"((has_dft ? nt : 0) * 256 + r * 4), "s"(&ST.fc), "i"(128 * e) : "memory");
          cnt += 2;
        }
      }
      if constexpr (T == 44) {
        asm volatile("s_waitcnt vmcnt(9)" : "+v"(sg), "+v"(cq[0]), "+v"(cq[1]), "+v"(cq[2]), "+v"(bia)::"memory");
        if (SET == 1) { PIN(fc[0]); PIN(fc[1]); }
        for (int it = 0; __builtin_amdgcn_ballot_w64(sg[1] != tag || cq[0][1] != tag || cq[1][1] != tag || cq[2][1] != tag) != 0; it++) {
          if (it >= SPIN) { put_flag(rw, true, WS_ERR, 2); break; }
          __builtin_amdgcn_s_sleep(1);
          gather();
          asm volatile("s_waitcnt vmcnt(0)" : "+v"(sg), "+v"(cq[0]), "+v"(cq[1]), "+v"(cq[2])::"memory");
        }
      }
      if constexpr (T == 45) {
        *(LAS u32x4*)(smem + SP_T + gl * 16) = u32x4{cq[0][0], cq[1][0], cq[2][0], bia};
        const unsigned nm = hh ? 0u : ((sg[0] & 0xffffu) ^ 0x8000u) * 0x10001u;
        sf = u32x4{nm, nm, nm, hh ? 0u : sg[0] >> 16};
        sdf = bhi(sg[0]); rstd = 1.0f / sdf;
        const float c0 = -2.0f * 0.7978845608028654f * 1.4426950408889634f, c1 = c0 * 0.044715f;
        k0 = c0 * rstd; k1 = c1 * rstd * rstd * rstd;
        PIN(sf); PIN(k0); PIN(k1); PIN(sdf);
        if (SET == 1) *(LAS float*)(smem + SP_RS + c * 128 + r * 4) = rstd;
      }
      if constexpr (!(SET == 1 && isM(k))) {
        if (half(k) == 1 && st == 15) tf = *(LAS u32x4*)(smem + SP_T + ((t == 0 ? flh : fl0 + 64 * (t - 1) + 32 * SET) + r) * 16);
        if (half(k) == 1 && st == 17) acc[t] = mfma32(tf, sf, acc[t]);
      } else if constexpr (k == 2 && st == 17) {
        acc[0] = mfma32(sf, u32x4{fc[0], 0, 0, 0}, acc[0]);
        acc[5] = mfma32(sf, u32x4{fc[1], 0, 0, 0}, acc[5]);
      }
      constexpr int TW = SET == 0 ? 77 : 95;
      if constexpr ((T >= TW && T < TW + 4) || T == TW + 36 || T == TW + 37) {
        const int wset = 8 * (lid >> 2) + (lane & 7), wp = 8 * wv + (lane >> 3), wcol = 72 * q + (q == 3) + 2 * wp;
        const bool on = wp <= 36 && wcol <= 287;
        if constexpr (T < TW + 4) {
          constexpr int i = T - TW;
          const unsigned vo = on ? ((16 * (wset >> 7) + (wset & 15) + 256 * ((wset >> 4) & 7)) * 289 + wcol) * 2u : OOB;
          BLD("dword", wl[2 * i], mkdesc(wr, 2048 * 578), vo, i * 64 * 578, "");
          BLD("dword", wl[2 * i + 1], mkdesc(wi, 2048 * 578), vo, i * 64 * 578, "");
          cnt += 2; mw = cnt;
        } else {
          if (T == TW + 36) asm volatile("s_waitcnt vmcnt(%8)" : "+v"(wl[0]), "+v"(wl[1]), "+v"(wl[2]), "+v"(wl[3]), "+v"(wl[4]), "+v"(wl[5]), "+v"(wl[6]), "+v"(wl[7]) : "i"(cnt - mw) : "memory");
          const unsigned sel = T == TW + 36 ? 0x05040100u : 0x07060302u;
          u32x4 o;
#pragma unroll
          for (int i = 0; i < 4; i++) o[i] = __builtin_amdgcn_perm(wl[2 * i + 1], wl[2 * i], sel);
          st16(rw, on ? wcol * 8192u + (lane & 7) * 16 : OOB, WS_WT + (lid >> 2) * 128 + (T - TW - 36) * 8192, o);
          cnt++;
        }
      }
      if constexpr (SET == 0) {
        if constexpr (T >= 55 && T < 73) gstep(0, 0, T - 55, 2);
        if constexpr (T >= 73 && T < 109 && ((T - 73) & 1) == 0) gstep(0, 1, (T - 73) >> 1, 2);
      } else if constexpr (T >= 55 && T < 61) {
        constexpr int u = (T - 55) / 3, ph = (T - 55) % 3;
        const int bc = 32 * nt + r;
        const float b1c = bc == 144 ? 0.f : 1.f, b2c = bc < 144 ? -1.f : 1.f, a2c = -b1c * b2c;
        if (ph == 0) { cr[0] = *(LAS f32x4*)(smem + SP_RS + c * 128 + u * 64 + hh * 16); cr[1] = *(LAS f32x4*)(smem + SP_RS + c * 128 + u * 64 + 32 + hh * 16); }
        else {
          float o1[4], o2[4];
#pragma unroll
          for (int i = 0; i < 4; i++) {
            const float e = acc[0][8 * u + 4 * (ph - 1) + i] * cr[ph - 1][i], o = acc[5][8 * u + 4 * (ph - 1) + i] * cr[ph - 1][i];
            o1[i] = __builtin_fmaf(b1c, o, e); o2[i] = __builtin_fmaf(a2c, e, b2c * o);
          }
          LAS char* p = smem + r * 272 + (2 * u + hh) * 64 + c * 16 + (ph - 1) * 8;
          *(LAS u32x2*)p = u32x2{pk2(o1[0], o1[1]), pk2(o1[2], o1[3])};
          *(LAS u32x2*)(p + 32 * 272) = u32x2{pk2(o2[0], o2[1]), pk2(o2[2], o2[3])};
        }
      } else if constexpr (T == 63) {
#pragma unroll
        for (int i = 0; i < 4; i++) xq[i] = *(LAS u32x4*)(smem + (16 * c + 4 * i + g) * 272 + l15 * 16);
      } else if constexpr (T >= 64 && T < 68) {
        const int bc = 32 * nt + 16 * (c & 1) + 4 * (T - 64) + g;
        const int p1 = 2 * bc - (bc > 144 ? 287 : 0);
        int p2 = (bc < 144 ? 576 : 865) - 2 * bc; p2 = bc == 0 ? 1 : p2; p2 = bc == 144 ? 289 : p2;
        st16(rw, has_dft ? (c < 2 ? p1 : p2) * 4096u + l15 * 16 : OOB, WS_XS + bh * 2048 + aq * 256, xq[T - 64]);
        cnt++;
      }
      if constexpr (T >= 73) {
        const int pt = (T - 73) / 36 + 1, s = (T - 73) % 36;
        if (s < 18) gstep(pt, 0, s, 0);
        if (s >= 16 && s < 34) gstep(pt, 1, s - 16, 1);
      }
      SB();
    });
    if (SET == 0) {
#pragma unroll
      for (int ks = 0; ks < 5; ks++) hload(ks);
    }
#pragma unroll
    for (int s = 0; s < 18; s++) {
      if (s == 12) { BLD("dwordx4", pf, dws, SET == 0 ? 0 : lane * 16, WS_F1 + (SET == 0 ? (lid >> 2) * 16 : 0), " sc1"); mpf = ++cnt; }
      gstep(4, 0, s, 0); gstep(4, 1, s, 1);
    }
    mst = cnt;
  }
  if constexpr (SET == 0) {
    constexpr int D = DIST2, R = RING2;
    const unsigned tb = (16 * (g >> 1) + 4 * (g & 1) + q4) * 288 + p4 * 8;
    const unsigned zb = 2 * SLOT + (8 * (g >> 1) + 2 * (g & 1) + (q4 >> 1)) * 512 + (2 * (p4 >> 1) + (q4 & 1)) * 64 + ((c ^ (2 * (g & 1) + (q4 >> 1))) * 16) + (p4 & 1) * 8;
    u32x2 ra[R], rb[R], xv[5], bv[5];
    f32x4 acc[9][2];
    LAS char* bs[11];
    int mx;
    const f32x4 z4 = {0, 0, 0, 0};
    auto fad = [&](int F) AI { return bs[F / 36] + ((F % 36) / 9) * 9216 + (F % 9) * 32; };
    bs[0] = smem + tb; PIN(bs[0]);
#pragma unroll
    for (int F = 0; F < D; F++) { ra[F] = trd(fad(F)); rb[F] = trd(fad(F) + 2304); }
    asm volatile("s_waitcnt vmcnt(%1)" : "+v"(pf) : "i"(cnt - mpf) : "memory");
    poll4(rw, dws, pf, 0, WS_F1 + (lid >> 2) * 16, tag);
#pragma unroll
    for (int ks = 5; ks < 12; ks++) hload(ks);
    const unsigned orow = (4 * aq + c + 32 * (32 * bh + 16 * (bb >> 1) + l15)) * 1152u + g * 8, ocol = 288 * q + 160 * (bb & 1);
    SB();
    sfor<369>([&](auto GC_) AI {
      constexpr int G = decltype(GC_)::v, ks = G / 9, dt = G % 9, F = G + D;
      if constexpr (F % 36 == 0) {
        constexpr int j = F / 36;
        if (j == 1) WAITV(cnt - mst);
        if (j < 10) { BARX(); }
        if (j == 1) { put_flag(rw, tid == 0, WS_FH + lid * 4, tag); cnt++; }
        bs[j] = smem + (j < 10 ? (j & 3) * SLOT : SP) + tb; PIN(bs[j]);
      }
      if constexpr (dt == 0 && ks < 36) asm volatile("s_waitcnt vmcnt(%2)" : "+v"(hb[ks % 12][0]), "+v"(hb[ks % 12][1]) : "i"(cnt - mh[ks]) : "memory");
      const u32x4 a = {ra[G % R][0], ra[G % R][1], rb[G % R][0], rb[G % R][1]};
      acc[dt][0] = mfma16(a, hb[ks % 12][0], ks == 0 ? z4 : acc[dt][0]);
      SB();
      if constexpr (F < 369) ra[F % R] = trd(fad(F));
      SB();
      acc[dt][1] = mfma16(a, hb[ks % 12][1], ks == 0 ? z4 : acc[dt][1]);
      SB();
      if constexpr (F < 369) rb[F % R] = trd(fad(F) + 2304);
      if constexpr (G == 62) { BLD("dwordx4", pf, dws, 0, WS_FH + (lid >> 2) * 16, " sc1"); mpf = ++cnt; }
      if constexpr (dt == 8 && ks + 12 < 36) {
        if (ks + 12 == 20) {
          asm volatile("s_waitcnt vmcnt(%1)" : "+v"(pf) : "i"(cnt - mpf) : "memory");
          poll4(rw, dws, pf, 0, WS_FH + (lid >> 2) * 16, tag);
        }
        hload(ks + 12);
      }
      if constexpr (ks >= 35 && ks < 40 && (dt == 5 || dt == 6)) {
        constexpr int sp = ks - 35;
        hb[(ks + 1) % 12][dt - 5] = trd2(smem + zb + (dt - 5) * 256 + sp * 8192 - (sp == 4 ? (g >> 1) * 4096 : 0), 2048);
      }
      if constexpr (G >= 330 && G < 335) {
        constexpr int i = G - 330;
        const bool on = i < 4 || (bb & 1) == 0;
        BLD("dwordx2", xv[i], dx, on ? orow : OOB, ocol + 32 * i, "");
        BLD("dwordx2", bv[i], mkdesc(b2, 1152), on ? g * 8 : OOB, ocol + 32 * i, "");
        cnt += 2; mx = cnt;
      }
      SB();
    });
    const unsigned pbase = WS_P + ((rt * 4 + q) * 64 + c) * 5120;
#pragma unroll
    for (int d = 0; d < 4; d++)
#pragma unroll
      for (int i = 0; i < 5 - (d & 1); i++)
        st16(rw, d == bb ? OOB : lane * 16 + i * 1024, pbase + (d * 16 + bb * 4) * 5120, __builtin_bit_cast(u32x4, acc[5 * (d & 1) + i][d >> 1]));
    asm volatile("s_waitcnt vmcnt(18)" : "+v"(xv[0]), "+v"(xv[1]), "+v"(xv[2]), "+v"(xv[3]), "+v"(xv[4]), "+v"(bv[0]), "+v"(bv[1]), "+v"(bv[2]), "+v"(bv[3]), "+v"(bv[4])::"memory");
    f32x4 own[5];
#pragma unroll
    for (int i = 0; i < 5; i++) {
      const int i2 = i < 4 ? 5 + i : 8;
#pragma unroll
      for (int e = 0; e < 4; e++) {
        const float lo = (bb & 2) ? acc[i][1][e] : acc[i][0][e], hi = (bb & 2) ? acc[i2][1][e] : acc[i2][0][e];
        const unsigned xw = xv[i][e >> 1], bw = bv[i][e >> 1];
        own[i][e] = ((bb & 1) ? hi : lo) + ((e & 1) ? bhi(xw) + bhi(bw) : blo(xw) + blo(bw));
      }
      PIN(own[i]);
    }
    WAITV(0); BARX();
    put_flag(rw, tid == 0, WS_FP + ((rt * 4 + q) * 4 + bb) * 4, tag);
    pf = u32x4{0, 0, 0, ~tag};
    poll4(rw, dws, pf, 0, WS_FP + (rt * 4 + q) * 16, tag);
    u32x4 pp[3][5];
#pragma unroll
    for (int s = 0; s < 3; s++)
#pragma unroll
      for (int i = 0; i < 5; i++) pp[s][i] = ld16(rw, (i < 4 || (bb & 1) == 0) ? lane * 16 + i * 1024 : OOB, pbase + (bb * 16 + ((bb + 1 + s) & 3) * 4) * 5120);
    const rsrc_t ro = mkrsrc(out, 2048 * 1152);
#pragma unroll
    for (int i = 0; i < 5; i++) {
      const f32x4 v = (own[i] + __builtin_bit_cast(f32x4, pp[0][i])) + (__builtin_bit_cast(f32x4, pp[1][i]) + __builtin_bit_cast(f32x4, pp[2][i]));
      __builtin_amdgcn_raw_buffer_store_b64(u32x2{pk2(v[0], v[1]), pk2(v[2], v[3])}, ro, (i < 4 || (bb & 1) == 0) ? orow : OOB, ocol + 32 * i, 0);
    }
  } else {
    auto stage = [&](int j) AI {
      const unsigned so = ftile(4 * j + c) * 36864 + q * 288;
#pragma unroll
      for (int n = 0; n < 9; n++) w2stage(so, j, n);
      cnt += 9; mark[j] = cnt;
    };
    auto bar = [&](int j, int m) AI { WAITV(cnt - m); WAITL(0); BARX(); };
    const u32x4 dst = mkdesc(&ST, sizeof(ST)), dg = mkdesc(&GT, sizeof(GT));
    const int e2 = rt * 7 + nt - 9;
    const bool has2 = nt >= 9 && e2 < 33;
    const int kc[2] = {lid, 256 + e2};
    u32x4 fa[2][3], fb[2][3], f32[2][3], t1[4], t2[4], xx[2][8], wt[2][2];
    const f32x4 z4 = {0, 0, 0, 0};
    mark[1] = mark[11];
    stage(2);
#pragma unroll
    for (int i = 0; i < 6; i++) {
      BLD("dwordx4", fa[i / 3][i % 3], dst, lane * 16, c * 6144 + i * 1024, "");
      BLD("dwordx4", f32[i / 3][i % 3], dst, lane * 16, 49152 + i * 1024, "");
    }
#pragma unroll
    for (int i = 0; i < 4; i++) {
      BLD("dwordx4", t1[i], dst, lane * 16, 55296 + c * 4096 + i * 1024, "");
      BLD("dwordx4", t2[i], dst, lane * 16, 71680 + c * 4096 + i * 1024, "");
    }
    cnt += 20;
    asm volatile("s_waitcnt vmcnt(%1)" : "+v"(pf) : "i"(cnt - mpf) : "memory");
    poll4(rw, dws, pf, lane * 16, WS_F1, tag);
#pragma unroll
    for (int z = 0; z < 2; z++) {
      const bool on = z == 0 || has2, cplx = kc[z] != 0 && kc[z] != 288;
      const unsigned vo = q4 * 256 + g * 64 + p4 * 16, so = WS_XS + (kc[z] == 288 ? 1 : 2 * kc[z]) * 4096;
#pragma unroll
      for (int i = 0; i < 4; i++) {
        BLD("dwordx4", xx[z][i], dws, on ? vo : OOB, so + i * 1024, " sc1");
        BLD("dwordx4", xx[z][4 + i], dws, on && cplx ? vo : OOB, so + 4096 + i * 1024, " sc1");
      }
#pragma unroll
      for (int ct = 0; ct < 2; ct++) BLD("dwordx4", wt[z][ct], dws, on ? lane * 16 : OOB, WS_WT + kc[z] * 8192 + (c * 2 + ct) * 1024, " sc1");
    }
    cnt += 20;
    const int mx = cnt;
    bar(1, mst);
    stage(3);
#pragma unroll
    for (int i = 0; i < 6; i++) BLD("dwordx4", fb[i / 3][i % 3], dst, lane * 16, 24576 + c * 6144 + i * 1024, "");
    cnt += 6;
    const int mb = cnt;
    WAITV(cnt - mx);
#pragma unroll
    for (int i = 0; i < 6; i++) { PIN(fa[i / 3][i % 3]); PIN(f32[i / 3][i % 3]); }
#pragma unroll
    for (int i = 0; i < 4; i++) { PIN(t1[i]); PIN(t2[i]); PIN(wt[i >> 1][i & 1]); }
#pragma unroll
    for (int i = 0; i < 16; i++) PIN(xx[i >> 3][i & 7]);
    auto front = [&](int z) AI {
      u32x4 br, bi, yr, yi;
#pragma unroll
      for (int at = 0; at < 2; at++) {
        f32x4 tr = z4, ti = z4;
#pragma unroll
        for (int t = 0; t < 2; t++) {
          const u32x4 xr = xx[z][2 * t + at], xi = xx[z][4 + 2 * t + at];
          tr = mfma16(xr, fa[t][0], tr); ti = mfma16(xi, fa[t][0], ti);
          tr = mfma16(xi, fa[t][1], tr); ti = mfma16(xr, fa[t][2], ti);
        }
        const f32x4 co = __builtin_bit_cast(f32x4, t1[2 * at]), si = __builtin_bit_cast(f32x4, t1[2 * at + 1]);
        const f32x4 ur = tr * co + ti * si, ui = ti * co - tr * si;
        br[2 * at] = pk2(ur[0], ur[1]); br[2 * at + 1] = pk2(ur[2], ur[3]);
        bi[2 * at] = pk2(ui[0], ui[1]); bi[2 * at + 1] = pk2(ui[2], ui[3]);
      }
#pragma unroll
      for (int ct = 0; ct < 2; ct++) {
        f32x4 xr = mfma16(f32[ct][0], br, z4), xi = mfma16(f32[ct][0], bi, z4);
        xr = mfma16(f32[ct][1], bi, xr); xi = mfma16(f32[ct][2], br, xi);
        float vr[4], vi[4];
#pragma unroll
        for (int i = 0; i < 4; i++) { const unsigned w = wt[z][ct][i]; const float a = blo(w), b = bhi(w); vr[i] = xr[i] * a - xi[i] * b; vi[i] = xr[i] * b + xi[i] * a; }
        yr[2 * ct] = pk2(vr[0], vr[1]); yr[2 * ct + 1] = pk2(vr[2], vr[3]);
        yi[2 * ct] = pk2(vi[0], vi[1]); yi[2 * ct + 1] = pk2(vi[2], vi[3]);
      }
#pragma unroll
      for (int at = 0; at < 2; at++) {
        f32x4 pr = mfma16(yr, f32[at][0], z4), pi = mfma16(yi, f32[at][0], z4);
        pr = mfma16(yi, f32[at][2], pr); pi = mfma16(yr, f32[at][1], pi);
        const f32x4 co = __builtin_bit_cast(f32x4, t2[2 * at]), si = __builtin_bit_cast(f32x4, t2[2 * at + 1]);
        const f32x4 ur = pr * co - pi * si, ui = pi * co + pr * si;
        LAS char* p = smem + SP + z * 8192 + (at * 4 + (c >> 1)) * 1024 + lane * 16 + (c & 1) * 8;
        *(LAS u32x2*)p = u32x2{pk2(ur[0], ur[1]), pk2(ur[2], ur[3])};
        *(LAS u32x2*)(p + 2048) = u32x2{pk2(ui[0], ui[1]), pk2(ui[2], ui[3])};
      }
    };
    front(0);
    if (has2) front(1);
    bar(2, mark[2]);
    stage(4);
    WAITV(cnt - mb);
#pragma unroll
    for (int i = 0; i < 6; i++) PIN(fb[i / 3][i % 3]);
    auto back = [&](int z) AI {
      const int pair = kc[z] == 288 ? 0 : kc[z];
      const bool on = kc[z] == 0 ? (g & 1) == 0 : (kc[z] == 288 ? (g & 1) == 1 : true);
#pragma unroll
      for (int at = 0; at < 2; at++) {
        f32x4 zr = z4, zi = z4;
#pragma unroll
        for (int t = 0; t < 2; t++) {
          LAS char* p = smem + SP + z * 8192 + (at * 4 + t) * 1024 + lane * 16;
          const u32x4 pr = *(LAS u32x4*)p, pi = *(LAS u32x4*)(p + 2048);
          zr = mfma16(fb[t][0], pr, zr); zi = mfma16(fb[t][0], pi, zi);
          zr = mfma16(fb[t][2], pi, zr); zi = mfma16(fb[t][1], pr, zi);
        }
        const unsigned r0 = pk2(zr[0], zr[1]), r1 = pk2(zr[2], zr[3]);
        const unsigned i0 = kc[z] == 288 ? r0 : pk2(zi[0], zi[1]), i1 = kc[z] == 288 ? r1 : pk2(zi[2], zi[3]);
        const auto s0 = __builtin_amdgcn_permlane16_swap(r0, i0, false, false), s1 = __builtin_amdgcn_permlane16_swap(r1, i1, false, false);
        const unsigned a0 = s0[0], a1 = s1[0], a2 = s0[1], a3 = s1[1];
        st16(rw, on ? q4 * 1024u + g * 64 + ((p4 ^ (pair & 3)) * 16) : OOB, WS_Z + pair * 8192 + at * 4096 + c * 256, u32x4{a0, a1, a2, a3});
      }
    };
    back(0);
    if (has2) back(1);
    cnt += 2;
    const int mz = cnt;
    bar(3, mark[3]);
    stage(5);
    bar(4, mz);
    put_flag(rw, tid == 256, WS_FZ + lid * 4, tag); cnt++;
    stage(6);
#pragma unroll
    for (int n = 0; n < 3; n++) { const int pc = n < 2 ? c + 4 * n : 8; GLDS(dg, lane * 16, (nt * 5 + 4) * 9216 + pc * 1024, 0, SP + pc * 1024, 0); }
    cnt += 3;
    bar(5, mark[5]);
    stage(7);
    bar(6, mark[6]);
    stage(8);
    bar(7, mark[7]);
    BLD("dwordx4", pf, dws, lane * 16, WS_FZ, " sc1"); mpf = ++cnt;
#pragma unroll
    for (int n = 0; n < 9; n++) GLDS(dg, lane * 16, (nt * 5 + c) * 9216, 1024 * n, mlb, SLOT + 1024 * n);
    cnt += 9;
    asm volatile("s_waitcnt vmcnt(%1)" : "+v"(pf) : "i"(cnt - mpf) : "memory");
    poll4(rw, dws, pf, lane * 16, WS_FZ, tag);
    u32x4 zz[9];
#pragma unroll
    for (int n = 0; n < 9; n++) BLD("dwordx4", zz[n], dws, hh * 8192 + r * 16, WS_Z + (72 * bb + 2 * c + 8 * n) * 8192 + rt * 512, " sc1");
    cnt += 9;
    bar(8, mark[8]);
    put_flag(rw, lid == 0 && tid == 256, WS_E, tag);
    WAITV(1);
#pragma unroll
    for (int n = 0; n < 9; n++) { PIN(zz[n]); *(LAS u32x4*)(smem + 2 * SLOT + (c + 4 * n) * 1024 + lane * 16) = zz[n]; }
    bar(9, cnt);
    BARX();
  }
  };
  if (wv < 4) body(IC<0>{}); else body(IC<1>{});
}

extern "C" void kernel_launch(void* const* d_in, const int* in_sizes, int n_in, void* d_out, int out_size, void* d_ws, size_t ws_size, hipStream_t stream) {
  (void)hipFuncSetAttribute((const void*)kmain, hipFuncAttributeMaxDynamicSharedMemorySize, LDSZ);
  kmain<<<256, 512, LDSZ, stream>>>((const u16*)d_in[0], (const u16*)d_in[1], (const u16*)d_in[2], (const u16*)d_in[4], (const u16*)d_in[5],
                                    (const u16*)d_in[6], (const u16*)d_in[7], (u16*)d_out, (char*)d_ws);
}
```

```cpp
#include <hip/hip_runtime.h>
#include <hip/hip_bf16.h>
#include <utility>

typedef unsigned short u16;
typedef __attribute__((ext_vector_type(8))) __bf16 bf16x8;
typedef __attribute__((ext_vector_type(2))) __bf16 bf16x2;
typedef __attribute__((ext_vector_type(4))) short s16x4;
typedef __attribute__((ext_vector_type(8))) short s16x8;
typedef __attribute__((ext_vector_type(4))) float f32x4;
typedef __attribute__((ext_vector_type(16))) float f32x16;
typedef __attribute__((ext_vector_type(4))) unsigned u32x4;
typedef __attribute__((ext_vector_type(2))) unsigned u32x2;
#define LAS __attribute__((address_space(3)))
#define GAS __attribute__((address_space(1)))
#define DI __device__ __forceinline__

namespace tab {
constexpr double PI_ = 3.14159265358979323846;
constexpr double tsin(double x) { double t = x, s = x; for (int i = 1; i < 12; i++) { t *= -x * x / ((2 * i) * (2 * i + 1)); s += t; } return s; }
constexpr double tcos(double x) { double t = 1, s = 1; for (int i = 1; i < 12; i++) { t *= -x * x / ((2 * i - 1) * (2 * i)); s += t; } return s; }
constexpr double ccos(int n, int N) {
  n %= N; if (n < 0) n += N;
  if (2 * n > N) n = N - n;
  bool neg = false;
  if (4 * n > N) { n = N / 2 - n; neg = true; }
  double v = (8 * n > N) ? tsin(2 * PI_ * (N / 4 - n) / N) : tcos(2 * PI_ * n / N);
  return neg ? -v : v;
}
constexpr double csin(int n, int N) { return ccos(n - N / 4, N); }
constexpr u16 f2bf(double d) { float f = (float)d; unsigned u = __builtin_bit_cast(unsigned, f); u += 0x7fffu + ((u >> 16) & 1u); return (u16)(u >> 16); }
struct C576 { u16 c[576]; u16 g1[576]; u16 g2[576]; };
constexpr C576 mk576() { C576 r{}; for (int i = 0; i < 576; i++) { double c = ccos(i, 576); r.c[i] = f2bf(c); r.g1[i] = f2bf(c / 576.0); r.g2[i] = f2bf(2.0 * c / 576.0); } return r; }
constexpr C576 c576 = mk576();
struct Blk { u16 v[9216]; };
constexpr int fidx(int bc, int d) {
  int n = bc < 144 ? bc * d : (bc == 144 ? ((d & 1) == 0 ? 144 * d : 144 * d + 144) : (bc - 144) * d + 144);
  return n % 576;
}
constexpr Blk mkF(int idx) {
  Blk r{}; int at = idx >> 1, hf = idx & 1;
  for (int st = 0; st < 18; st++) for (int hh = 0; hh < 2; hh++) for (int j = 0; j < 8; j++) {
    int d = 288 * hf + 32 * (st >> 1) + 16 * (j >> 2) + 8 * hh + 2 * (j & 3) + (st & 1);
    for (int cc = 0; cc < 32; cc++) r.v[((st * 2 + hh) * 8 + j) * 32 + cc] = c576.c[fidx(32 * at + cc, d)];
  }
  return r;
}
template <int I> constexpr Blk fblk = mkF(I);
struct FTab { Blk b[18]; };
template <int... I> constexpr FTab mkFT(std::integer_sequence<int, I...>) { return FTab{{fblk<I>...}}; }
constexpr double bf2d(u16 b) { return (double)__builtin_bit_cast(float, (unsigned)b << 16); }
struct FCB { unsigned v[64]; };
constexpr FCB mkFC(int at) {
  FCB r{};
  for (int e = 0; e < 2; e++) for (int cc = 0; cc < 32; cc++) {
    double s = 0;
    for (int d = e; d < 576; d += 2) s += bf2d(c576.c[fidx(32 * at + cc, d)]);
    u16 hi = f2bf(s), lo = f2bf(s - bf2d(hi));
    r.v[cc * 2 + e] = (unsigned)hi | ((unsigned)lo << 16);
  }
  return r;
}
template <int I> constexpr FCB fcb = mkFC(I);
struct FCT { FCB b[9]; };
template <int... I> constexpr FCT mkFCT(std::integer_sequence<int, I...>) { return FCT{{fcb<I>...}}; }
struct GBlk { u16 v[4608]; };
constexpr GBlk mkG(int idx) {
  GBlk r{}; int step = idx % 5, q = (idx / 5) & 3, b = idx / 20;
  for (int row = 0; row < 32; row++) {
    int rho = 32 * step + row;
    if (rho >= 144) continue;
    int kc = 72 * b + (rho >> 1), ri = rho & 1;
    for (int c = 0; c < 144; c++) {
      int d = 144 * q + c;
      r.v[row * 144 + c] = kc == 0 ? c576.g1[(ri * 288 * d) % 576] : c576.g2[(kc * d + ri * 144) % 576];
    }
  }
  return r;
}
template <int I> constexpr GBlk gblk = mkG(I);
struct GTab { GBlk b[80]; };
template <int... I> constexpr GTab mkGT(std::integer_sequence<int, I...>) { return GTab{{gblk<I>...}}; }
struct C2048 { float c[2048]; };
constexpr C2048 mk2048() { C2048 r{}; for (int i = 0; i < 2048; i++) r.c[i] = (float)ccos(i, 2048); return r; }
constexpr C2048 c2048 = mk2048();
constexpr u16 trig(int n2048, int type) {
  int n = type == 0 ? n2048 : (type == 1 ? n2048 + 1536 : n2048 + 512);
  return f2bf(c2048.c[n & 2047]);
}
struct F64T { u16 v[4][2][3][64][8]; };
constexpr F64T mkF64(int which) {
  F64T r{};
  for (int j = 0; j < 4; j++) for (int t = 0; t < 2; t++) for (int ty = 0; ty < 3; ty++) for (int l = 0; l < 64; l++) for (int jj = 0; jj < 8; jj++) {
    int g = l >> 4, o = 16 * j + (l & 15);
    int k = which == 0 ? 32 * t + 16 * (g >> 1) + 8 * (jj >> 2) + 4 * (g & 1) + (jj & 3) : 32 * t + 16 * (jj >> 2) + 4 * g + (jj & 3);
    r.v[j][t][ty][l][jj] = trig(o * k * 32, ty);
  }
  return r;
}
struct F32T { u16 v[2][3][64][8]; };
constexpr F32T mkF32() {
  F32T r{};
  for (int ct = 0; ct < 2; ct++) for (int ty = 0; ty < 3; ty++) for (int l = 0; l < 64; l++) for (int jj = 0; jj < 8; jj++) {
    int g = l >> 4, o = 16 * ct + (l & 15), k = 16 * (jj >> 2) + 4 * g + (jj & 3);
    r.v[ct][ty][l][jj] = trig(o * k * 64, ty);
  }
  return r;
}
struct TWT { float v[4][4][64][4]; };
constexpr TWT mkTW(int which) {
  TWT r{};
  for (int j = 0; j < 4; j++) for (int n = 0; n < 4; n++) for (int l = 0; l < 64; l++) for (int i = 0; i < 4; i++) {
    int g = l >> 4, at = n >> 1;
    int a = which == 0 ? 16 * at + 4 * g + i : 16 * at + (l & 15);
    int d = which == 0 ? 16 * j + (l & 15) : 16 * j + 4 * g + i;
    float v = c2048.c[(a * d + ((n & 1) ? 1536 : 0)) & 2047];
    r.v[j][n][l][i] = which == 0 ? v : v * (1.0f / 2048.0f);
  }
  return r;
}
constexpr F64T f64a = mkF64(0), f64b = mkF64(1);
constexpr F32T f32t = mkF32();
constexpr TWT tw1 = mkTW(0), tw2 = mkTW(1);
struct Small { F64T a, b; F32T f; TWT t1, t2; FCT fc; };
}
#if defined(__HIP_DEVICE_COMPILE__)
__device__ __attribute__((aligned(128))) static const tab::FTab FT = tab::mkFT(std::make_integer_sequence<int, 18>{});
__device__ __attribute__((aligned(128))) static const tab::GTab GT = tab::mkGT(std::make_integer_sequence<int, 80>{});
__device__ __attribute__((aligned(128))) static const tab::Small ST = {tab::f64a, tab::f64b, tab::f32t, tab::tw1, tab::tw2, tab::mkFCT(std::make_integer_sequence<int, 9>{})};
#else
__device__ static const tab::FTab FT = {};
__device__ static const tab::GTab GT = {};
__device__ static const tab::Small ST = {};
#endif
typedef unsigned long long u64;
DI float bfr(float x) { return (float)(__bf16)x; }
DI unsigned pk2(float a, float b) { bf16x2 c; c[0] = (__bf16)a; c[1] = (__bf16)b; return __builtin_bit_cast(unsigned, c); }
DI float blo(unsigned u) { return __builtin_bit_cast(float, u << 16); }
DI float bhi(unsigned u) { return __builtin_bit_cast(float, u & 0xffff0000u); }
DI f32x16 mfma32(u32x4 a, u32x4 b, f32x16 c) { return __builtin_amdgcn_mfma_f32_32x32x16_bf16(__builtin_bit_cast(bf16x8, a), __builtin_bit_cast(bf16x8, b), c, 0, 0, 0); }
DI f32x4 mfma16(u32x4 a, u32x4 b, f32x4 c) { return __builtin_amdgcn_mfma_f32_16x16x32_bf16(__builtin_bit_cast(bf16x8, a), __builtin_bit_cast(bf16x8, b), c, 0, 0, 0); }
DI u32x2 trd(LAS char* p) { return __builtin_bit_cast(u32x2, __builtin_amdgcn_ds_read_tr16_b64_v4i16((LAS s16x4*)p)); }
DI u32x4 trd2(LAS char* p, int off2) { u32x2 a = trd(p), b = trd(p + off2); u32x4 r; r[0] = a[0]; r[1] = a[1]; r[2] = b[0]; r[3] = b[1]; return r; }
template <int C> DI float dppadd(float v) { return v + __builtin_bit_cast(float, __builtin_amdgcn_mov_dpp(__builtin_bit_cast(int, v), C, 0xf, 0xf, true)); }
DI u32x4 mkdesc(const void* p, unsigned bytes) { u64 a = (u64)p; return u32x4{(unsigned)a, (unsigned)(a >> 32) & 0xffffu, bytes, 0x00020000u}; }
typedef __amdgpu_buffer_rsrc_t rsrc_t;
DI rsrc_t mkrsrc(const void* p, unsigned bytes) { return __builtin_amdgcn_make_buffer_rsrc((void*)p, 0, bytes, 0x00020000); }
DI void st16(rsrc_t r, unsigned voff, unsigned soff, u32x4 v) {
  __builtin_amdgcn_raw_buffer_store_b128(v, r, voff, soff, 16);
  asm volatile("s_nop 1" ::"v"(v) : "memory");
}
DI u32x4 ld16(rsrc_t r, unsigned voff, unsigned soff) { return __builtin_amdgcn_raw_buffer_load_b128(r, voff, soff, 16); }
#define GLDS(d, voff, sbase, simm, lbase, limm) do { unsigned t_; \
  asm volatile("s_add_i32 m0, %1, %2\n\ts_add_i32 %0, %3, %4\n\tbuffer_load_dwordx4 %5, %6, %0 offen lds" \
               : "=&s"(t_) : "s"(lbase), "i"(limm), "s"(sbase), "i"(simm), "v"(voff), "s"(d) : "memory", "scc"); } while (0)
#define GLDS1(d, voff, soff, lb) asm volatile("s_mov_b32 m0, %0\n\tbuffer_load_dword %1, %2, %3 offen lds" ::"i"(lb), "v"(voff), "s"(d), "s"(soff) : "memory")
#define WAITV(n) asm volatile("s_waitcnt vmcnt(%0)" ::"i"(n) : "memory")
#define WAITL(n) asm volatile("s_waitcnt lgkmcnt(%0)" ::"i"(n) : "memory")
#define PIN(x) asm volatile("" : "+v"(x))
#define BARX() asm volatile("s_barrier" ::: "memory")
#define SB() __builtin_amdgcn_sched_barrier(0)
#define AI __attribute__((always_inline))
#if defined(__HIP_DEVICE_COMPILE__)
#define NOPK __attribute__((target("no-packed-fp32-ops")))
#else
#define NOPK
#endif
template <int N> struct IC { static constexpr int v = N; };
template <class F, int... I> DI void sfor_(F&& f, std::integer_sequence<int, I...>) { (f(IC<I>{}), ...); }
template <int N, class F> DI void sfor(F&& f) { sfor_(f, std::make_integer_sequence<int, N>{}); }

#define BLD(op, dst, d, voff, soff, pol) asm volatile("buffer_load_" op " %0, %1, %2, %3 offen" pol : "=v"(dst) : "v"(voff), "s"(d), "s"(soff) : "memory")
DI float asf(unsigned u) { return __builtin_bit_cast(float, u); }
DI unsigned asu(float f) { return __builtin_bit_cast(unsigned, f); }
DI float swadd32(float a, float b) { auto s = __builtin_amdgcn_permlane32_swap(asu(a), asu(b), false, false); const unsigned x = s[0], y = s[1]; return asf(x) + asf(y); }
DI float swadd16(float a, float b) { auto s = __builtin_amdgcn_permlane16_swap(asu(a), asu(b), false, false); const unsigned x = s[0], y = s[1]; return asf(x) + asf(y); }
DI float half_sum(float v) {
  v = dppadd<0xB1>(v); v = dppadd<0x4E>(v); v = dppadd<0x141>(v); v = dppadd<0x140>(v);
  return swadd16(v, v);
}

constexpr int SLOT = 36864, SP = 4 * SLOT, LDSZ = 163840;
constexpr int SP_T = SP + 8192, SP_CS = SP_T + 4608, SP_RS = SP_T + 6656, SP_PF = SP_T + 7168;
constexpr unsigned WS_E = 0, WS_F1 = 1024, WS_FH = 2048, WS_FZ = 3072, WS_F0 = 4096, WS_ERR = 5120, WS_ST = 8192;
constexpr unsigned WS_CS = 32768;
constexpr unsigned WS_H = WS_CS + 3 * 36864;
constexpr unsigned WS_XS = WS_H + 64u * 144 * 2048;
constexpr unsigned WS_Z = WS_XS + 576u * 4096;
constexpr unsigned WS_WT = WS_Z + 576u * 4096;
constexpr unsigned WS_P = WS_WT + 289u * 8192;
constexpr unsigned WS_END = WS_P + 4096u * 5120;
constexpr int SPIN = 200000;
constexpr unsigned OOB = 0x80000000u;
DI void put_flag(rsrc_t rw, bool on, unsigned off, unsigned tag) { __builtin_amdgcn_raw_buffer_store_b32(tag, rw, on ? 0u : OOB, off, 16); }
DI bool bad4(u32x4 v, unsigned tag) { return __builtin_amdgcn_ballot_w64(v[0] != tag || v[1] != tag || v[2] != tag || v[3] != tag) != 0; }
DI void poll4(rsrc_t rw, u32x4 dws, u32x4 v, unsigned voff, unsigned soff, unsigned tag) {
  for (int it = 0; bad4(v, tag); it++) {
    if (it >= SPIN) { put_flag(rw, true, WS_ERR, 1); break; }
    __builtin_amdgcn_s_sleep(4);
    BLD("dwordx4", v, dws, voff, soff, " sc1");
    asm volatile("s_waitcnt vmcnt(0)" : "+v"(v)::"memory");
  }
}
#define DIST1 4
#define RING1 5
#define DIST2 4
#define RING2 5
#define ABL 0
#define PREF 1
#define PREFC 1
__global__ __launch_bounds__(512, 2) NOPK void kmain(const u16* __restrict__ x, const u16* __restrict__ wr, const u16* __restrict__ wi,
                                                     const u16* __restrict__ w1, const u16* __restrict__ b1, const u16* __restrict__ w2,
                                                     const u16* __restrict__ b2, u16* __restrict__ out, char* __restrict__ ws) {
  extern __shared__ __attribute__((aligned(16))) char smem_[];
  LAS char* smem = (LAS char*)smem_;
  const int tid = threadIdx.x, lane = tid & 63;
  const int wv = __builtin_amdgcn_readfirstlane(tid >> 6);
  auto body = [&](auto SETC) AI {
  constexpr int SET = decltype(SETC)::v;
  const int id = blockIdx.x, xcd = id & 7, slot = id >> 3;
  const int rt = (xcd >> 2) * 8 + (slot >> 2), bb = xcd & 3, q = slot & 3, nt = bb * 4 + q, lid = rt * 16 + nt;
  const int aq = rt >> 1, bh = rt & 1, c = wv & 3;
  const int fl0 = (nt & 1) * 32, flh = (nt & 1) ? 0 : 256;
  const bool has_dft = nt < 9;
  const rsrc_t rw = mkrsrc(ws, WS_END);
  const u32x4 dws = mkdesc(ws, WS_END), dx = mkdesc(x, 2048 * 1152), dw1 = mkdesc(w1, 576 * 9216), dw2 = mkdesc(w2, 4608 * 1152);
  const int r = lane & 31, hh = lane >> 5;
  unsigned ev, xc;
  u32x4 xs[2];
  asm volatile("s_load_dword %0, %1, 0x0" : "=s"(ev) : "s"(ws) : "memory");
  if (SET == 1 && PREFC) {
    u64 pc; unsigned len;
    asm volatile("s_getpc_b64 %0\n.Lkpc:\n\ts_mov_b32 %1, .Lkend-.Lkpc" : "=s"(pc), "=s"(len));
    GLDS1(mkdesc((const void*)pc, len < 0x40000u ? len : 0u), lane * 128u + (c * 32 + slot) * 8192u, 0, SP_PF);
  }
  if (SET == 0) {
    const int tr = 8 * nt + 2 * c;
    const unsigned ro = (4 * aq + (tr >> 5) + 32 * (32 * bh + (tr & 31))) * 1152u;
    BLD("dwordx4", xs[0], dx, hh * 36864 + r * 16, ro, "");
    BLD("dwordx4", xs[1], dx, hh * 36864 + r * 16, ro, " offset:512");
    BLD("dword", xc, dx, hh * 36864 + r * 4, ro, " offset:1024");
  }
  const unsigned xvo = (4 * aq + (wv >> 1) + 32 * (32 * bh + 16 * (wv & 1) + (lane >> 3))) * 1152u + (((lane & 7) ^ (lane >> 4)) * 16);
  const unsigned xvo1 = (xvo ^ 64) + 8 * 32 * 1152;
  const unsigned xlb = 2 * SLOT + wv * 10240;
#pragma unroll
  for (int n = 0; n < 5; n++) { GLDS(dx, xvo, 0, 128 * n, xlb, 1024 * n); GLDS(dx, xvo1, 0, 128 * n, xlb, 5120 + 1024 * n); }
  const unsigned wvo = (8 * c + ((lane >> 2) & 7)) * 9216u + hh * 64 + (lane & 3) * 16;
  const unsigned wso = (288 * nt + fl0) * 2, wlb = c * 1024;
  const u32x4 dft = mkdesc(&FT, has_dft ? sizeof(FT) : 0);
  const u32x4 dm = c < 2 ? dw1 : dft;
  const unsigned mvo = c < 2 ? (144 * c + (lane >> 2)) * 9216u + (lane & 3) * 16 : lane * 16;
  const unsigned mso = c < 2 ? (288 * nt + flh) * 2 : nt * 36864 + (c & 1) * 9216;
  const unsigned mstep = c < 2 ? 16 * 9216 : 1024, mhf = c < 2 ? 288 * 9216 : 18432, mlb = c * 9216;
#pragma unroll
  for (int n = SET; n < 9; n += 2) GLDS(dm, mvo, mso + n * mstep, 0, mlb, 1024 * n);
  const int cu = (xcd >> 2) * 32 + slot, cg = 18 * bb + cu / 3, g3 = cu % 3;
  u32x4 uu[3];
#pragma unroll
  for (int n = 0; n < 3; n++) BLD("dwordx4", uu[n], dw1, cu < 54 ? (lane >> 3) * 9216u + (lane & 7) * 16 : OOB, (192 * g3 + 24 * wv + 8 * n) * 9216 + cg * 128, "");
  asm volatile("s_waitcnt lgkmcnt(0)" : "+s"(ev)::"memory");
  const unsigned tag = ev * 0x9E3779B1u + 0x7F4A7C15u;
  if (SET == 0) {
    asm volatile("s_waitcnt vmcnt(18)" : "+v"(xs[0]), "+v"(xs[1]), "+v"(xc)::"memory");
    float s = blo(xc) + bhi(xc);
#pragma unroll
    for (int i = 0; i < 8; i++) { const unsigned w = xs[i >> 2][i & 3]; s += blo(w) + bhi(w); }
    const float mean = half_sum(s) * (1.0f / 576.0f);
    float v = 0.f;
#pragma unroll
    for (int i = 0; i < 9; i++) { const unsigned w = i < 8 ? xs[(i >> 2) & 1][i & 3] : xc; const float d0 = blo(w) - mean, d1 = bhi(w) - mean; v = __builtin_fmaf(d0, d0, v); v = __builtin_fmaf(d1, d1, v); }
    const float var = half_sum(v) * (1.0f / 576.0f);
    __builtin_amdgcn_raw_buffer_store_b64(u32x2{pk2(mean, __builtin_sqrtf(bfr(bfr(var) + 1e-5f))), tag}, rw, r == 0 ? hh * 8u : OOB, WS_ST + (rt * 128 + 8 * nt + 2 * c) * 8, 16);
  }
  const int q4 = (lane & 15) >> 2, p4 = lane & 3, g = lane >> 4, l15 = lane & 15;
  const unsigned hoff = (r >> 4) * 1024 + hh * 256 + (r & 15) * 16;
  const unsigned hrow = WS_H + (4 * rt + c) * 144 * 2048;
  const unsigned hso = hrow + 9 * nt * 2048;
  unsigned w2o[9];
  auto ftile = [&](int ks) AI {
    const int qq = (q + (ks < 20 ? ks / 5 : (ks - 20) >> 2)) & 3, e = ks < 20 ? ks % 5 : 5 + ((ks - 20) & 3);
    return 36 * bb + 9 * qq + ((qq & 1) ? e : (e == 0 ? 8 : e - 1));
  };
  auto w2stage = [&](unsigned so, int j, int n) AI { GLDS(dw2, w2o[n], so, 0, mlb, (j & 3) * SLOT + 1024 * n); };
  int cnt = 0, mark[12] = {};
  constexpr int HW = 8;
  u32x4 hb[HW][2];
  int mh[36];
  auto hload = [&](int ks) AI {
    const unsigned so = hrow + ftile(ks) * 2048;
    BLD("dwordx4", hb[ks % HW][0], dws, lane * 16, so, " sc1");
    BLD("dwordx4", hb[ks % HW][1], dws, lane * 16, so, " offset:1024 sc1");
    cnt += 2; mh[ks] = cnt;
  };
  u32x4 pf;
  int mpf, mst, mx;
  const unsigned pbase = WS_P + ((rt * 4 + q) * 64 + c) * 3072;
  const u32x4 dst = mkdesc(&ST, sizeof(ST));
  const int e2 = rt * 7 + nt - 9;
  const bool has2 = nt >= 9 && e2 < 33;
  const int kc[2] = {lid, 256 + e2};
  u32x4 fa[2][3], fb[2][3], f32[2][3], xx[2][8], wt[2][2];
  auto xload = [&](int z, int i) AI {
    const bool cplx = kc[z] != 0 && kc[z] != 288;
    const unsigned vo = q4 * 256 + g * 64 + p4 * 16;
    if (i < 8) BLD("dwordx4", xx[z][i], dws, (i < 4 || cplx) ? vo : OOB, WS_XS + (kc[z] == 288 ? 1 : 2 * kc[z]) * 4096 + i * 1024, " sc1");
    else BLD("dwordx4", wt[z][i - 8], dws, lane * 16, WS_WT + kc[z] * 8192 + (c * 2 + i - 8) * 1024, " sc1");
  };
  auto tload = [&](u32x4 (&f)[2][3], unsigned so, int i) AI { BLD("dwordx4", f[i >> 1][i & 1], dst, lane * 16, so + (i >> 1) * 3072 + (i & 1) * 1024, ""); };

  {
    constexpr int D = DIST1, R = RING1;
    const unsigned tq = q4 * 64 + (g & 1) * 32 + p4 * 8;
    LAS char* const tbP = smem + hh * 1024 + tq + SET * 512;
    LAS char* const tbM = smem + hh * 512 + tq + SET * 18432;
    const unsigned xa = 2 * SLOT + (4 * c + (r >> 3)) * 5120 + (r & 7) * 128 + ((hh ^ ((r >> 1) & 7)) * 16);
    u32x4 xf[36], ring[R], pm[2], sf, tf, xq[4];
    u32x2 sg, fc, wl[8];
    u32x4 cq[3];
    unsigned bia, w2s, wvv;
    f32x16 acc[6];
    f32x4 cr[2];
    float cs[8], gt[3][8], ge[3][8], k0, k1, sdf;
    u32x4 go[3];
    LAS char* bs[11];
    int mw;
    const f32x16 zero = {0, 0, 0, 0, 0, 0, 0, 0, 0, 0, 0, 0, 0, 0, 0, 0};
    constexpr auto isM = [](int k) constexpr { return k == 0 || k == 2; };
    constexpr auto tile = [](int k) constexpr { return k < 4 ? (k & 1) : (k >> 1); };
    constexpr auto half = [](int k) constexpr { return k < 4 ? (k >> 1) : (k & 1); };
    constexpr auto slotof = [](int k) constexpr { return k < 2 ? k : (k + 2) & 3; };
    auto fetch = [&](int F) AI { const int k = F / 18, st = F % 18; ring[F % R] = trd2(bs[k] + st * (isM(k) ? 1024 : 2048), 256); };
    auto xread = [&](int i) AI { if (i < 36) xf[i] = *(LAS u32x4*)(smem + (xa ^ ((i & 3) * 32)) + ((i >> 2) % 5) * 1024); };
    auto perm = [&](int T) AI {
      const int st = T % 18, b = 18 * half(T / 18) + (st & ~1); const unsigned sel = (st & 1) ? 0x07060302u : 0x05040100u;
      u32x4 o;
      o[0] = __builtin_amdgcn_perm(xf[b][1], xf[b][0], sel); o[1] = __builtin_amdgcn_perm(xf[b][3], xf[b][2], sel);
      o[2] = __builtin_amdgcn_perm(xf[b + 1][1], xf[b + 1][0], sel); o[3] = __builtin_amdgcn_perm(xf[b + 1][3], xf[b + 1][2], sel);
      PIN(o); pm[st & 1] = o;
    };
    auto gstep = [&](int t, int u, int s, int z) AI {
      float* T_ = gt[z]; float* E_ = ge[z]; const int i = s >> 1;
      auto v = [&](int n) AI { return acc[t][8 * u + n]; };
      if (s == 0) { T_[0] = v(0) * v(0); PIN(T_[0]); }
      if ((s & 1) == 0) {
        if (i < 8) { T_[i] = v(i) * __builtin_fmaf(T_[i], k1, k0); PIN(T_[i]); }
        if (i >= 1) { E_[i - 1] = __builtin_amdgcn_rcpf(__builtin_fmaf(E_[i - 1], sdf, sdf)); PIN(E_[i - 1]); }
      } else {
        if (i < 8) { E_[i] = __builtin_amdgcn_exp2f(T_[i]); PIN(E_[i]); }
        if (i >= 1) { T_[i - 1] = v(i - 1) * E_[i - 1]; if ((i & 1) == 0) { unsigned w_ = pk2(T_[i - 2], T_[i - 1]); PIN(w_); go[z][(i >> 1) - 1] = w_; } else PIN(T_[i - 1]); }
        if (i + 1 < 8) { T_[i + 1] = v(i + 1) * v(i + 1); PIN(T_[i + 1]); }
      }
      if (s == 17 && (ABL & 2)) { PIN(go[z]); } else if (s == 17) { st16(rw, hoff, hso + (t == 0 ? flh : fl0 + 64 * (t - 1) + 32 * SET) * 64 + u * 512, go[z]); cnt++; if (t == 2) mst = cnt; }
    };
    const int gl = 36 * c + (lane < 35 ? lane : 35);
    auto gather = [&]() AI {
      BLD("dwordx2", sg, dws, r * 8, WS_ST + (rt * 128 + c * 32) * 8, " sc1");
#pragma unroll
      for (int i = 0; i < 3 * (1 - SET); i++) BLD("dwordx4", cq[i], dws, gl * 16, WS_CS + i * 36864 + nt * 2304, " sc1");
    };
    WAITV(4 - SET); BARX();
    bs[0] = tbM; PIN(bs[0]);
    xread(0); xread(1);
#pragma unroll
    for (int F = 0; F < D; F++) fetch(F);
    xread(2); xread(3);
    if (SET == 1) perm(0);
    SB();
    sfor<180>([&](auto TC_) AI {
      constexpr int T = decltype(TC_)::v, k = T / 18, st = T % 18, F = T + D, t = tile(k);
      if constexpr (T == 5) { WAITL(2); BARX(); }
      if constexpr (T == 9) { }
      if constexpr (T == 14) { }
      if constexpr (T == 19) { }
      if constexpr (T == 32) { }
      if constexpr (T == 26) { }
      if constexpr (T == 65) { if (SET == 1) WAITL(4); BARX(); }
      if constexpr (F % 18 == 0) {
        constexpr int kb = F / 18;
        WAITV(cnt - (kb == 9 ? mst : mark[kb]));
        if (kb <= 2 || kb == 10) WAITL(0); else if (kb == 3) WAITL(2); else if (kb == 4) WAITL(4);
        if (kb == 1) { } if (kb == 2) { } BARX();
        if ((kb == 8 || kb == 9) && SET == 0) { put_flag(rw, tid == 0, (kb == 8 ? WS_F0 : WS_F1) + lid * 4, tag); cnt++; }
        if constexpr (kb < 10) { bs[kb] = (isM(kb) ? tbM : tbP) + slotof(kb) * SLOT; PIN(bs[kb]); }
      }
      if constexpr (F < 180) fetch(F);
      if constexpr (SET == 1 && isM(k)) {
        const int a = (st & 1) ? 5 : 0;
        acc[a] = mfma32(pm[st & 1], ring[T % R], (k == 0 && st < 2) ? zero : acc[a]);
        if (st < 17) perm(T + 1);
      } else {
        acc[t] = mfma32(ring[T % R], xf[18 * half(k) + st], (half(k) == 0 && st == 0) ? zero : acc[t]);
      }
      if (SET == 1 && T == 35) perm(36);
      if constexpr (T < 4) { xread(4 + 4 * T); xread(5 + 4 * T); xread(6 + 4 * T); xread(7 + 4 * T); }
      if constexpr (T >= 32 && T < 38) { xread(20 + 3 * (T - 32)); xread(21 + 3 * (T - 32)); xread(22 + 3 * (T - 32)); }
      constexpr int xp = T == 5 ? 0 : T == 7 ? 1 : T == 8 ? 2 : T == 10 ? 3 : T == 11 ? 4 : T == 13 ? 5 : T == 24 ? 6 : T == 25 ? 7 : -1;
      if constexpr (xp >= 0) { GLDS(dx, (xp & 1) ? xvo1 : xvo, 0, 640 + 128 * (xp >> 1), xlb, (xp & 1) * 5120 + 1024 * (xp >> 1)); mark[2] = ++cnt; }
      if constexpr (T < 5 && 2 * T + 1 - SET < 9) { GLDS(dw1, wvo, wso, (2 * T + 1 - SET) * 32 * 9216, wlb, SLOT + 4096 * (2 * T + 1 - SET)); mark[1] = ++cnt; }
      if constexpr (F >= 18 && F < 180) {
        constexpr int kb = F / 18, fs = F % 18;
        constexpr int ks = kb <= 2 ? (fs < 9 ? kb + 1 : -1) : kb == 3 ? (fs < 9 ? 4 : 5) : (fs % 2 == 0 ? kb + 2 : -1);
        constexpr int o = (SET + ks) & 1;
        constexpr int n = ks <= 5 ? ((fs % 9) % 2 == o ? fs % 9 : 9) : ((fs / 2) % 2 == o ? fs / 2 : 9);
        if constexpr (ks >= 2 && n < 9 && !((ABL & 1) && ks >= 5 && ks < 10)) {
          if constexpr (ks == 2) GLDS(dm, mvo, mso + mhf + n * mstep, 0, mlb, 1024 * n);
          else if constexpr (ks <= 9) GLDS(dw1, wvo, wso, half(ks) * 288 * 9216 + (tile(ks) - 1) * 128 + n * 32 * 9216, wlb, slotof(ks) * SLOT + 4096 * n);
          else {
            const int e = 64 * n + lane; w2o[n] = (e / 18) * 1152 + (e % 18) * 16;
            if (n < 2) w2s = ftile(4 * (ks - 10) + c) * 36864 + q * 288;
            w2stage(w2s, ks - 10, n);
          }
          mark[ks] = ++cnt;
        }
      }
      if constexpr (T == 10) asm volatile("s_waitcnt vmcnt(%3)" : "+v"(uu[0]), "+v"(uu[1]), "+v"(uu[2]) : "i"(cnt + 1 - SET) : "memory");
      if constexpr (T == 10 || T == 11) {
#pragma unroll
        for (int i = 2 * (T - 10); i < 2 * (T - 10) + 2; i++) {
          cs[2 * i] = blo(uu[0][i]) + blo(uu[1][i]) + blo(uu[2][i]); cs[2 * i + 1] = bhi(uu[0][i]) + bhi(uu[1][i]) + bhi(uu[2][i]);
          PIN(cs[2 * i]); PIN(cs[2 * i + 1]);
        }
      }
      if constexpr (T == 12) {
#pragma unroll
        for (int i = 0; i < 4; i++) { cs[i] = swadd32(cs[i], cs[i + 4]); PIN(cs[i]); }
      }
      if constexpr (T == 13) {
        const float a = dppadd<0x128>(swadd16(cs[0], cs[2])), b = dppadd<0x128>(swadd16(cs[1], cs[3]));
        *(LAS float*)(smem + SP_CS + (lane & 7) * 256 + (lane >> 3) * 32 + wv * 4) = (lane & 8) ? b : a;
      }
      if constexpr (SET == 0 && T == 14) { cr[0] = *(LAS f32x4*)(smem + SP_CS + lane * 32); cr[1] = *(LAS f32x4*)(smem + SP_CS + lane * 32 + 16); }
      if constexpr (SET == 0 && T == 16) {
        const float s = ((cr[0][0] + cr[0][1]) + (cr[0][2] + cr[0][3])) + ((cr[1][0] + cr[1][1]) + (cr[1][2] + cr[1][3]));
        __builtin_amdgcn_raw_buffer_store_b64(u32x2{pk2(s, s - bfr(s)), tag}, rw, (c == 0 && cu < 54) ? lane * 8u : OOB, WS_CS + g3 * 36864 + cg * 512, 16);
        cnt++;
      }
      if constexpr (T == 41) {
        gather();
        if (SET == 0) BLD("dword", bia, mkdesc(b1, 9216), gl * 4, 576 * nt, "");
        else BLD("dwordx2", fc, mkdesc(&ST.fc, 2304), r * 8, (has_dft ? nt : 0) * 256, "");
        cnt += SET == 0 ? 5 : 2;
      }
      if constexpr (T == 48) {
        auto gwait = [&]() AI {
          if (SET == 0) asm volatile("s_waitcnt vmcnt(0)" : "+v"(sg), "+v"(cq[0]), "+v"(cq[1]), "+v"(cq[2]), "+v"(bia)::"memory");
          else asm volatile("s_waitcnt vmcnt(0)" : "+v"(sg), "+v"(fc)::"memory");
          unsigned m = sg[1] ^ tag;
          if (SET == 0) m |= (cq[0][1] ^ tag) | (cq[0][3] ^ tag) | (cq[1][1] ^ tag) | (cq[1][3] ^ tag) | (cq[2][1] ^ tag) | (cq[2][3] ^ tag);
          return __builtin_amdgcn_ballot_w64(m != 0) != 0;
        };
        for (int it = 0; gwait(); it++) {
          if (it >= SPIN) { put_flag(rw, true, WS_ERR, 2); break; }
          __builtin_amdgcn_s_sleep(4);
          gather();
        }
        if (SET == 0) {
          *(LAS u32x4*)(smem + SP_T + gl * 32) = u32x4{cq[0][0], cq[1][0], cq[2][0], bia & 0xffffu};
          *(LAS u32x4*)(smem + SP_T + gl * 32 + 16) = u32x4{cq[0][2], cq[1][2], cq[2][2], bia >> 16};
        }
      }
      if constexpr (T == 51) {
        const unsigned nm = hh ? 0u : ((sg[0] & 0xffffu) ^ 0x8000u) * 0x10001u;
        sf = u32x4{nm, nm, nm, hh ? 0u : sg[0] >> 16};
        sdf = bhi(sg[0]);
        PIN(sf); PIN(sdf);
      }
      if constexpr (SET == 1 && T == 52) *(LAS float*)(smem + SP_RS + c * 128 + r * 4) = __builtin_amdgcn_rcpf(sdf);
      if constexpr (T == (SET == 0 ? 52 : 70)) {
        const float rstd = __builtin_amdgcn_rcpf(sdf), c0 = -2.0f * 0.7978845608028654f * 1.4426950408889634f, c1 = c0 * 0.044715f;
        k0 = c0 * rstd; k1 = c1 * rstd * rstd * rstd;
        PIN(k0); PIN(k1);
      }
      if (SET == 0 && T == 51) tf = *(LAS u32x4*)(smem + SP_T + (flh + r) * 16);
      if (SET == 0 && T == 53) acc[0] = mfma32(tf, sf, acc[0]);
      if constexpr (!isM(k)) {
        if (half(k) == 1 && st == 15) tf = *(LAS u32x4*)(smem + SP_T + (fl0 + 64 * (t - 1) + 32 * SET + r) * 16);
        if (half(k) == 1 && st == 17) acc[t] = mfma32(tf, sf, acc[t]);
      } else if constexpr (SET == 1 && k == 2 && st == 17) {
        acc[0] = mfma32(sf, u32x4{fc[0], 0, 0, 0}, acc[0]);
        acc[5] = mfma32(sf, u32x4{fc[1], 0, 0, 0}, acc[5]);
      }
      constexpr int TW = 73;
      if constexpr (SET == 1 && T >= TW && T < TW + 40 && (T - TW) % 2 == 0 && (T < TW + 16 || T >= TW + 32)) {
        const int wset = 8 * (lid >> 2) + (lane & 7), wp = 5 * c + (lane >> 3), wcol = 72 * q + (q == 3) + 4 * wp;
        const bool on = lane < 40 && wp < 19 && wcol <= 285;
        if constexpr (T < TW + 16) {
          constexpr int i = (T - TW) / 2;
          if (i == 0) { wvv = ((16 * (wset >> 7) + (wset & 15) + 256 * ((wset >> 4) & 7)) * 289 + wcol) * 2u; PIN(wvv); wvv = on ? wvv : OOB; }
          BLD("dwordx2", wl[i], mkdesc((i & 1) ? wi : wr, 2048 * 578), wvv, (i >> 1) * 64 * 578, "");
          mw = ++cnt;
        } else {
          constexpr int j = (T - TW - 32) / 2;
          if (j == 0) {
            asm volatile("s_waitcnt vmcnt(%8)" : "+v"(wl[0]), "+v"(wl[1]), "+v"(wl[2]), "+v"(wl[3]), "+v"(wl[4]), "+v"(wl[5]), "+v"(wl[6]), "+v"(wl[7]) : "i"(cnt - mw) : "memory");
            wvv = wcol * 8192u + wset * 16; PIN(wvv); wvv = on ? wvv : OOB;
          }
          u32x4 o;
#pragma unroll
          for (int i = 0; i < 4; i++) o[i] = __builtin_amdgcn_perm(wl[2 * i + 1][j >> 1], wl[2 * i][j >> 1], (j & 1) ? 0x07060302u : 0x05040100u);
          st16(rw, wvv, WS_WT + j * 8192, o);
          mw = ++cnt;
        }
      }
      if constexpr (SET == 0 && T >= 74 && T < 134 && (T - 74) % 6 == 0) {
        constexpr int z = (T - 74) / 6, d = z < 3 ? 0 : z < 5 ? 1 : z < 8 ? 2 : 3, i = z - (d * 5 + 1) / 2;
        st16(rw, d == bb ? OOB : lane * 16, pbase + (d * 16 + bb * 4) * 3072 + i * 1024, u32x4{~0u, ~0u, ~0u, ~0u});
        cnt++;
      }
      if constexpr (SET == 0 && T >= 163 && T < 173 && (T & 1) == 1) hload((T - 163) / 2);
      if constexpr (SET == 1) {
        if constexpr (T == 161) { BLD("dwordx4", pf, dws, lane * 16, WS_F0, " sc1"); mpf = ++cnt; }
        if constexpr (T >= 162 && T < 170) { tload(T < 166 ? fa : f32, T < 166 ? c * 6144 : 49152, (T - 162) & 3); cnt++; }
        if constexpr (T == 170) {
          asm volatile("s_waitcnt vmcnt(%1)" : "+v"(pf) : "i"(cnt - mpf) : "memory");
          poll4(rw, dws, pf, lane * 16, WS_F0, tag);
        }
        if constexpr (T >= 170) { xload(0, T - 170); mx = ++cnt; }
      }
      if constexpr (PREF && T == 6) {
        const int u = wv * 256 + id;
        const int u0 = u < 648 ? 0 : u < 793 ? 648 : u < 938 ? 793 : u < 1028 ? 938 : 1028;
        const u32x4 dp = u < 648 ? dw2 : u < 793 ? mkdesc(wr, 2048 * 578) : u < 938 ? mkdesc(wi, 2048 * 578) : u < 1028 ? mkdesc(&GT, sizeof(GT)) : dst;
        GLDS1(dp, lane * 128u + (u - u0) * 8192u, 0, SP_PF);
        cnt++;
      }
      if constexpr (SET == 0) {
        if constexpr (T >= 55 && T < 73) gstep(0, 0, T - 55, 2);
        if constexpr (T >= 73 && T < 109 && ((T - 73) & 1) == 0) gstep(0, 1, (T - 73) >> 1, 2);
      } else if constexpr (T >= 55 && T < 61) {
        constexpr int u = (T - 55) / 3, ph = (T - 55) % 3;
        const int bc = 32 * nt + r;
        const float b1c = bc == 144 ? 0.f : 1.f, b2c = bc < 144 ? -1.f : 1.f, a2c = -b1c * b2c;
        if (ph == 0) { cr[0] = *(LAS f32x4*)(smem + SP_RS + c * 128 + u * 64 + hh * 16); cr[1] = *(LAS f32x4*)(smem + SP_RS + c * 128 + u * 64 + 32 + hh * 16); }
        else {
          float o1[4], o2[4];
#pragma unroll
          for (int i = 0; i < 4; i++) {
            const float e = acc[0][8 * u + 4 * (ph - 1) + i] * cr[ph - 1][i], o = acc[5][8 * u + 4 * (ph - 1) + i] * cr[ph - 1][i];
            o1[i] = __builtin_fmaf(b1c, o, e); o2[i] = __builtin_fmaf(a2c, e, b2c * o);
          }
          LAS char* p = smem + r * 272 + (2 * u + hh) * 64 + c * 16 + (ph - 1) * 8;
          *(LAS u32x2*)p = u32x2{pk2(o1[0], o1[1]), pk2(o1[2], o1[3])};
          *(LAS u32x2*)(p + 32 * 272) = u32x2{pk2(o2[0], o2[1]), pk2(o2[2], o2[3])};
        }
      } else if constexpr (T == 65) {
#pragma unroll
        for (int i = 0; i < 4; i++) xq[i] = *(LAS u32x4*)(smem + (16 * c + 4 * i + g) * 272 + l15 * 16);
      } else if constexpr (T >= 67 && T < 71) {
        const int bc = 32 * nt + 16 * (c & 1) + 4 * (T - 67) + g;
        const int p1 = 2 * bc - (bc > 144 ? 287 : 0);
        int p2 = (bc < 144 ? 576 : 865) - 2 * bc; p2 = bc == 0 ? 1 : p2; p2 = bc == 144 ? 289 : p2;
        unsigned vo = (c < 2 ? p1 : p2) * 4096u + l15 * 16; PIN(vo);
        st16(rw, has_dft ? vo : OOB, WS_XS + bh * 2048 + aq * 256, xq[T - 67]);
        cnt++;
      }
      if constexpr (T >= 73 && (ABL & 4)) { if constexpr ((T - 73) % 36 == 0) PIN(acc[(T - 73) / 36 + 1]); } else if constexpr (T >= 73) {
        const int pt = (T - 73) / 36 + 1, s = (T - 73) % 36;
        if (s < 18) gstep(pt, 0, s, 0);
        if (s >= 16 && s < 34) gstep(pt, 1, s - 16, 1);
      }
      SB();
    });
#pragma unroll
    for (int s = 0; s < 18; s++) {
      if (s == 12 && SET == 0) { BLD("dwordx4", pf, dws, 0, WS_F1 + (lid >> 2) * 16, " sc1"); mpf = ++cnt; }
      gstep(4, 0, s, 0); gstep(4, 1, s, 1);
    }
    mst = cnt;
  }
  if constexpr (SET == 0) {
    constexpr int D = DIST2, R = RING2;
    const unsigned tb = (16 * (g >> 1) + 4 * (g & 1) + q4) * 288 + p4 * 8;
    const unsigned zb = 2 * SLOT + (8 * (g >> 1) + 2 * (g & 1) + (q4 >> 1)) * 512 + (2 * (p4 >> 1) + (q4 & 1)) * 64 + ((c ^ (2 * (g & 1) + (q4 >> 1))) * 16) + (p4 & 1) * 8;
    u32x2 ra[R], rb[R], xv[5], bv[5];
    f32x4 acc[9][2];
    LAS char* bs[11];
    const f32x4 z4 = {0, 0, 0, 0};
    auto fad = [&](int F) AI { return bs[F / 36] + ((F % 36) / 9) * 9216 + (F % 9) * 32; };
    bs[0] = smem + tb; PIN(bs[0]);
#pragma unroll
    for (int F = 0; F < D; F++) { ra[F] = trd(fad(F)); rb[F] = trd(fad(F) + 2304); }
    asm volatile("s_waitcnt vmcnt(%1)" : "+v"(pf) : "i"(cnt - mpf) : "memory");
    poll4(rw, dws, pf, 0, WS_F1 + (lid >> 2) * 16, tag);
#pragma unroll
    for (int ks = 5; ks < HW; ks++) hload(ks);
    const unsigned orow = (4 * aq + c + 32 * (32 * bh + 16 * (bb >> 1) + l15)) * 1152u + g * 8, ocol = 288 * q + 160 * (bb & 1);
    SB();
    sfor<369>([&](auto GC_) AI {
      constexpr int G = decltype(GC_)::v, ks = G / 9, dt = G % 9, F = G + D;
      if constexpr (F % 36 == 0) {
        constexpr int j = F / 36;
        if (j == 1) WAITV(cnt - mst);
        if (j < 10) { BARX(); }
        if (j == 1) { put_flag(rw, tid == 0, WS_FH + lid * 4, tag); cnt++; }
        bs[j] = smem + (j < 10 ? (j & 3) * SLOT : SP) + tb; PIN(bs[j]);
      }
      if constexpr (dt == 0 && ks < 36) asm volatile("s_waitcnt vmcnt(%2)" : "+v"(hb[ks % HW][0]), "+v"(hb[ks % HW][1]) : "i"(cnt - mh[ks]) : "memory");
      const u32x4 a = {ra[G % R][0], ra[G % R][1], rb[G % R][0], rb[G % R][1]};
      acc[dt][0] = mfma16(a, hb[ks % HW][0], ks == 0 ? z4 : acc[dt][0]);
      SB();
      if constexpr (F < 369) ra[F % R] = trd(fad(F));
      SB();
      acc[dt][1] = mfma16(a, hb[ks % HW][1], ks == 0 ? z4 : acc[dt][1]);
      SB();
      if constexpr (F < 369) rb[F % R] = trd(fad(F) + 2304);
      if constexpr (G == 9 * (18 - HW) + 7) { BLD("dwordx4", pf, dws, 0, WS_FH + (lid >> 2) * 16, " sc1"); mpf = ++cnt; }
      if constexpr (dt == 8 && ks + HW < 36) {
        if (ks + HW == 20) {
          asm volatile("s_waitcnt vmcnt(%1)" : "+v"(pf) : "i"(cnt - mpf) : "memory");
          poll4(rw, dws, pf, 0, WS_FH + (lid >> 2) * 16, tag);
        }
        hload(ks + HW);
      }
      if constexpr (ks >= 35 && ks < 40 && (dt == 5 || dt == 6)) {
        constexpr int sp = ks - 35;
        hb[(ks + 1) % HW][dt - 5] = trd2(smem + zb + (dt - 5) * 256 + sp * 8192 - (sp == 4 ? (g >> 1) * 4096 : 0), 2048);
      }
      if constexpr (G >= 270 && G < 320 && G % 5 == 0) {
        constexpr int i = (G - 270) / 10;
        const bool on = i < 4 || (bb & 1) == 0;
        if (G % 10 == 0) BLD("dwordx2", xv[i], dx, on ? orow : OOB, ocol + 32 * i, "");
        else BLD("dwordx2", bv[i], mkdesc(b2, 1152), on ? g * 8 : OOB, ocol + 32 * i, "");
        cnt++;
      }
      SB();
    });
    auto pk4 = [&](f32x4 a, f32x4 b) AI { return u32x4{pk2(a[0], a[1]), pk2(a[2], a[3]), pk2(b[0], b[1]), pk2(b[2], b[3])}; };
#pragma unroll
    for (int d = 0; d < 4; d++)
#pragma unroll
      for (int i = 0; i < 3 - (d & 1); i++) {
        const int t0 = 5 * (d & 1) + 2 * i;
        st16(rw, d == bb ? OOB : lane * 16 + i * 1024, pbase + (d * 16 + bb * 4) * 3072, pk4(acc[t0][d >> 1], acc[i < 2 ? t0 + 1 : t0][d >> 1]));
      }
    u32x4 pp[3][3];
    auto pload = [&]() AI {
#pragma unroll
      for (int s = 0; s < 3; s++)
#pragma unroll
        for (int i = 0; i < 3; i++) BLD("dwordx4", pp[s][i], dws, (i < 2 || (bb & 1) == 0) ? lane * 16 + i * 1024 : OOB, pbase + (bb * 16 + ((bb + 1 + s) & 3) * 4) * 3072, " sc1");
    };
    asm volatile("s_waitcnt vmcnt(10)" : "+v"(xv[0]), "+v"(xv[1]), "+v"(xv[2]), "+v"(xv[3]), "+v"(xv[4]), "+v"(bv[0]), "+v"(bv[1]), "+v"(bv[2]), "+v"(bv[3]), "+v"(bv[4])::"memory");
    f32x4 own[5];
#pragma unroll
    for (int i = 0; i < 5; i++) {
      const int i2 = i < 4 ? 5 + i : 8;
#pragma unroll
      for (int e = 0; e < 4; e++) {
        const float lo = (bb & 2) ? acc[i][1][e] : acc[i][0][e], hi = (bb & 2) ? acc[i2][1][e] : acc[i2][0][e];
        const unsigned xw = xv[i][e >> 1], bw = bv[i][e >> 1];
        own[i][e] = ((bb & 1) ? hi : lo) + ((e & 1) ? bhi(xw) + bhi(bw) : blo(xw) + blo(bw));
      }
      PIN(own[i]);
    }
    for (int it = 0;; it++) {
      pload();
      asm volatile("s_waitcnt vmcnt(0)" : "+v"(pp[0][0]), "+v"(pp[0][1]), "+v"(pp[0][2]), "+v"(pp[1][0]), "+v"(pp[1][1]), "+v"(pp[1][2]), "+v"(pp[2][0]), "+v"(pp[2][1]), "+v"(pp[2][2])::"memory");
      unsigned m = 0;
#pragma unroll
      for (int i = 0; i < 36; i++) { const unsigned w = pp[i / 12][(i >> 2) % 3][i & 3]; m = m > w ? m : w; }
      if (__builtin_amdgcn_ballot_w64(m == 0xffffffffu) == 0) break;
      if (it >= SPIN / 16) { put_flag(rw, true, WS_ERR, 4); break; }
    }
    const rsrc_t ro = mkrsrc(out, 2048 * 1152);
#pragma unroll
    for (int i = 0; i < 5; i++) {
      float v[4];
#pragma unroll
      for (int e = 0; e < 4; e++) {
        const unsigned w0 = pp[0][i >> 1][2 * (i & 1) + (e >> 1)], w1 = pp[1][i >> 1][2 * (i & 1) + (e >> 1)], w2_ = pp[2][i >> 1][2 * (i & 1) + (e >> 1)];
        v[e] = (e & 1) ? (own[i][e] + bhi(w0)) + (bhi(w1) + bhi(w2_)) : (own[i][e] + blo(w0)) + (blo(w1) + blo(w2_));
      }
      __builtin_amdgcn_raw_buffer_store_b64(u32x2{pk2(v[0], v[1]), pk2(v[2], v[3])}, ro, (i < 4 || (bb & 1) == 0) ? orow : OOB, ocol + 32 * i, 0);
    }
  } else {
    auto stage = [&](int j) AI {
      const unsigned so = ftile(4 * j + c) * 36864 + q * 288;
#pragma unroll
      for (int n = 0; n < 9; n++) w2stage(so, j, n);
      cnt += 9; mark[j] = cnt;
    };
    auto bar = [&](int j, int m) AI { WAITV(cnt - m); WAITL(0); BARX(); };
    const u32x4 dg = mkdesc(&GT, sizeof(GT));
    f32x4 t1[4], t2[4];
    const f32x4 z4 = {0, 0, 0, 0};
    auto xpin = [&](int z) AI {
#pragma unroll
      for (int i = 0; i < 8; i++) PIN(xx[z][i]);
      PIN(wt[z][0]); PIN(wt[z][1]);
    };
    auto tpin = [&](u32x4 (&f)[2][3]) AI {
#pragma unroll
      for (int t = 0; t < 2; t++) { PIN(f[t][0]); PIN(f[t][1]); f[t][2] = f[t][1] ^ 0x80008000u; }
    };
    if (has2) {
#pragma unroll
      for (int i = 0; i < 10; i++) xload(1, i);
    }
#pragma unroll
    for (int i = 0; i < 4; i++) tload(fb, 24576 + c * 6144, i);
    cnt += 4;
    const int mb = cnt;
    stage(2);
    {
      const float f1 = (16 * c + l15) * (1.0f / 2048.0f), f2 = (16 * c + 4 * g) * (1.0f / 2048.0f);
#pragma unroll
      for (int at = 0; at < 2; at++)
#pragma unroll
        for (int i = 0; i < 4; i++) {
          const float a1 = (float)(16 * at + 4 * g + i) * f1, a2 = (float)(16 * at + l15) * (f2 + i * (1.0f / 2048.0f));
          t1[2 * at][i] = __builtin_amdgcn_cosf(a1); t1[2 * at + 1][i] = __builtin_amdgcn_sinf(a1);
          t2[2 * at][i] = __builtin_amdgcn_cosf(a2) * (1.0f / 2048.0f); t2[2 * at + 1][i] = __builtin_amdgcn_sinf(a2) * (1.0f / 2048.0f);
        }
#pragma unroll
      for (int i = 0; i < 4; i++) { PIN(t1[i]); PIN(t2[i]); }
    }
    WAITV(cnt - mx);
    tpin(fa); tpin(f32); xpin(0);
    auto front = [&](int z) AI {
      u32x4 br, bi, yr, yi;
#pragma unroll
      for (int at = 0; at < 2; at++) {
        f32x4 tr = z4, ti = z4;
#pragma unroll
        for (int t = 0; t < 2; t++) {
          const u32x4 xr = xx[z][2 * t + at], xi = xx[z][4 + 2 * t + at];
          tr = mfma16(xr, fa[t][0], tr); ti = mfma16(xi, fa[t][0], ti);
          tr = mfma16(xi, fa[t][1], tr); ti = mfma16(xr, fa[t][2], ti);
        }
        const f32x4 co = t1[2 * at], si = t1[2 * at + 1];
        const f32x4 ur = tr * co + ti * si, ui = ti * co - tr * si;
        br[2 * at] = pk2(ur[0], ur[1]); br[2 * at + 1] = pk2(ur[2], ur[3]);
        bi[2 * at] = pk2(ui[0], ui[1]); bi[2 * at + 1] = pk2(ui[2], ui[3]);
      }
#pragma unroll
      for (int ct = 0; ct < 2; ct++) {
        f32x4 xr = mfma16(f32[ct][0], br, z4), xi = mfma16(f32[ct][0], bi, z4);
        xr = mfma16(f32[ct][1], bi, xr); xi = mfma16(f32[ct][2], br, xi);
        float vr[4], vi[4];
#pragma unroll
        for (int i = 0; i < 4; i++) { const unsigned w = wt[z][ct][i]; const float a = blo(w), b = bhi(w); vr[i] = xr[i] * a - xi[i] * b; vi[i] = xr[i] * b + xi[i] * a; }
        yr[2 * ct] = pk2(vr[0], vr[1]); yr[2 * ct + 1] = pk2(vr[2], vr[3]);
        yi[2 * ct] = pk2(vi[0], vi[1]); yi[2 * ct + 1] = pk2(vi[2], vi[3]);
      }
#pragma unroll
      for (int at = 0; at < 2; at++) {
        f32x4 pr = mfma16(yr, f32[at][0], z4), pi = mfma16(yi, f32[at][0], z4);
        pr = mfma16(yi, f32[at][2], pr); pi = mfma16(yr, f32[at][1], pi);
        const f32x4 co = t2[2 * at], si = t2[2 * at + 1];
        const f32x4 ur = pr * co - pi * si, ui = pi * co + pr * si;
        LAS char* p = smem + SP + z * 8192 + (at * 4 + (c >> 1)) * 1024 + lane * 16 + (c & 1) * 8;
        *(LAS u32x2*)p = u32x2{pk2(ur[0], ur[1]), pk2(ur[2], ur[3])};
        *(LAS u32x2*)(p + 2048) = u32x2{pk2(ui[0], ui[1]), pk2(ui[2], ui[3])};
      }
    };
    front(0);
    bar(1, mb);
    tpin(fb);
    stage(3);
    auto back = [&](int z) AI {
      const int pair = kc[z] == 288 ? 0 : kc[z];
      const bool on = kc[z] == 0 ? (g & 1) == 0 : (kc[z] == 288 ? (g & 1) == 1 : true);
#pragma unroll
      for (int at = 0; at < 2; at++) {
        f32x4 zr = z4, zi = z4;
#pragma unroll
        for (int t = 0; t < 2; t++) {
          LAS char* p = smem + SP + z * 8192 + (at * 4 + t) * 1024 + lane * 16;
          const u32x4 pr = *(LAS u32x4*)p, pi = *(LAS u32x4*)(p + 2048);
          zr = mfma16(fb[t][0], pr, zr); zi = mfma16(fb[t][0], pi, zi);
          zr = mfma16(fb[t][2], pi, zr); zi = mfma16(fb[t][1], pr, zi);
        }
        const unsigned r0 = pk2(zr[0], zr[1]), r1 = pk2(zr[2], zr[3]);
        const unsigned i0 = kc[z] == 288 ? r0 : pk2(zi[0], zi[1]), i1 = kc[z] == 288 ? r1 : pk2(zi[2], zi[3]);
        const auto s0 = __builtin_amdgcn_permlane16_swap(r0, i0, false, false), s1 = __builtin_amdgcn_permlane16_swap(r1, i1, false, false);
        const unsigned a0 = s0[0], a1 = s1[0], a2 = s0[1], a3 = s1[1];
        st16(rw, on ? q4 * 1024u + g * 64 + ((p4 ^ (pair & 3)) * 16) : OOB, WS_Z + pair * 8192 + at * 4096 + c * 256, u32x4{a0, a1, a2, a3});
      }
    };
    back(0);
    cnt += 2;
    if (has2) { xpin(1); front(1); }
    bar(2, mark[2]);
    if (has2) back(1);
    const int mz = cnt;
    stage(4);
    bar(3, mz);
    put_flag(rw, tid == 256, WS_FZ + lid * 4, tag); cnt++;
    stage(5);
#pragma unroll
    for (int n = 0; n < 3; n++) { const int pc = n < 2 ? c + 4 * n : 8; GLDS(dg, lane * 16, (nt * 5 + 4) * 9216 + pc * 1024, 0, SP + pc * 1024, 0); }
    cnt += 3;
    bar(4, mark[4]);
    stage(6);
    bar(5, mark[5]);
    stage(7);
    bar(6, mark[6]);
    stage(8);
    bar(7, mark[7]);
    BLD("dwordx4", pf, dws, lane * 16, WS_FZ, " sc1"); mpf = ++cnt;
#pragma unroll
    for (int n = 0; n < 9; n++) GLDS(dg, lane * 16, (nt * 5 + c) * 9216, 1024 * n, mlb, SLOT + 1024 * n);
    cnt += 9;
    asm volatile("s_waitcnt vmcnt(%1)" : "+v"(pf) : "i"(cnt - mpf) : "memory");
    poll4(rw, dws, pf, lane * 16, WS_FZ, tag);
    u32x4 zz[9];
#pragma unroll
    for (int n = 0; n < 9; n++) BLD("dwordx4", zz[n], dws, hh * 8192 + r * 16, WS_Z + (72 * bb + 2 * c + 8 * n) * 8192 + rt * 512, " sc1");
    cnt += 9;
    bar(8, mark[8]);
    put_flag(rw, lid == 0 && tid == 256, WS_E, tag);
    WAITV(1);
#pragma unroll
    for (int n = 0; n < 9; n++) { PIN(zz[n]); *(LAS u32x4*)(smem + 2 * SLOT + (c + 4 * n) * 1024 + lane * 16) = zz[n]; }
    bar(9, cnt);
  }
  if (SET == 0) asm volatile(".Lkend:");
  };
  if (wv < 4) body(IC<0>{}); else body(IC<1>{});
}

extern "C" void kernel_launch(void* const* d_in, const int* in_sizes, int n_in, void* d_out, int out_size, void* d_ws, size_t ws_size, hipStream_t stream) {
  (void)hipFuncSetAttribute((const void*)kmain, hipFuncAttributeMaxDynamicSharedMemorySize, LDSZ);
  kmain<<<256, 512, LDSZ, stream>>>((const u16*)d_in[0], (const u16*)d_in[1], (const u16*)d_in[2], (const u16*)d_in[4], (const u16*)d_in[5],
                                    (const u16*)d_in[6], (const u16*)d_in[7], (u16*)d_out, (char*)d_ws);
}
```

```cpp
#include <hip/hip_runtime.h>
#include <hip/hip_bf16.h>
#include <utility>

typedef unsigned short u16;
typedef __attribute__((ext_vector_type(8))) __bf16 bf16x8;
typedef __attribute__((ext_vector_type(2))) __bf16 bf16x2;
typedef __attribute__((ext_vector_type(4))) short s16x4;
typedef __attribute__((ext_vector_type(8))) short s16x8;
typedef __attribute__((ext_vector_type(4))) float f32x4;
typedef __attribute__((ext_vector_type(16))) float f32x16;
typedef __attribute__((ext_vector_type(4))) unsigned u32x4;
typedef __attribute__((ext_vector_type(2))) unsigned u32x2;
#define LAS __attribute__((address_space(3)))
#define GAS __attribute__((address_space(1)))
#define DI __device__ __forceinline__

namespace tab {
constexpr double PI_ = 3.14159265358979323846;
constexpr double tsin(double x) { double t = x, s = x; for (int i = 1; i < 12; i++) { t *= -x * x / ((2 * i) * (2 * i + 1)); s += t; } return s; }
constexpr double tcos(double x) { double t = 1, s = 1; for (int i = 1; i < 12; i++) { t *= -x * x / ((2 * i - 1) * (2 * i)); s += t; } return s; }
constexpr double ccos(int n, int N) {
  n %= N; if (n < 0) n += N;
  if (2 * n > N) n = N - n;
  bool neg = false;
  if (4 * n > N) { n = N / 2 - n; neg = true; }
  double v = (8 * n > N) ? tsin(2 * PI_ * (N / 4 - n) / N) : tcos(2 * PI_ * n / N);
  return neg ? -v : v;
}
constexpr double csin(int n, int N) { return ccos(n - N / 4, N); }
constexpr u16 f2bf(double d) { float f = (float)d; unsigned u = __builtin_bit_cast(unsigned, f); u += 0x7fffu + ((u >> 16) & 1u); return (u16)(u >> 16); }
struct C576 { u16 c[576]; u16 g1[576]; u16 g2[576]; };
constexpr C576 mk576() { C576 r{}; for (int i = 0; i < 576; i++) { double c = ccos(i, 576); r.c[i] = f2bf(c); r.g1[i] = f2bf(c / 576.0); r.g2[i] = f2bf(2.0 * c / 576.0); } return r; }
constexpr C576 c576 = mk576();
struct Blk { u16 v[9216]; };
constexpr int fidx(int bc, int d) {
  int n = bc < 144 ? bc * d : (bc == 144 ? ((d & 1) == 0 ? 144 * d : 144 * d + 144) : (bc - 144) * d + 144);
  return n % 576;
}
constexpr Blk mkF(int idx) {
  Blk r{}; int at = idx >> 1, hf = idx & 1;
  for (int st = 0; st < 18; st++) for (int hh = 0; hh < 2; hh++) for (int j = 0; j < 8; j++) {
    int d = 288 * hf + 32 * (st >> 1) + 16 * (j >> 2) + 8 * hh + 2 * (j & 3) + (st & 1);
    for (int cc = 0; cc < 32; cc++) r.v[((st * 2 + hh) * 8 + j) * 32 + cc] = c576.c[fidx(32 * at + cc, d)];
  }
  return r;
}
template <int I> constexpr Blk fblk = mkF(I);
struct FTab { Blk b[18]; };
template <int... I> constexpr FTab mkFT(std::integer_sequence<int, I...>) { return FTab{{fblk<I>...}}; }
constexpr double bf2d(u16 b) { return (double)__builtin_bit_cast(float, (unsigned)b << 16); }
struct FCB { unsigned v[64]; };
constexpr FCB mkFC(int at) {
  FCB r{};
  for (int e = 0; e < 2; e++) for (int cc = 0; cc < 32; cc++) {
    double s = 0;
    for (int d = e; d < 576; d += 2) s += bf2d(c576.c[fidx(32 * at + cc, d)]);
    u16 hi = f2bf(s), lo = f2bf(s - bf2d(hi));
    r.v[cc * 2 + e] = (unsigned)hi | ((unsigned)lo << 16);
  }
  return r;
}
template <int I> constexpr FCB fcb = mkFC(I);
struct FCT { FCB b[9]; };
template <int... I> constexpr FCT mkFCT(std::integer_sequence<int, I...>) { return FCT{{fcb<I>...}}; }
struct GBlk { u16 v[4608]; };
constexpr GBlk mkG(int idx) {
  GBlk r{}; int step = idx % 5, q = (idx / 5) & 3, b = idx / 20;
  for (int row = 0; row < 32; row++) {
    int rho = 32 * step + row;
    if (rho >= 144) continue;
    int kc = 72 * b + (rho >> 1), ri = rho & 1;
    for (int c = 0; c < 144; c++) {
      int d = 144 * q + c;
      r.v[row * 144 + c] = kc == 0 ? c576.g1[(ri * 288 * d) % 576] : c576.g2[(kc * d + ri * 144) % 576];
    }
  }
  return r;
}
template <int I> constexpr GBlk gblk = mkG(I);
struct GTab { GBlk b[80]; };
template <int... I> constexpr GTab mkGT(std::integer_sequence<int, I...>) { return GTab{{gblk<I>...}}; }
struct C2048 { float c[2048]; };
constexpr C2048 mk2048() { C2048 r{}; for (int i = 0; i < 2048; i++) r.c[i] = (float)ccos(i, 2048); return r; }
constexpr C2048 c2048 = mk2048();
constexpr u16 trig(int n2048, int type) {
  int n = type == 0 ? n2048 : (type == 1 ? n2048 + 1536 : n2048 + 512);
  return f2bf(c2048.c[n & 2047]);
}
struct F64T { u16 v[4][2][3][64][8]; };
constexpr F64T mkF64(int which) {
  F64T r{};
  for (int j = 0; j < 4; j++) for (int t = 0; t < 2; t++) for (int ty = 0; ty < 3; ty++) for (int l = 0; l < 64; l++) for (int jj = 0; jj < 8; jj++) {
    int g = l >> 4, o = 16 * j + (l & 15);
    int k = which == 0 ? 32 * t + 16 * (g >> 1) + 8 * (jj >> 2) + 4 * (g & 1) + (jj & 3) : 32 * t + 16 * (jj >> 2) + 4 * g + (jj & 3);
    r.v[j][t][ty][l][jj] = trig(o * k * 32, ty);
  }
  return r;
}
struct F32T { u16 v[2][3][64][8]; };
constexpr F32T mkF32() {
  F32T r{};
  for (int ct = 0; ct < 2; ct++) for (int ty = 0; ty < 3; ty++) for (int l = 0; l < 64; l++) for (int jj = 0; jj < 8; jj++) {
    int g = l >> 4, o = 16 * ct + (l & 15), k = 16 * (jj >> 2) + 4 * g + (jj & 3);
    r.v[ct][ty][l][jj] = trig(o * k * 64, ty);
  }
  return r;
}
struct TWT { float v[4][4][64][4]; };
constexpr TWT mkTW(int which) {
  TWT r{};
  for (int j = 0; j < 4; j++) for (int n = 0; n < 4; n++) for (int l = 0; l < 64; l++) for (int i = 0; i < 4; i++) {
    int g = l >> 4, at = n >> 1;
    int a = which == 0 ? 16 * at + 4 * g + i : 16 * at + (l & 15);
    int d = which == 0 ? 16 * j + (l & 15) : 16 * j + 4 * g + i;
    float v = c2048.c[(a * d + ((n & 1) ? 1536 : 0)) & 2047];
    r.v[j][n][l][i] = which == 0 ? v : v * (1.0f / 2048.0f);
  }
  return r;
}
constexpr F64T f64a = mkF64(0), f64b = mkF64(1);
constexpr F32T f32t = mkF32();
constexpr TWT tw1 = mkTW(0), tw2 = mkTW(1);
struct Small { F64T a, b; F32T f; TWT t1, t2; FCT fc; };
}
#if defined(__HIP_DEVICE_COMPILE__)
__device__ __attribute__((aligned(128))) static const tab::FTab FT = tab::mkFT(std::make_integer_sequence<int, 18>{});
__device__ __attribute__((aligned(128))) static const tab::GTab GT = tab::mkGT(std::make_integer_sequence<int, 80>{});
__device__ __attribute__((aligned(128))) static const tab::Small ST = {tab::f64a, tab::f64b, tab::f32t, tab::tw1, tab::tw2, tab::mkFCT(std::make_integer_sequence<int, 9>{})};
#else
__device__ static const tab::FTab FT = {};
__device__ static const tab::GTab GT = {};
__device__ static const tab::Small ST = {};
#endif
typedef unsigned long long u64;
DI float bfr(float x) { return (float)(__bf16)x; }
DI unsigned pk2(float a, float b) { bf16x2 c; c[0] = (__bf16)a; c[1] = (__bf16)b; return __builtin_bit_cast(unsigned, c); }
DI float blo(unsigned u) { return __builtin_bit_cast(float, u << 16); }
DI float bhi(unsigned u) { return __builtin_bit_cast(float, u & 0xffff0000u); }
DI f32x16 mfma32(u32x4 a, u32x4 b, f32x16 c) { return __builtin_amdgcn_mfma_f32_32x32x16_bf16(__builtin_bit_cast(bf16x8, a), __builtin_bit_cast(bf16x8, b), c, 0, 0, 0); }
DI f32x4 mfma16(u32x4 a, u32x4 b, f32x4 c) { return __builtin_amdgcn_mfma_f32_16x16x32_bf16(__builtin_bit_cast(bf16x8, a), __builtin_bit_cast(bf16x8, b), c, 0, 0, 0); }
DI u32x2 trd(LAS char* p) { return __builtin_bit_cast(u32x2, __builtin_amdgcn_ds_read_tr16_b64_v4i16((LAS s16x4*)p)); }
DI u32x4 trd2(LAS char* p, int off2) { u32x2 a = trd(p), b = trd(p + off2); u32x4 r; r[0] = a[0]; r[1] = a[1]; r[2] = b[0]; r[3] = b[1]; return r; }
template <int C> DI float dppadd(float v) { return v + __builtin_bit_cast(float, __builtin_amdgcn_mov_dpp(__builtin_bit_cast(int, v), C, 0xf, 0xf, true)); }
DI u32x4 mkdesc(const void* p, unsigned bytes) { u64 a = (u64)p; return u32x4{(unsigned)a, (unsigned)(a >> 32) & 0xffffu, bytes, 0x00020000u}; }
typedef __amdgpu_buffer_rsrc_t rsrc_t;
DI rsrc_t mkrsrc(const void* p, unsigned bytes) { return __builtin_amdgcn_make_buffer_rsrc((void*)p, 0, bytes, 0x00020000); }
DI void st16(rsrc_t r, unsigned voff, unsigned soff, u32x4 v) {
  __builtin_amdgcn_raw_buffer_store_b128(v, r, voff, soff, 16);
  asm volatile("s_nop 1" ::"v"(v) : "memory");
}
DI u32x4 ld16(rsrc_t r, unsigned voff, unsigned soff) { return __builtin_amdgcn_raw_buffer_load_b128(r, voff, soff, 16); }
#define GLDS(d, voff, sbase, simm, lbase, limm) do { unsigned t_; \
  asm volatile("s_add_i32 m0, %1, %2\n\ts_add_i32 %0, %3, %4\n\tbuffer_load_dwordx4 %5, %6, %0 offen lds" \
               : "=&s"(t_) : "s"(lbase), "i"(limm), "s"(sbase), "i"(simm), "v"(voff), "s"(d) : "memory", "scc"); } while (0)
#define GLDS1(d, voff, soff, lb) asm volatile("s_mov_b32 m0, %0\n\tbuffer_load_dword %1, %2, %3 offen lds" ::"i"(lb), "v"(voff), "s"(d), "s"(soff) : "memory")
#define WAITV(n) asm volatile("s_waitcnt vmcnt(%0)" ::"i"(n) : "memory")
#define WAITL(n) asm volatile("s_waitcnt lgkmcnt(%0)" ::"i"(n) : "memory")
#define PIN(x) asm volatile("" : "+v"(x))
#define BARX() asm volatile("s_barrier" ::: "memory")
#define SB() __builtin_amdgcn_sched_barrier(0)
#define AI __attribute__((always_inline))
#if defined(__HIP_DEVICE_COMPILE__)
#define NOPK __attribute__((target("no-packed-fp32-ops")))
#else
#define NOPK
#endif
template <int N> struct IC { static constexpr int v = N; };
template <class F, int... I> DI void sfor_(F&& f, std::integer_sequence<int, I...>) { (f(IC<I>{}), ...); }
template <int N, class F> DI void sfor(F&& f) { sfor_(f, std::make_integer_sequence<int, N>{}); }

#define BLD(op, dst, d, voff, soff, pol) asm volatile("buffer_load_" op " %0, %1, %2, %3 offen" pol : "=v"(dst) : "v"(voff), "s"(d), "s"(soff) : "memory")
DI float asf(unsigned u) { return __builtin_bit_cast(float, u); }
DI unsigned asu(float f) { return __builtin_bit_cast(unsigned, f); }
DI float swadd32(float a, float b) { auto s = __builtin_amdgcn_permlane32_swap(asu(a), asu(b), false, false); const unsigned x = s[0], y = s[1]; return asf(x) + asf(y); }
DI float swadd16(float a, float b) { auto s = __builtin_amdgcn_permlane16_swap(asu(a), asu(b), false, false); const unsigned x = s[0], y = s[1]; return asf(x) + asf(y); }
DI float half_sum(float v) {
  v = dppadd<0xB1>(v); v = dppadd<0x4E>(v); v = dppadd<0x141>(v); v = dppadd<0x140>(v);
  return swadd16(v, v);
}

constexpr int SLOT = 36864, SP = 4 * SLOT, LDSZ = 163840;
constexpr int SP_T = SP + 8192, SP_CS = SP_T + 4608, SP_RS = SP_T + 6656, SP_PF = SP_T + 7168;
constexpr unsigned WS_E = 0, WS_F1 = 1024, WS_FH = 2048, WS_FA = 3072, WS_ERR = 5120, WS_ST = 8192, WS_F0 = 24576, WS_FZ = 32768;
constexpr unsigned WS_CS = 40960;
constexpr unsigned WS_H = WS_CS + 3 * 36864;
constexpr unsigned WS_XS = WS_H + 64u * 144 * 2048;
constexpr unsigned WS_Z = WS_XS + 576u * 4096;
constexpr unsigned WS_WT = WS_Z + 576u * 4096;
constexpr unsigned WS_P = WS_WT + 289u * 8192;
constexpr unsigned WS_END = WS_P + 4096u * 5120;
constexpr int SPIN = 200000;
constexpr unsigned OOB = 0x80000000u;
DI void put_flag(rsrc_t rw, bool on, unsigned off, unsigned tag) { __builtin_amdgcn_raw_buffer_store_b32(tag, rw, on ? 0u : OOB, off, 16); }
DI void put_flag8(rsrc_t rw, unsigned t, unsigned off, unsigned tag) { __builtin_amdgcn_raw_buffer_store_b32(tag, rw, t < 8 ? t * 1024 : OOB, off, 16); }
DI bool bad4(u32x4 v, unsigned tag) { return __builtin_amdgcn_ballot_w64(v[0] != tag || v[1] != tag || v[2] != tag || v[3] != tag) != 0; }
DI void poll4(rsrc_t rw, u32x4 dws, u32x4 v, unsigned voff, unsigned soff, unsigned tag) {
  for (int it = 0; bad4(v, tag); it++) {
    if (it >= SPIN) { put_flag(rw, true, WS_ERR, 1); break; }
    __builtin_amdgcn_s_sleep(4);
    BLD("dwordx4", v, dws, voff, soff, " sc1");
    asm volatile("s_waitcnt vmcnt(0)" : "+v"(v)::"memory");
  }
}
#define DIST1 4
#define RING1 5
#define DIST2 4
#define RING2 5
#define ABL 0
#define PREF 1
#define PREFC 1
#define PREF2 103
__global__ __launch_bounds__(512, 2) NOPK void kmain(const u16* __restrict__ x, const u16* __restrict__ wr, const u16* __restrict__ wi,
                                                     const u16* __restrict__ w1, const u16* __restrict__ b1, const u16* __restrict__ w2,
                                                     const u16* __restrict__ b2, u16* __restrict__ out, char* __restrict__ ws) {
  extern __shared__ __attribute__((aligned(16))) char smem_[];
  LAS char* smem = (LAS char*)smem_;
  const int tid = threadIdx.x, lane = tid & 63;
  const int wv = __builtin_amdgcn_readfirstlane(tid >> 6);
  auto body = [&](auto SETC) AI {
  constexpr int SET = decltype(SETC)::v;
  const int id = blockIdx.x, xcd = id & 7, slot = id >> 3;
  const int rt = (xcd >> 2) * 8 + (slot >> 2), bb = xcd & 3, q = slot & 3, nt = bb * 4 + q, lid = rt * 16 + nt;
  const int aq = rt >> 1, bh = rt & 1, c = wv & 3;
  const int fl0 = (nt & 1) * 32, flh = (nt & 1) ? 0 : 256;
  const bool has_dft = nt < 9;
  const rsrc_t rw = mkrsrc(ws, WS_END);
  const u32x4 dws = mkdesc(ws, WS_END), dx = mkdesc(x, 2048 * 1152), dw1 = mkdesc(w1, 576 * 9216), dw2 = mkdesc(w2, 4608 * 1152);
  const int r = lane & 31, hh = lane >> 5;
  unsigned ev, xc;
  u32x4 xs[2];
  asm volatile("s_load_dword %0, %1, 0x0" : "=s"(ev) : "s"(ws) : "memory");
  if (SET == 1 && PREFC) {
    u64 pc; unsigned len;
    asm volatile("s_getpc_b64 %0\n.Lkpc:\n\ts_mov_b32 %1, .Lkend-.Lkpc" : "=s"(pc), "=s"(len));
    GLDS1(mkdesc((const void*)pc, len < 0x40000u ? len : 0u), lane * 128u + (c * 32 + slot) * 8192u, 0, SP_PF);
  }
  if (SET == 0) {
    const int tr = 8 * nt + 2 * c;
    const unsigned ro = (4 * aq + (tr >> 5) + 32 * (32 * bh + (tr & 31))) * 1152u;
    BLD("dwordx4", xs[0], dx, hh * 36864 + r * 16, ro, "");
    BLD("dwordx4", xs[1], dx, hh * 36864 + r * 16, ro, " offset:512");
    BLD("dword", xc, dx, hh * 36864 + r * 4, ro, " offset:1024");
  }
  const unsigned xvo = (4 * aq + (wv >> 1) + 32 * (32 * bh + 16 * (wv & 1) + (lane >> 3))) * 1152u + (((lane & 7) ^ (lane >> 4)) * 16);
  const unsigned xvo1 = (xvo ^ 64) + 8 * 32 * 1152;
  const unsigned xlb = 2 * SLOT + wv * 10240;
#pragma unroll
  for (int n = 0; n < 5; n++) { GLDS(dx, xvo, 0, 128 * n, xlb, 1024 * n); GLDS(dx, xvo1, 0, 128 * n, xlb, 5120 + 1024 * n); }
  const unsigned wvo = (8 * c + ((lane >> 2) & 7)) * 9216u + hh * 64 + (lane & 3) * 16;
  const unsigned wso = (288 * nt + fl0) * 2, wlb = c * 1024;
  const u32x4 dft = mkdesc(&FT, has_dft ? sizeof(FT) : 0);
  const u32x4 dm = c < 2 ? dw1 : dft;
  const unsigned mvo = c < 2 ? (144 * c + (lane >> 2)) * 9216u + (lane & 3) * 16 : lane * 16;
  const unsigned mso = c < 2 ? (288 * nt + flh) * 2 : nt * 36864 + (c & 1) * 9216;
  const unsigned mstep = c < 2 ? 16 * 9216 : 1024, mhf = c < 2 ? 288 * 9216 : 18432, mlb = c * 9216;
#pragma unroll
  for (int n = SET; n < 9; n += 2) GLDS(dm, mvo, mso + n * mstep, 0, mlb, 1024 * n);
  const int cu = (xcd >> 2) * 32 + slot, cg = 18 * bb + cu / 3, g3 = cu % 3;
  u32x4 uu[3];
#pragma unroll
  for (int n = 0; n < 3; n++) BLD("dwordx4", uu[n], dw1, cu < 54 ? (lane >> 3) * 9216u + (lane & 7) * 16 : OOB, (192 * g3 + 24 * wv + 8 * n) * 9216 + cg * 128, "");
  asm volatile("s_waitcnt lgkmcnt(0)" : "+s"(ev)::"memory");
  const unsigned tag = ev * 0x9E3779B1u + 0x7F4A7C15u;
  if (SET == 0) {
    asm volatile("s_waitcnt vmcnt(18)" : "+v"(xs[0]), "+v"(xs[1]), "+v"(xc)::"memory");
    float s = blo(xc) + bhi(xc);
#pragma unroll
    for (int i = 0; i < 8; i++) { const unsigned w = xs[i >> 2][i & 3]; s += blo(w) + bhi(w); }
    const float mean = half_sum(s) * (1.0f / 576.0f);
    float v = 0.f;
#pragma unroll
    for (int i = 0; i < 9; i++) { const unsigned w = i < 8 ? xs[(i >> 2) & 1][i & 3] : xc; const float d0 = blo(w) - mean, d1 = bhi(w) - mean; v = __builtin_fmaf(d0, d0, v); v = __builtin_fmaf(d1, d1, v); }
    const float var = half_sum(v) * (1.0f / 576.0f);
    __builtin_amdgcn_raw_buffer_store_b64(u32x2{pk2(mean, __builtin_sqrtf(bfr(bfr(var) + 1e-5f))), tag}, rw, r == 0 ? hh * 8u : OOB, WS_ST + (rt * 128 + 8 * nt + 2 * c) * 8, 16);
  }
  const int q4 = (lane & 15) >> 2, p4 = lane & 3, g = lane >> 4, l15 = lane & 15;
  const unsigned hoff = (r >> 4) * 1024 + hh * 256 + (r & 15) * 16;
  const unsigned hrow = WS_H + (4 * rt + c) * 144 * 2048;
  const unsigned hso = hrow + 9 * nt * 2048;
  unsigned w2o[9];
  constexpr auto ftile = [](int ks) constexpr {
    const int qq = ks < 12 ? ks / 3 : ks < 20 ? (ks - 12) / 2 : (ks - 20) / 4, e = ks < 12 ? ks % 3 : ks < 20 ? 3 + (ks & 1) : 5 + (ks & 3);
    return 9 * qq + ((qq & 1) ? e : (e == 0 ? 8 : e - 1));
  };
  auto w2off = [&](int j) AI {
    const unsigned t = ftile(4 * j) | ftile(4 * j + 1) << 8 | ftile(4 * j + 2) << 16 | ftile(4 * j + 3) << 24;
    return (36 * bb + ((t >> (8 * c)) & 255)) * 36864 + q * 288;
  };
  auto w2stage = [&](unsigned so, int j, int n) AI { GLDS(dw2, w2o[n], so, 0, mlb, (j & 3) * SLOT + 1024 * n); };
  int cnt = 0, mark[12] = {};
  constexpr int HW = 8;
  u32x4 hb[HW][2];
  int mh[36];
  auto hload = [&](int ks) AI {
    const unsigned so = hrow + (36 * bb + ftile(ks)) * 2048;
    BLD("dwordx4", hb[ks % HW][0], dws, lane * 16, so, " sc1");
    BLD("dwordx4", hb[ks % HW][1], dws, lane * 16, so, " offset:1024 sc1");
    cnt += 2; mh[ks] = cnt;
  };
  u32x4 pf;
  int mpf, mst, mx;
  const unsigned pbase = WS_P + ((rt * 4 + q) * 64 + c) * 3072;
  const u32x4 dst = mkdesc(&ST, sizeof(ST));
  const int e2 = rt * 7 + nt - 9;
  const bool has2 = nt >= 9 && e2 < 33;
  const int kc[2] = {lid, 256 + e2};
  u32x4 fa[2][3], fb[2][3], f32[2][3], xx[2][8], wt[2][2];
  auto xload = [&](int z, int i) AI {
    const bool cplx = kc[z] != 0 && kc[z] != 288;
    const unsigned vo = q4 * 256 + g * 64 + p4 * 16;
    if (i < 8) BLD("dwordx4", xx[z][i], dws, (i < 4 || cplx) ? vo : OOB, WS_XS + (kc[z] == 288 ? 1 : 2 * kc[z]) * 4096 + i * 1024, " sc1");
    else BLD("dwordx4", wt[z][i - 8], dws, lane * 16, WS_WT + kc[z] * 8192 + (c * 2 + i - 8) * 1024, " sc1");
  };
  auto tload = [&](u32x4 (&f)[2][3], unsigned so, int i) AI { BLD("dwordx4", f[i >> 1][i & 1], dst, lane * 16, so + (i >> 1) * 3072 + (i & 1) * 1024, ""); };

  {
    constexpr int D = DIST1, R = RING1;
    const unsigned tq = q4 * 64 + (g & 1) * 32 + p4 * 8;
    LAS char* const tbP = smem + hh * 1024 + tq + SET * 512;
    LAS char* const tbM = smem + hh * 512 + tq + SET * 18432;
    const unsigned xa = 2 * SLOT + (4 * c + (r >> 3)) * 5120 + (r & 7) * 128 + ((hh ^ ((r >> 1) & 7)) * 16);
    u32x4 xf[36], ring[R], pm[2], sf, tf, xq[4];
    u32x2 sg, fc, wl[8];
    u32x4 cq[3];
    unsigned bia, w2s, wvv;
    f32x16 acc[6];
    f32x4 cr[2];
    float cs[8], gt[3][8], ge[3][8], k0, k1, sdf;
    u32x4 go[3];
    LAS char* bs[11];
    int mw, mt[5];
    const f32x16 zero = {0, 0, 0, 0, 0, 0, 0, 0, 0, 0, 0, 0, 0, 0, 0, 0};
    constexpr auto isM = [](int k) constexpr { return k == 0 || k == 2; };
    constexpr auto tile = [](int k) constexpr { return k < 4 ? (k & 1) : (k >> 1); };
    constexpr auto half = [](int k) constexpr { return k < 4 ? (k >> 1) : (k & 1); };
    constexpr auto slotof = [](int k) constexpr { return k < 2 ? k : (k + 2) & 3; };
    auto fetch = [&](int F) AI { const int k = F / 18, st = F % 18; ring[F % R] = trd2(bs[k] + st * (isM(k) ? 1024 : 2048), 256); };
    auto xread = [&](int i) AI { if (i < 36) xf[i] = *(LAS u32x4*)(smem + (xa ^ ((i & 3) * 32)) + ((i >> 2) % 5) * 1024); };
    auto perm = [&](int T) AI {
      const int st = T % 18, b = 18 * half(T / 18) + (st & ~1); const unsigned sel = (st & 1) ? 0x07060302u : 0x05040100u;
      u32x4 o;
      o[0] = __builtin_amdgcn_perm(xf[b][1], xf[b][0], sel); o[1] = __builtin_amdgcn_perm(xf[b][3], xf[b][2], sel);
      o[2] = __builtin_amdgcn_perm(xf[b + 1][1], xf[b + 1][0], sel); o[3] = __builtin_amdgcn_perm(xf[b + 1][3], xf[b + 1][2], sel);
      PIN(o); pm[st & 1] = o;
    };
    auto gstep = [&](int t, int u, int s, int z) AI {
      float* T_ = gt[z]; float* E_ = ge[z]; const int i = s >> 1;
      auto v = [&](int n) AI { return acc[t][8 * u + n]; };
      if (s == 0) { T_[0] = v(0) * v(0); PIN(T_[0]); }
      if ((s & 1) == 0) {
        if (i < 8) { T_[i] = v(i) * __builtin_fmaf(T_[i], k1, k0); PIN(T_[i]); }
        if (i >= 1) { E_[i - 1] = __builtin_amdgcn_rcpf(__builtin_fmaf(E_[i - 1], sdf, sdf)); PIN(E_[i - 1]); }
      } else {
        if (i < 8) { E_[i] = __builtin_amdgcn_exp2f(T_[i]); PIN(E_[i]); }
        if (i >= 1) { T_[i - 1] = v(i - 1) * E_[i - 1]; if ((i & 1) == 0) { unsigned w_ = pk2(T_[i - 2], T_[i - 1]); PIN(w_); go[z][(i >> 1) - 1] = w_; } else PIN(T_[i - 1]); }
        if (i + 1 < 8) { T_[i + 1] = v(i + 1) * v(i + 1); PIN(T_[i + 1]); }
      }
      if (s == 17 && (ABL & 2)) { PIN(go[z]); mt[t] = cnt; } else if (s == 17) { st16(rw, hoff, hso + (t == 0 ? flh : fl0 + 64 * (t - 1) + 32 * SET) * 64 + u * 512, go[z]); mt[t] = ++cnt; }
    };
    const int gl = 36 * c + (lane < 35 ? lane : 35);
    auto gather = [&]() AI {
      BLD("dwordx2", sg, dws, r * 8, WS_ST + (rt * 128 + c * 32) * 8, " sc1");
#pragma unroll
      for (int i = 0; i < 3 * (1 - SET); i++) BLD("dwordx4", cq[i], dws, gl * 16, WS_CS + i * 36864 + nt * 2304, " sc1");
    };
    WAITV(4 - SET); BARX();
    bs[0] = tbM; PIN(bs[0]);
    xread(0); xread(1);
#pragma unroll
    for (int F = 0; F < D; F++) fetch(F);
    xread(2); xread(3);
    if (SET == 1) perm(0);
    SB();
    sfor<180>([&](auto TC_) AI {
      constexpr int T = decltype(TC_)::v, k = T / 18, st = T % 18, F = T + D, t = tile(k);
      if constexpr (T == 5) { WAITL(2); BARX(); }
      if constexpr (T == 9) { }
      if constexpr (T == 14) { }
      if constexpr (T == 19) { }
      if constexpr (T == 32) { }
      if constexpr (T == 26) { }
      if constexpr (T == 158) { }
      if constexpr (T == 159) { }
      if constexpr (T == 162) { }
      if constexpr (T == 166) { }
      if constexpr (T == 170) { }
      if constexpr (T == 171) { }
      if constexpr (T == 174) { }
      if constexpr (T == 176) { }
      if constexpr (T == 177) { }
      if constexpr (T == 65) { if (SET == 1) WAITL(4); BARX(); }
      if constexpr (F % 18 == 0) {
        constexpr int kb = F / 18;
        if (kb <= 5 || (kb & 1) == SET) WAITV(cnt - mark[kb]);
        else if (kb == 8 || kb == 10) WAITV(cnt - mt[kb / 2 - 3]);
        if (kb <= 2 || kb == 10) WAITL(0); else if (kb == 3) WAITL(2); else if (kb == 4) WAITL(4);
        if (kb == 1) { } if (kb == 2) { } BARX();
        if (kb == 7 && SET == 0) { put_flag8(rw, tid, WS_F0 + lid * 4, tag); cnt++; }
        if ((kb == 8 || kb == 10) && SET == 0) { put_flag(rw, tid == 0, (kb == 8 ? WS_FA : WS_F1) + lid * 4, tag); cnt++; }
        if constexpr (kb < 10) { bs[kb] = (isM(kb) ? tbM : tbP) + slotof(kb) * SLOT; PIN(bs[kb]); }
      }
      if constexpr (F < 180) fetch(F);
      if constexpr (SET == 1 && isM(k)) {
        const int a = (st & 1) ? 5 : 0;
        acc[a] = mfma32(pm[st & 1], ring[T % R], (k == 0 && st < 2) ? zero : acc[a]);
        if (st < 17) perm(T + 1);
      } else {
        acc[t] = mfma32(ring[T % R], xf[18 * half(k) + st], (half(k) == 0 && st == 0) ? zero : acc[t]);
      }
      if (SET == 1 && T == 35) perm(36);
      if constexpr (T < 4) { xread(4 + 4 * T); xread(5 + 4 * T); xread(6 + 4 * T); xread(7 + 4 * T); }
      if constexpr (T >= 32 && T < 38) { xread(20 + 3 * (T - 32)); xread(21 + 3 * (T - 32)); xread(22 + 3 * (T - 32)); }
      constexpr int xp = T == 5 ? 0 : T == 7 ? 1 : T == 8 ? 2 : T == 10 ? 3 : T == 11 ? 4 : T == 13 ? 5 : T == 24 ? 6 : T == 25 ? 7 : -1;
      if constexpr (xp >= 0) { GLDS(dx, (xp & 1) ? xvo1 : xvo, 0, 640 + 128 * (xp >> 1), xlb, (xp & 1) * 5120 + 1024 * (xp >> 1)); mark[2] = ++cnt; }
      if constexpr (T < 5 && 2 * T + 1 - SET < 9) { GLDS(dw1, wvo, wso, (2 * T + 1 - SET) * 32 * 9216, wlb, SLOT + 4096 * (2 * T + 1 - SET)); mark[1] = ++cnt; }
      if constexpr (F >= 18 && F < 180) {
        constexpr int kb = F / 18, fs = F % 18;
        constexpr int ks = kb <= 2 ? (fs < 9 ? kb + 1 : -1) : kb == 3 ? (fs < 9 ? 4 : 5) : (fs < 9 ? kb + 2 : -1);
        constexpr int o = (SET + ks) & 1;
        constexpr int n = ks <= 5 ? ((fs % 9) % 2 == o ? fs % 9 : 9) : (o == 0 ? fs : 9);
        if constexpr (ks >= 2 && n < 9 && !((ABL & 1) && ks >= 5 && ks < 10)) {
          if constexpr (ks == 2) GLDS(dm, mvo, mso + mhf + n * mstep, 0, mlb, 1024 * n);
          else if constexpr (ks <= 9) GLDS(dw1, wvo, wso, half(ks) * 288 * 9216 + (tile(ks) - 1) * 128 + n * 32 * 9216, wlb, slotof(ks) * SLOT + 4096 * n);
          else {
            const int e = 64 * n + lane; w2o[n] = (e / 18) * 1152 + (e % 18) * 16;
            if (n == 0) w2s = w2off(ks - 10);
            w2stage(w2s, ks - 10, n);
          }
          mark[ks] = ++cnt;
        }
      }
      if constexpr (T == 10) asm volatile("s_waitcnt vmcnt(%3)" : "+v"(uu[0]), "+v"(uu[1]), "+v"(uu[2]) : "i"(cnt + 1 - SET) : "memory");
      if constexpr (T == 10 || T == 11) {
#pragma unroll
        for (int i = 2 * (T - 10); i < 2 * (T - 10) + 2; i++) {
          cs[2 * i] = blo(uu[0][i]) + blo(uu[1][i]) + blo(uu[2][i]); cs[2 * i + 1] = bhi(uu[0][i]) + bhi(uu[1][i]) + bhi(uu[2][i]);
          PIN(cs[2 * i]); PIN(cs[2 * i + 1]);
        }
      }
      if constexpr (T == 12) {
#pragma unroll
        for (int i = 0; i < 4; i++) { cs[i] = swadd32(cs[i], cs[i + 4]); PIN(cs[i]); }
      }
      if constexpr (T == 13) {
        const float a = dppadd<0x128>(swadd16(cs[0], cs[2])), b = dppadd<0x128>(swadd16(cs[1], cs[3]));
        *(LAS float*)(smem + SP_CS + (lane & 7) * 256 + (lane >> 3) * 32 + wv * 4) = (lane & 8) ? b : a;
      }
      if constexpr (SET == 0 && T == 14) { cr[0] = *(LAS f32x4*)(smem + SP_CS + lane * 32); cr[1] = *(LAS f32x4*)(smem + SP_CS + lane * 32 + 16); }
      if constexpr (SET == 0 && T == 16) {
        const float s = ((cr[0][0] + cr[0][1]) + (cr[0][2] + cr[0][3])) + ((cr[1][0] + cr[1][1]) + (cr[1][2] + cr[1][3]));
        __builtin_amdgcn_raw_buffer_store_b64(u32x2{pk2(s, s - bfr(s)), tag}, rw, (c == 0 && cu < 54) ? lane * 8u : OOB, WS_CS + g3 * 36864 + cg * 512, 16);
        cnt++;
      }
      if constexpr (T == 41) {
        gather();
        if (SET == 0) BLD("dword", bia, mkdesc(b1, 9216), gl * 4, 576 * nt, "");
        else BLD("dwordx2", fc, mkdesc(&ST.fc, 2304), r * 8, (has_dft ? nt : 0) * 256, "");
        cnt += SET == 0 ? 5 : 2;
      }
      if constexpr (T == 48) {
        auto gwait = [&]() AI {
          if (SET == 0) asm volatile("s_waitcnt vmcnt(4)" : "+v"(sg), "+v"(cq[0]), "+v"(cq[1]), "+v"(cq[2]), "+v"(bia)::"memory");
          else asm volatile("s_waitcnt vmcnt(0)" : "+v"(sg), "+v"(fc)::"memory");
          unsigned m = sg[1] ^ tag;
          if (SET == 0) m |= (cq[0][1] ^ tag) | (cq[0][3] ^ tag) | (cq[1][1] ^ tag) | (cq[1][3] ^ tag) | (cq[2][1] ^ tag) | (cq[2][3] ^ tag);
          return __builtin_amdgcn_ballot_w64(m != 0) != 0;
        };
        for (int it = 0; gwait(); it++) {
          if (it >= SPIN) { put_flag(rw, true, WS_ERR, 2); break; }
          __builtin_amdgcn_s_sleep(4);
          gather();
          WAITV(0);
        }
        if (SET == 0) {
          *(LAS u32x4*)(smem + SP_T + gl * 32) = u32x4{cq[0][0], cq[1][0], cq[2][0], bia & 0xffffu};
          *(LAS u32x4*)(smem + SP_T + gl * 32 + 16) = u32x4{cq[0][2], cq[1][2], cq[2][2], bia >> 16};
        }
      }
      if constexpr (T == 51) {
        const unsigned nm = hh ? 0u : ((sg[0] & 0xffffu) ^ 0x8000u) * 0x10001u;
        sf = u32x4{nm, nm, nm, hh ? 0u : sg[0] >> 16};
        sdf = bhi(sg[0]);
        PIN(sf); PIN(sdf);
      }
      if constexpr (SET == 1 && T == 52) *(LAS float*)(smem + SP_RS + c * 128 + r * 4) = __builtin_amdgcn_rcpf(sdf);
      if constexpr (T == (SET == 0 ? 52 : 70)) {
        const float rstd = __builtin_amdgcn_rcpf(sdf), c0 = -2.0f * 0.7978845608028654f * 1.4426950408889634f, c1 = c0 * 0.044715f;
        k0 = c0 * rstd; k1 = c1 * rstd * rstd * rstd;
        PIN(k0); PIN(k1);
      }
      if (SET == 0 && T == 51) tf = *(LAS u32x4*)(smem + SP_T + (flh + r) * 16);
      if (SET == 0 && T == 53) acc[0] = mfma32(tf, sf, acc[0]);
      if constexpr (!isM(k)) {
        if (half(k) == 1 && st == 15) tf = *(LAS u32x4*)(smem + SP_T + (fl0 + 64 * (t - 1) + 32 * SET + r) * 16);
        if (half(k) == 1 && st == 17) acc[t] = mfma32(tf, sf, acc[t]);
      } else if constexpr (SET == 1 && k == 2 && st == 17) {
        acc[0] = mfma32(sf, u32x4{fc[0], 0, 0, 0}, acc[0]);
        acc[5] = mfma32(sf, u32x4{fc[1], 0, 0, 0}, acc[5]);
      }
      constexpr int TW = 26;
      if constexpr (!(ABL & 16) && SET == 0 && ((T >= TW && T < TW + 4) || (T >= TW + 17 && T < TW + 21))) {
        const int wset = 8 * (lid >> 2) + (lane & 7), wp = 5 * c + (lane >> 3), wcol = 72 * q + (q == 3) + 4 * wp;
        const bool on = lane < 40 && wp < 19 && wcol <= 285;
        if constexpr (T < TW + 8) {
          constexpr int i = T - TW;
          if (i == 0) { wvv = ((16 * (wset >> 7) + (wset & 15) + 256 * ((wset >> 4) & 7)) * 289 + wcol) * 2u; PIN(wvv); wvv = on ? wvv : OOB; }
          BLD("dwordx2", wl[2 * i], mkdesc(wr, 2048 * 578), wvv, i * 64 * 578, "");
          BLD("dwordx2", wl[2 * i + 1], mkdesc(wi, 2048 * 578), wvv, i * 64 * 578, "");
          cnt += 2; mw = cnt;
        } else {
          constexpr int j = T - TW - 17;
          if (j == 0) {
            asm volatile("s_waitcnt vmcnt(%8)" : "+v"(wl[0]), "+v"(wl[1]), "+v"(wl[2]), "+v"(wl[3]), "+v"(wl[4]), "+v"(wl[5]), "+v"(wl[6]), "+v"(wl[7]) : "i"(cnt - mw) : "memory");
            wvv = wcol * 8192u + wset * 16; PIN(wvv); wvv = on ? wvv : OOB;
          }
          u32x4 o;
#pragma unroll
          for (int i = 0; i < 4; i++) o[i] = __builtin_amdgcn_perm(wl[2 * i + 1][j >> 1], wl[2 * i][j >> 1], (j & 1) ? 0x07060302u : 0x05040100u);
          st16(rw, wvv, WS_WT + j * 8192, o);
          mw = ++cnt;
        }
      }
      if constexpr (SET == 0) {
        if constexpr (T == 161) { BLD("dwordx4", pf, dws, 0, WS_FA + (lid >> 2) * 16, " sc1"); mpf = ++cnt; }
        if constexpr (T == 168) {
          asm volatile("s_waitcnt vmcnt(%1)" : "+v"(pf) : "i"(cnt - mpf) : "memory");
          poll4(rw, dws, pf, 0, WS_FA + (lid >> 2) * 16, tag);
        }
        if constexpr (T >= 168 && (T - 168) % 3 == 0) hload((T - 168) / 3);
      }
      if constexpr (SET == 1) {
        if constexpr (T >= 168 && T < 176) { tload(T < 172 ? fa : f32, T < 172 ? c * 6144 : 49152, T & 3); cnt++; }
      }
      if constexpr (PREF && (SET == 0 ? (T == 77 || T == 113) : T == 107)) {
        const int u = wv * 32 + (rt & 7) * 4 + q;
        const u32x4 dp = u < 162 ? dw2 : u < 185 ? mkdesc(&GT, sizeof(GT)) : dst;
        GLDS1(dp, (c >> 1) == (T == 113) ? lane * 128u : OOB, u < 162 ? bb * 1327104 + u * 8192 : u < 185 ? bb * 184320 + (u - 162) * 8192 : (u - 185) * 8192, SP_PF);
        cnt++;
      }
      if constexpr (SET == 0) {
        if constexpr (T >= 55 && T < 73) gstep(0, 0, T - 55, 2);
        if constexpr (T >= 73 && T < 109 && ((T - 73) & 1) == 0) gstep(0, 1, (T - 73) >> 1, 2);
      } else if constexpr (T >= 55 && T < 61) {
        constexpr int u = (T - 55) / 3, ph = (T - 55) % 3;
        const int bc = 32 * nt + r;
        const float b1c = bc == 144 ? 0.f : 1.f, b2c = bc < 144 ? -1.f : 1.f, a2c = -b1c * b2c;
        if (ph == 0) { cr[0] = *(LAS f32x4*)(smem + SP_RS + c * 128 + u * 64 + hh * 16); cr[1] = *(LAS f32x4*)(smem + SP_RS + c * 128 + u * 64 + 32 + hh * 16); }
        else {
          float o1[4], o2[4];
#pragma unroll
          for (int i = 0; i < 4; i++) {
            const float e = acc[0][8 * u + 4 * (ph - 1) + i] * cr[ph - 1][i], o = acc[5][8 * u + 4 * (ph - 1) + i] * cr[ph - 1][i];
            o1[i] = __builtin_fmaf(b1c, o, e); o2[i] = __builtin_fmaf(a2c, e, b2c * o);
          }
          LAS char* p = smem + r * 272 + (2 * u + hh) * 64 + c * 16 + (ph - 1) * 8;
          *(LAS u32x2*)p = u32x2{pk2(o1[0], o1[1]), pk2(o1[2], o1[3])};
          *(LAS u32x2*)(p + 32 * 272) = u32x2{pk2(o2[0], o2[1]), pk2(o2[2], o2[3])};
        }
      } else if constexpr (T == 65) {
#pragma unroll
        for (int i = 0; i < 4; i++) xq[i] = *(LAS u32x4*)(smem + (16 * c + 4 * i + g) * 272 + l15 * 16);
      } else if constexpr (T >= 67 && T < 71) {
        const int bc = 32 * nt + 16 * (c & 1) + 4 * (T - 67) + g;
        const int p1 = 2 * bc - (bc > 144 ? 287 : 0);
        int p2 = (bc < 144 ? 576 : 865) - 2 * bc; p2 = bc == 0 ? 1 : p2; p2 = bc == 144 ? 289 : p2;
        unsigned vo = (c < 2 ? p1 : p2) * 4096u + l15 * 16; PIN(vo);
        st16(rw, (has_dft && !(ABL & 32)) ? vo : OOB, WS_XS + bh * 2048 + aq * 256, xq[T - 67]);
        cnt++;
      }
      if constexpr (T >= 73 && (ABL & 4)) { if constexpr ((T - 73) % 36 == 0) PIN(acc[(T - 73) / 36 + 1]); } else if constexpr (T >= 73) {
        const int pt = (T - 73) / 36 + 1, s = (T - 73) % 36;
        if (s < 18) gstep(pt, 0, s, 0);
        if (s >= 16 && s < 34) gstep(pt, 1, s - 16, 1);
      }
      SB();
    });
#pragma unroll
    for (int s = 0; s < 18; s++) {
      if (s % 4 == 1 && s < 16 && SET == 0) hload(4 + s / 4);
      if (s == 6 && SET == 1) { BLD("dwordx4", pf, dws, lane * 16, WS_F0 + xcd * 1024, " sc1"); mpf = ++cnt; }
      gstep(4, 0, s, 0); gstep(4, 1, s, 1);
    }
    mst = cnt;
  }
  if constexpr (SET == 0) {
    constexpr int D = DIST2, R = RING2;
    const unsigned tb = (16 * (g >> 1) + 4 * (g & 1) + q4) * 288 + p4 * 8;
    const unsigned zb = 2 * SLOT + (8 * (g >> 1) + 2 * (g & 1) + (q4 >> 1)) * 512 + (2 * (p4 >> 1) + (q4 & 1)) * 64 + ((c ^ (2 * (g & 1) + (q4 >> 1))) * 16) + (p4 & 1) * 8;
    u32x2 ra[R], rb[R], xv[5], bv[5];
    f32x4 acc[9][2];
    LAS char* bs[11];
    const f32x4 z4 = {0, 0, 0, 0};
    auto fad = [&](int F) AI { return bs[F / 36] + ((F % 36) / 9) * 9216 + (F % 9) * 32; };
    bs[0] = smem + tb; PIN(bs[0]);
#pragma unroll
    for (int F = 0; F < D; F++) { ra[F] = trd(fad(F)); rb[F] = trd(fad(F) + 2304); }
    const unsigned orow = (4 * aq + c + 32 * (32 * bh + 16 * (bb >> 1) + l15)) * 1152u + g * 8, ocol = 288 * q + 160 * (bb & 1);
    SB();
    sfor<369>([&](auto GC_) AI {
      constexpr int G = decltype(GC_)::v, ks = G / 9, dt = G % 9, F = G + D;
      if constexpr (F % 36 == 0) {
        constexpr int j = F / 36;
        if (j == 1) { WAITV(cnt - mst); }
        if (j < 10) { BARX(); }
        if (j == 1) { put_flag(rw, tid == 0, WS_FH + lid * 4, tag); cnt++; }
        bs[j] = smem + (j < 10 ? (j & 3) * SLOT : SP) + tb; PIN(bs[j]);
      }
      if constexpr (dt == 0 && ks < 36) asm volatile("s_waitcnt vmcnt(%2)" : "+v"(hb[ks % HW][0]), "+v"(hb[ks % HW][1]) : "i"(cnt - mh[ks]) : "memory");
      const u32x4 a = {ra[G % R][0], ra[G % R][1], rb[G % R][0], rb[G % R][1]};
      acc[dt][0] = mfma16(a, hb[ks % HW][0], ks == 0 ? z4 : acc[dt][0]);
      SB();
      if constexpr (F < 369) ra[F % R] = trd(fad(F));
      SB();
      acc[dt][1] = mfma16(a, hb[ks % HW][1], ks == 0 ? z4 : acc[dt][1]);
      SB();
      if constexpr (F < 369) rb[F % R] = trd(fad(F) + 2304);
      if constexpr (G == 9 * (10 - HW) + 7 || G == 9 * (18 - HW) + 7) { BLD("dwordx4", pf, dws, 0, (ks + HW < 12 ? WS_F1 : WS_FH) + (lid >> 2) * 16, " sc1"); mpf = ++cnt; }
      if constexpr (dt == 8 && ks + HW < 36) {
        if (ks + HW == 12 || ks + HW == 20) {
          asm volatile("s_waitcnt vmcnt(%1)" : "+v"(pf) : "i"(cnt - mpf) : "memory");
          poll4(rw, dws, pf, 0, (ks + HW == 12 ? WS_F1 : WS_FH) + (lid >> 2) * 16, tag);
        }
        hload(ks + HW);
      }
      if constexpr (ks >= 35 && ks < 40 && (dt == 5 || dt == 6)) {
        constexpr int sp = ks - 35;
        hb[(ks + 1) % HW][dt - 5] = trd2(smem + zb + (dt - 5) * 256 + sp * 8192 - (sp == 4 ? (g >> 1) * 4096 : 0), 2048);
      }
      if constexpr (G >= 270 && G < 320 && G % 5 == 0) {
        constexpr int i = (G - 270) / 10;
        const bool on = i < 4 || (bb & 1) == 0;
        if (G % 10 == 0) BLD("dwordx2", xv[i], dx, on ? orow : OOB, ocol + 32 * i, "");
        else BLD("dwordx2", bv[i], mkdesc(b2, 1152), on ? g * 8 : OOB, ocol + 32 * i, "");
        cnt++;
      }
      SB();
    });
    auto pk4 = [&](f32x4 a, f32x4 b) AI { return u32x4{pk2(a[0], a[1]), pk2(a[2], a[3]), pk2(b[0], b[1]), pk2(b[2], b[3])}; };
#pragma unroll
    for (int d = 0; d < 4; d++)
#pragma unroll
      for (int i = 0; i < 3 - (d & 1); i++) {
        const int t0 = 5 * (d & 1) + 2 * i;
        st16(rw, d == bb ? OOB : lane * 16 + i * 1024, pbase + (d * 16 + bb * 4) * 3072, pk4(acc[t0][d >> 1], acc[i < 2 ? t0 + 1 : t0][d >> 1]));
      }
    u32x4 pp[3][3];
    auto pload = [&]() AI {
#pragma unroll
      for (int s = 0; s < 3; s++)
#pragma unroll
        for (int i = 0; i < 3; i++) BLD("dwordx4", pp[s][i], dws, (i < 2 || (bb & 1) == 0) ? lane * 16 + i * 1024 : OOB, pbase + (bb * 16 + ((bb + 1 + s) & 3) * 4) * 3072, " sc1");
    };
    asm volatile("s_waitcnt vmcnt(10)" : "+v"(xv[0]), "+v"(xv[1]), "+v"(xv[2]), "+v"(xv[3]), "+v"(xv[4]), "+v"(bv[0]), "+v"(bv[1]), "+v"(bv[2]), "+v"(bv[3]), "+v"(bv[4])::"memory");
    f32x4 own[5];
#pragma unroll
    for (int i = 0; i < 5; i++) {
      const int i2 = i < 4 ? 5 + i : 8;
#pragma unroll
      for (int e = 0; e < 4; e++) {
        const float lo = (bb & 2) ? acc[i][1][e] : acc[i][0][e], hi = (bb & 2) ? acc[i2][1][e] : acc[i2][0][e];
        const unsigned xw = xv[i][e >> 1], bw = bv[i][e >> 1];
        own[i][e] = ((bb & 1) ? hi : lo) + ((e & 1) ? bhi(xw) + bhi(bw) : blo(xw) + blo(bw));
      }
      PIN(own[i]);
    }
    for (int it = 0;; it++) {
      pload();
      asm volatile("s_waitcnt vmcnt(0)" : "+v"(pp[0][0]), "+v"(pp[0][1]), "+v"(pp[0][2]), "+v"(pp[1][0]), "+v"(pp[1][1]), "+v"(pp[1][2]), "+v"(pp[2][0]), "+v"(pp[2][1]), "+v"(pp[2][2])::"memory");
      unsigned m = 0;
#pragma unroll
      for (int i = 0; i < 36; i++) { const unsigned w = pp[i / 12][(i >> 2) % 3][i & 3]; m = m > w ? m : w; }
      if (__builtin_amdgcn_ballot_w64(m == 0xffffffffu) == 0) break;
      if (it >= SPIN / 16) { put_flag(rw, true, WS_ERR, 4); break; }
    }
    const rsrc_t ro = mkrsrc(out, 2048 * 1152);
#pragma unroll
    for (int i = 0; i < 5; i++) {
      float v[4];
#pragma unroll
      for (int e = 0; e < 4; e++) {
        const unsigned w0 = pp[0][i >> 1][2 * (i & 1) + (e >> 1)], w1 = pp[1][i >> 1][2 * (i & 1) + (e >> 1)], w2_ = pp[2][i >> 1][2 * (i & 1) + (e >> 1)];
        v[e] = (e & 1) ? (own[i][e] + bhi(w0)) + (bhi(w1) + bhi(w2_)) : (own[i][e] + blo(w0)) + (blo(w1) + blo(w2_));
      }
      __builtin_amdgcn_raw_buffer_store_b64(u32x2{pk2(v[0], v[1]), pk2(v[2], v[3])}, ro, (i < 4 || (bb & 1) == 0) ? orow : OOB, ocol + 32 * i, 0);
    }
  } else {
    auto stage = [&](int j) AI {
      const unsigned so = w2off(j);
#pragma unroll
      for (int n = 0; n < 9; n++) w2stage(so, j, n);
      cnt += 9; mark[j] = cnt;
    };
    auto bar = [&](int j, int m) AI { WAITV(cnt - m); WAITL(0); BARX(); };
    const u32x4 dg = mkdesc(&GT, sizeof(GT));
    f32x4 t1[4], t2[4];
    const f32x4 z4 = {0, 0, 0, 0};
    auto xpin = [&](int z) AI {
#pragma unroll
      for (int i = 0; i < 8; i++) PIN(xx[z][i]);
      PIN(wt[z][0]); PIN(wt[z][1]);
    };
    auto tpin = [&](u32x4 (&f)[2][3]) AI {
#pragma unroll
      for (int t = 0; t < 2; t++) { PIN(f[t][0]); PIN(f[t][1]); f[t][2] = f[t][1] ^ 0x80008000u; }
    };
    asm volatile("s_waitcnt vmcnt(%1)" : "+v"(pf) : "i"(cnt - mpf) : "memory");
    poll4(rw, dws, pf, lane * 16, WS_F0 + xcd * 1024, tag);
#pragma unroll
    for (int i = 0; i < 10; i++) xload(0, i);
    cnt += 10; mx = cnt;
    stage(2);
    {
      const float f1 = (16 * c + l15) * (1.0f / 2048.0f), f2 = (16 * c + 4 * g) * (1.0f / 2048.0f);
#pragma unroll
      for (int at = 0; at < 2; at++)
#pragma unroll
        for (int i = 0; i < 4; i++) {
          const float a1 = (float)(16 * at + 4 * g + i) * f1, a2 = (float)(16 * at + l15) * (f2 + i * (1.0f / 2048.0f));
          t1[2 * at][i] = __builtin_amdgcn_cosf(a1); t1[2 * at + 1][i] = __builtin_amdgcn_sinf(a1);
          t2[2 * at][i] = __builtin_amdgcn_cosf(a2) * (1.0f / 2048.0f); t2[2 * at + 1][i] = __builtin_amdgcn_sinf(a2) * (1.0f / 2048.0f);
        }
#pragma unroll
      for (int i = 0; i < 4; i++) { PIN(t1[i]); PIN(t2[i]); }
    }
    bar(1, mst);
    if (has2) {
#pragma unroll
      for (int i = 0; i < 10; i++) xload(1, i);
    }
#pragma unroll
    for (int i = 0; i < 4; i++) tload(fb, 24576 + c * 6144, i);
    cnt += 4;
    const int mb = cnt;
    stage(3);
    WAITV(cnt - mx);
    tpin(fa); tpin(f32); xpin(0);
    auto front = [&](int z) AI {
      u32x4 br, bi, yr, yi;
#pragma unroll
      for (int at = 0; at < 2; at++) {
        f32x4 tr = z4, ti = z4;
#pragma unroll
        for (int t = 0; t < 2; t++) {
          const u32x4 xr = xx[z][2 * t + at], xi = xx[z][4 + 2 * t + at];
          tr = mfma16(xr, fa[t][0], tr); ti = mfma16(xi, fa[t][0], ti);
          tr = mfma16(xi, fa[t][1], tr); ti = mfma16(xr, fa[t][2], ti);
        }
        const f32x4 co = t1[2 * at], si = t1[2 * at + 1];
        const f32x4 ur = tr * co + ti * si, ui = ti * co - tr * si;
        br[2 * at] = pk2(ur[0], ur[1]); br[2 * at + 1] = pk2(ur[2], ur[3]);
        bi[2 * at] = pk2(ui[0], ui[1]); bi[2 * at + 1] = pk2(ui[2], ui[3]);
      }
#pragma unroll
      for (int ct = 0; ct < 2; ct++) {
        f32x4 xr = mfma16(f32[ct][0], br, z4), xi = mfma16(f32[ct][0], bi, z4);
        xr = mfma16(f32[ct][1], bi, xr); xi = mfma16(f32[ct][2], br, xi);
        float vr[4], vi[4];
#pragma unroll
        for (int i = 0; i < 4; i++) { const unsigned w = wt[z][ct][i]; const float a = blo(w), b = bhi(w); vr[i] = xr[i] * a - xi[i] * b; vi[i] = xr[i] * b + xi[i] * a; }
        yr[2 * ct] = pk2(vr[0], vr[1]); yr[2 * ct + 1] = pk2(vr[2], vr[3]);
        yi[2 * ct] = pk2(vi[0], vi[1]); yi[2 * ct + 1] = pk2(vi[2], vi[3]);
      }
#pragma unroll
      for (int at = 0; at < 2; at++) {
        f32x4 pr = mfma16(yr, f32[at][0], z4), pi = mfma16(yi, f32[at][0], z4);
        pr = mfma16(yi, f32[at][2], pr); pi = mfma16(yr, f32[at][1], pi);
        const f32x4 co = t2[2 * at], si = t2[2 * at + 1];
        const f32x4 ur = pr * co - pi * si, ui = pi * co + pr * si;
        LAS char* p = smem + SP + z * 8192 + (at * 4 + (c >> 1)) * 1024 + lane * 16 + (c & 1) * 8;
        *(LAS u32x2*)p = u32x2{pk2(ur[0], ur[1]), pk2(ur[2], ur[3])};
        *(LAS u32x2*)(p + 2048) = u32x2{pk2(ui[0], ui[1]), pk2(ui[2], ui[3])};
      }
    };
    front(0);
    bar(2, mb);
    tpin(fb);
    stage(4);
    auto back = [&](int z) AI {
      const int pair = kc[z] == 288 ? 0 : kc[z];
      const bool on = kc[z] == 0 ? (g & 1) == 0 : (kc[z] == 288 ? (g & 1) == 1 : true);
#pragma unroll
      for (int at = 0; at < 2; at++) {
        f32x4 zr = z4, zi = z4;
#pragma unroll
        for (int t = 0; t < 2; t++) {
          LAS char* p = smem + SP + z * 8192 + (at * 4 + t) * 1024 + lane * 16;
          const u32x4 pr = *(LAS u32x4*)p, pi = *(LAS u32x4*)(p + 2048);
          zr = mfma16(fb[t][0], pr, zr); zi = mfma16(fb[t][0], pi, zi);
          zr = mfma16(fb[t][2], pi, zr); zi = mfma16(fb[t][1], pr, zi);
        }
        const unsigned r0 = pk2(zr[0], zr[1]), r1 = pk2(zr[2], zr[3]);
        const unsigned i0 = kc[z] == 288 ? r0 : pk2(zi[0], zi[1]), i1 = kc[z] == 288 ? r1 : pk2(zi[2], zi[3]);
        const auto s0 = __builtin_amdgcn_permlane16_swap(r0, i0, false, false), s1 = __builtin_amdgcn_permlane16_swap(r1, i1, false, false);
        const unsigned a0 = s0[0], a1 = s1[0], a2 = s0[1], a3 = s1[1];
        st16(rw, on ? q4 * 1024u + g * 64 + ((p4 ^ (pair & 3)) * 16) : OOB, WS_Z + pair * 8192 + at * 4096 + c * 256, u32x4{a0, a1, a2, a3});
      }
    };
    back(0);
    cnt += 2;
    if (has2) { xpin(1); front(1); }
    bar(3, mark[3]);
    if (has2) back(1);
    stage(5);
#pragma unroll
    for (int d = 0; d < 4; d++)
#pragma unroll
      for (int i = 0; i < 3 - (d & 1); i++) st16(rw, d == bb ? OOB : lane * 16 + i * 1024, pbase + (d * 16 + bb * 4) * 3072, u32x4{~0u, ~0u, ~0u, ~0u});
    cnt += 10;
    const int mz = cnt;
    bar(4, mark[4]);
    stage(6);
#pragma unroll
    for (int n = 0; n < 3; n++) { const int pc = n < 2 ? c + 4 * n : 8; GLDS(dg, lane * 16, (nt * 5 + 4) * 9216 + pc * 1024, 0, SP + pc * 1024, 0); }
    cnt += 3;
    bar(5, mz);
    put_flag8(rw, tid - 256, WS_FZ + lid * 4, tag); cnt++;
    stage(7);
    bar(6, mark[6]);
    stage(8);
    bar(7, mark[7]);
#pragma unroll
    for (int n = 0; n < 9; n++) GLDS(dg, lane * 16, (nt * 5 + c) * 9216, 1024 * n, mlb, SLOT + 1024 * n);
    __builtin_amdgcn_s_sleep(4);
    BLD("dwordx4", pf, dws, c == 0 ? lane * 16 : OOB, WS_FZ + xcd * 1024, " sc1");
    asm volatile("s_waitcnt vmcnt(0)" : "+v"(pf)::"memory");
    if (c == 0) poll4(rw, dws, pf, lane * 16, WS_FZ + xcd * 1024, tag);
    bar(8, cnt);
    u32x4 zz[9];
#pragma unroll
    for (int n = 0; n < 9; n++) BLD("dwordx4", zz[n], dws, hh * 8192 + r * 16, WS_Z + (72 * bb + 2 * c + 8 * n) * 8192 + rt * 512, " sc1");
    put_flag(rw, lid == 0 && tid == 256, WS_E, tag);
    WAITV(1);
#pragma unroll
    for (int n = 0; n < 9; n++) { PIN(zz[n]); *(LAS u32x4*)(smem + 2 * SLOT + (c + 4 * n) * 1024 + lane * 16) = zz[n]; }
    bar(9, cnt - 1);
  }
  if (SET == 0) asm volatile(".Lkend:");
  };
  if (wv < 4) body(IC<0>{}); else body(IC<1>{});
}

extern "C" void kernel_launch(void* const* d_in, const int* in_sizes, int n_in, void* d_out, int out_size, void* d_ws, size_t ws_size, hipStream_t stream) {
  (void)hipFuncSetAttribute((const void*)kmain, hipFuncAttributeMaxDynamicSharedMemorySize, LDSZ);
  kmain<<<256, 512, LDSZ, stream>>>((const u16*)d_in[0], (const u16*)d_in[1], (const u16*)d_in[2], (const u16*)d_in[4], (const u16*)d_in[5],
                                    (const u16*)d_in[6], (const u16*)d_in[7], (u16*)d_out, (char*)d_ws);
}
```

```cpp
#include <hip/hip_runtime.h>
#include <hip/hip_bf16.h>
#include <utility>

typedef unsigned short u16;
typedef __attribute__((ext_vector_type(8))) __bf16 bf16x8;
typedef __attribute__((ext_vector_type(2))) __bf16 bf16x2;
typedef __attribute__((ext_vector_type(4))) short s16x4;
typedef __attribute__((ext_vector_type(8))) short s16x8;
typedef __attribute__((ext_vector_type(4))) float f32x4;
typedef __attribute__((ext_vector_type(16))) float f32x16;
typedef __attribute__((ext_vector_type(4))) unsigned u32x4;
typedef __attribute__((ext_vector_type(2))) unsigned u32x2;
#define LAS __attribute__((address_space(3)))
#define GAS __attribute__((address_space(1)))
#define DI __device__ __forceinline__

namespace tab {
constexpr double PI_ = 3.14159265358979323846;
constexpr double tsin(double x) { double t = x, s = x; for (int i = 1; i < 12; i++) { t *= -x * x / ((2 * i) * (2 * i + 1)); s += t; } return s; }
constexpr double tcos(double x) { double t = 1, s = 1; for (int i = 1; i < 12; i++) { t *= -x * x / ((2 * i - 1) * (2 * i)); s += t; } return s; }
constexpr double ccos(int n, int N) {
  n %= N; if (n < 0) n += N;
  if (2 * n > N) n = N - n;
  bool neg = false;
  if (4 * n > N) { n = N / 2 - n; neg = true; }
  double v = (8 * n > N) ? tsin(2 * PI_ * (N / 4 - n) / N) : tcos(2 * PI_ * n / N);
  return neg ? -v : v;
}
constexpr double csin(int n, int N) { return ccos(n - N / 4, N); }
constexpr u16 f2bf(double d) { float f = (float)d; unsigned u = __builtin_bit_cast(unsigned, f); u += 0x7fffu + ((u >> 16) & 1u); return (u16)(u >> 16); }
struct C576 { u16 c[576]; u16 g1[576]; u16 g2[576]; };
constexpr C576 mk576() { C576 r{}; for (int i = 0; i < 576; i++) { double c = ccos(i, 576); r.c[i] = f2bf(c); r.g1[i] = f2bf(c / 576.0); r.g2[i] = f2bf(2.0 * c / 576.0); } return r; }
constexpr C576 c576 = mk576();
struct Blk { u16 v[9216]; };
constexpr int fidx(int bc, int d) {
  int n = bc < 144 ? bc * d : (bc == 144 ? ((d & 1) == 0 ? 144 * d : 144 * d + 144) : (bc - 144) * d + 144);
  return n % 576;
}
constexpr Blk mkF(int idx) {
  Blk r{}; int at = idx >> 1, hf = idx & 1;
  for (int st = 0; st < 18; st++) for (int hh = 0; hh < 2; hh++) for (int j = 0; j < 8; j++) {
    int d = 288 * hf + 32 * (st >> 1) + 16 * (j >> 2) + 8 * hh + 2 * (j & 3) + (st & 1);
    for (int cc = 0; cc < 32; cc++) r.v[((st * 2 + hh) * 8 + j) * 32 + cc] = c576.c[fidx(32 * at + cc, d)];
  }
  return r;
}
template <int I> constexpr Blk fblk = mkF(I);
struct FTab { Blk b[18]; };
template <int... I> constexpr FTab mkFT(std::integer_sequence<int, I...>) { return FTab{{fblk<I>...}}; }
constexpr double bf2d(u16 b) { return (double)__builtin_bit_cast(float, (unsigned)b << 16); }
struct FCB { unsigned v[64]; };
constexpr FCB mkFC(int at) {
  FCB r{};
  for (int e = 0; e < 2; e++) for (int cc = 0; cc < 32; cc++) {
    double s = 0;
    for (int d = e; d < 576; d += 2) s += bf2d(c576.c[fidx(32 * at + cc, d)]);
    u16 hi = f2bf(s), lo = f2bf(s - bf2d(hi));
    r.v[cc * 2 + e] = (unsigned)hi | ((unsigned)lo << 16);
  }
  return r;
}
template <int I> constexpr FCB fcb = mkFC(I);
struct FCT { FCB b[9]; };
template <int... I> constexpr FCT mkFCT(std::integer_sequence<int, I...>) { return FCT{{fcb<I>...}}; }
struct GBlk { u16 v[4608]; };
constexpr GBlk mkG(int idx) {
  GBlk r{}; int step = idx % 5, q = (idx / 5) & 3, b = idx / 20;
  for (int row = 0; row < 32; row++) {
    int rho = 32 * step + row;
    if (rho >= 144) continue;
    int kc = 72 * b + (rho >> 1), ri = rho & 1;
    for (int c = 0; c < 144; c++) {
      int d = 144 * q + c;
      r.v[row * 144 + c] = kc == 0 ? c576.g1[(ri * 288 * d) % 576] : c576.g2[(kc * d + ri * 144) % 576];
    }
  }
  return r;
}
template <int I> constexpr GBlk gblk = mkG(I);
struct GTab { GBlk b[80]; };
template <int... I> constexpr GTab mkGT(std::integer_sequence<int, I...>) { return GTab{{gblk<I>...}}; }
struct C2048 { float c[2048]; };
constexpr C2048 mk2048() { C2048 r{}; for (int i = 0; i < 2048; i++) r.c[i] = (float)ccos(i, 2048); return r; }
constexpr C2048 c2048 = mk2048();
constexpr u16 trig(int n2048, int type) {
  int n = type == 0 ? n2048 : (type == 1 ? n2048 + 1536 : n2048 + 512);
  return f2bf(c2048.c[n & 2047]);
}
struct F64T { u16 v[4][2][3][64][8]; };
constexpr F64T mkF64(int which) {
  F64T r{};
  for (int j = 0; j < 4; j++) for (int t = 0; t < 2; t++) for (int ty = 0; ty < 3; ty++) for (int l = 0; l < 64; l++) for (int jj = 0; jj < 8; jj++) {
    int g = l >> 4, o = 16 * j + (l & 15);
    int k = which == 0 ? 32 * t + 16 * (g >> 1) + 8 * (jj >> 2) + 4 * (g & 1) + (jj & 3) : 32 * t + 16 * (jj >> 2) + 4 * g + (jj & 3);
    r.v[j][t][ty][l][jj] = trig(o * k * 32, ty);
  }
  return r;
}
struct F32T { u16 v[2][3][64][8]; };
constexpr F32T mkF32() {
  F32T r{};
  for (int ct = 0; ct < 2; ct++) for (int ty = 0; ty < 3; ty++) for (int l = 0; l < 64; l++) for (int jj = 0; jj < 8; jj++) {
    int g = l >> 4, o = 16 * ct + (l & 15), k = 16 * (jj >> 2) + 4 * g + (jj & 3);
    r.v[ct][ty][l][jj] = trig(o * k * 64, ty);
  }
  return r;
}
struct TWT { float v[4][4][64][4]; };
constexpr TWT mkTW(int which) {
  TWT r{};
  for (int j = 0; j < 4; j++) for (int n = 0; n < 4; n++) for (int l = 0; l < 64; l++) for (int i = 0; i < 4; i++) {
    int g = l >> 4, at = n >> 1;
    int a = which == 0 ? 16 * at + 4 * g + i : 16 * at + (l & 15);
    int d = which == 0 ? 16 * j + (l & 15) : 16 * j + 4 * g + i;
    float v = c2048.c[(a * d + ((n & 1) ? 1536 : 0)) & 2047];
    r.v[j][n][l][i] = which == 0 ? v : v * (1.0f / 2048.0f);
  }
  return r;
}
constexpr F64T f64a = mkF64(0), f64b = mkF64(1);
constexpr F32T f32t = mkF32();
constexpr TWT tw1 = mkTW(0), tw2 = mkTW(1);
struct Small { F64T a, b; F32T f; TWT t1, t2; FCT fc; };
}
#if defined(__HIP_DEVICE_COMPILE__)
__device__ __attribute__((aligned(128))) static const tab::FTab FT = tab::mkFT(std::make_integer_sequence<int, 18>{});
__device__ __attribute__((aligned(128))) static const tab::GTab GT = tab::mkGT(std::make_integer_sequence<int, 80>{});
__device__ __attribute__((aligned(128))) static const tab::Small ST = {tab::f64a, tab::f64b, tab::f32t, tab::tw1, tab::tw2, tab::mkFCT(std::make_integer_sequence<int, 9>{})};
#else
__device__ static const tab::FTab FT = {};
__device__ static const tab::GTab GT = {};
__device__ static const tab::Small ST = {};
#endif
typedef unsigned long long u64;
DI float bfr(float x) { return (float)(__bf16)x; }
DI unsigned pk2(float a, float b) { bf16x2 c; c[0] = (__bf16)a; c[1] = (__bf16)b; return __builtin_bit_cast(unsigned, c); }
DI float blo(unsigned u) { return __builtin_bit_cast(float, u << 16); }
DI float bhi(unsigned u) { return __builtin_bit_cast(float, u & 0xffff0000u); }
DI f32x16 mfma32(u32x4 a, u32x4 b, f32x16 c) { return __builtin_amdgcn_mfma_f32_32x32x16_bf16(__builtin_bit_cast(bf16x8, a), __builtin_bit_cast(bf16x8, b), c, 0, 0, 0); }
DI f32x4 mfma16(u32x4 a, u32x4 b, f32x4 c) { return __builtin_amdgcn_mfma_f32_16x16x32_bf16(__builtin_bit_cast(bf16x8, a), __builtin_bit_cast(bf16x8, b), c, 0, 0, 0); }
DI u32x2 trd(LAS char* p) { return __builtin_bit_cast(u32x2, __builtin_amdgcn_ds_read_tr16_b64_v4i16((LAS s16x4*)p)); }
DI u32x4 trd2(LAS char* p, int off2) { u32x2 a = trd(p), b = trd(p + off2); u32x4 r; r[0] = a[0]; r[1] = a[1]; r[2] = b[0]; r[3] = b[1]; return r; }
template <int C> DI float dppadd(float v) { return v + __builtin_bit_cast(float, __builtin_amdgcn_mov_dpp(__builtin_bit_cast(int, v), C, 0xf, 0xf, true)); }
DI u32x4 mkdesc(const void* p, unsigned bytes) { u64 a = (u64)p; return u32x4{(unsigned)a, (unsigned)(a >> 32) & 0xffffu, bytes, 0x00020000u}; }
typedef __amdgpu_buffer_rsrc_t rsrc_t;
DI rsrc_t mkrsrc(const void* p, unsigned bytes) { return __builtin_amdgcn_make_buffer_rsrc((void*)p, 0, bytes, 0x00020000); }
DI void st16(rsrc_t r, unsigned voff, unsigned soff, u32x4 v) {
  __builtin_amdgcn_raw_buffer_store_b128(v, r, voff, soff, 16);
  asm volatile("s_nop 1" ::"v"(v) : "memory");
}
DI u32x4 ld16(rsrc_t r, unsigned voff, unsigned soff) { return __builtin_amdgcn_raw_buffer_load_b128(r, voff, soff, 16); }
#define GLDS(d, voff, sbase, simm, lbase, limm) do { unsigned t_; \
  asm volatile("s_add_i32 m0, %1, %2\n\ts_add_i32 %0, %3, %4\n\tbuffer_load_dwordx4 %5, %6, %0 offen lds" \
               : "=&s"(t_) : "s"(lbase), "i"(limm), "s"(sbase), "i"(simm), "v"(voff), "s"(d) : "memory", "scc"); } while (0)
#define GLDS1(d, voff, soff, lb) asm volatile("s_mov_b32 m0, %0\n\tbuffer_load_dword %1, %2, %3 offen lds" ::"i"(lb), "v"(voff), "s"(d), "s"(soff) : "memory")
#define WAITV(n) asm volatile("s_waitcnt vmcnt(%0)" ::"i"(n) : "memory")
#define WAITL(n) asm volatile("s_waitcnt lgkmcnt(%0)" ::"i"(n) : "memory")
#define PIN(x) asm volatile("" : "+v"(x))
#define BARX() asm volatile("s_barrier" ::: "memory")
#define SB() __builtin_amdgcn_sched_barrier(0)
#define AI __attribute__((always_inline))
#if defined(__HIP_DEVICE_COMPILE__)
#define NOPK __attribute__((target("no-packed-fp32-ops")))
#else
#define NOPK
#endif
template <int N> struct IC { static constexpr int v = N; };
template <class F, int... I> DI void sfor_(F&& f, std::integer_sequence<int, I...>) { (f(IC<I>{}), ...); }
template <int N, class F> DI void sfor(F&& f) { sfor_(f, std::make_integer_sequence<int, N>{}); }

#define BLD(op, dst, d, voff, soff, pol) asm volatile("buffer_load_" op " %0, %1, %2, %3 offen" pol : "=v"(dst) : "v"(voff), "s"(d), "s"(soff) : "memory")
DI float asf(unsigned u) { return __builtin_bit_cast(float, u); }
DI unsigned asu(float f) { return __builtin_bit_cast(unsigned, f); }
DI float swadd32(float a, float b) { auto s = __builtin_amdgcn_permlane32_swap(asu(a), asu(b), false, false); const unsigned x = s[0], y = s[1]; return asf(x) + asf(y); }
DI float swadd16(float a, float b) { auto s = __builtin_amdgcn_permlane16_swap(asu(a), asu(b), false, false); const unsigned x = s[0], y = s[1]; return asf(x) + asf(y); }
DI float half_sum(float v) {
  v = dppadd<0xB1>(v); v = dppadd<0x4E>(v); v = dppadd<0x141>(v); v = dppadd<0x140>(v);
  return swadd16(v, v);
}

constexpr int SLOT = 36864, SP = 4 * SLOT, LDSZ = 163840;
constexpr int SP_T = SP + 8192, SP_CS = SP_T + 4608, SP_RS = SP_T + 6656, SP_PF = SP_T + 7168;
constexpr unsigned WS_E = 0, WS_F1 = 1024, WS_FH = 2048, WS_FA = 3072, WS_ERR = 5120, WS_ST = 8192, WS_F0 = 24576, WS_FZ = 32768;
constexpr unsigned WS_CS = 40960;
constexpr unsigned WS_H = WS_CS + 3 * 36864;
constexpr unsigned WS_XS = WS_H + 64u * 144 * 2048;
constexpr unsigned WS_Z = WS_XS + 576u * 4096;
constexpr unsigned WS_WT = WS_Z + 576u * 4096;
constexpr unsigned WS_P = WS_WT + 289u * 8192;
constexpr unsigned WS_END = WS_P + 4096u * 5120;
constexpr int SPIN = 200000;
constexpr unsigned OOB = 0x80000000u;

#define TS(ph, n) do { if ((ph == 9 && (n) == 0) || ph == 0) { if (SET == 0 || ph == 0) { u64 t_; asm volatile("s_memrealtime %0\n\ts_waitcnt lgkmcnt(0)" : "=s"(t_)); tsv[ph == 0 ? 0 : 1] = (unsigned)t_; } } } while (0)
#define TSDECL unsigned tsv[2] = {}
#define TSDUMP() do { if (SET == 0) { const unsigned lim = 6000u + (0 ? tsv[1] - tsv[0] : 0u); for (int i_ = 0; i_ < 100000; i_++) { u64 t_; asm volatile("s_memrealtime %0\n\ts_waitcnt lgkmcnt(0)" : "=s"(t_)); if ((unsigned)t_ - tsv[0] >= lim) break; __builtin_amdgcn_s_sleep(1); } } } while (0)
DI void put_flag(rsrc_t rw, bool on, unsigned off, unsigned tag) { __builtin_amdgcn_raw_buffer_store_b32(tag, rw, on ? 0u : OOB, off, 16); }
DI void put_flag8(rsrc_t rw, unsigned t, unsigned off, unsigned tag) { __builtin_amdgcn_raw_buffer_store_b32(tag, rw, t < 8 ? t * 1024 : OOB, off, 16); }
DI bool bad4(u32x4 v, unsigned tag) { return __builtin_amdgcn_ballot_w64(v[0] != tag || v[1] != tag || v[2] != tag || v[3] != tag) != 0; }
DI void poll4(rsrc_t rw, u32x4 dws, u32x4 v, unsigned voff, unsigned soff, unsigned tag) {
  for (int it = 0; bad4(v, tag); it++) {
    if (it >= SPIN) { put_flag(rw, true, WS_ERR, 1); break; }
    __builtin_amdgcn_s_sleep(4);
    BLD("dwordx4", v, dws, voff, soff, " sc1");
    asm volatile("s_waitcnt vmcnt(0)" : "+v"(v)::"memory");
  }
}
#define DIST1 4
#define RING1 5
#define DIST2 4
#define RING2 5
#define ABL 0
#define PREF 1
#define PREFC 1
#define PREF2 103
__global__ __launch_bounds__(512, 2) NOPK void kmain(const u16* __restrict__ x, const u16* __restrict__ wr, const u16* __restrict__ wi,
                                                     const u16* __restrict__ w1, const u16* __restrict__ b1, const u16* __restrict__ w2,
                                                     const u16* __restrict__ b2, u16* __restrict__ out, char* __restrict__ ws) {
  extern __shared__ __attribute__((aligned(16))) char smem_[];
  LAS char* smem = (LAS char*)smem_;
  const int tid = threadIdx.x, lane = tid & 63;
  const int wv = __builtin_amdgcn_readfirstlane(tid >> 6);
  auto body = [&](auto SETC) AI {
  constexpr int SET = decltype(SETC)::v;
  TSDECL;
  TS(0, 0);
  const int id = blockIdx.x, xcd = id & 7, slot = id >> 3;
  const int rt = (xcd >> 2) * 8 + (slot >> 2), bb = xcd & 3, q = slot & 3, nt = bb * 4 + q, lid = rt * 16 + nt;
  const int aq = rt >> 1, bh = rt & 1, c = wv & 3;
  const int fl0 = (nt & 1) * 32, flh = (nt & 1) ? 0 : 256;
  const bool has_dft = nt < 9;
  const rsrc_t rw = mkrsrc(ws, WS_END);
  const u32x4 dws = mkdesc(ws, WS_END), dx = mkdesc(x, 2048 * 1152), dw1 = mkdesc(w1, 576 * 9216), dw2 = mkdesc(w2, 4608 * 1152);
  const int r = lane & 31, hh = lane >> 5;
  unsigned ev, xc;
  u32x4 xs[2];
  asm volatile("s_load_dword %0, %1, 0x0" : "=s"(ev) : "s"(ws) : "memory");
  if (SET == 1 && PREFC) {
    u64 pc; unsigned len;
    asm volatile("s_getpc_b64 %0\n.Lkpc:\n\ts_mov_b32 %1, .Lkend-.Lkpc" : "=s"(pc), "=s"(len));
    GLDS1(mkdesc((const void*)pc, len < 0x40000u ? len : 0u), lane * 128u + (c * 32 + slot) * 8192u, 0, SP_PF);
  }
  if (SET == 0) {
    const int tr = 8 * nt + 2 * c;
    const unsigned ro = (4 * aq + (tr >> 5) + 32 * (32 * bh + (tr & 31))) * 1152u;
    BLD("dwordx4", xs[0], dx, hh * 36864 + r * 16, ro, "");
    BLD("dwordx4", xs[1], dx, hh * 36864 + r * 16, ro, " offset:512");
    BLD("dword", xc, dx, hh * 36864 + r * 4, ro, " offset:1024");
  }
  const unsigned xvo = (4 * aq + (wv >> 1) + 32 * (32 * bh + 16 * (wv & 1) + (lane >> 3))) * 1152u + (((lane & 7) ^ (lane >> 4)) * 16);
  const unsigned xvo1 = (xvo ^ 64) + 8 * 32 * 1152;
  const unsigned xlb = 2 * SLOT + wv * 10240;
#pragma unroll
  for (int n = 0; n < 5; n++) { GLDS(dx, xvo, 0, 128 * n, xlb, 1024 * n); GLDS(dx, xvo1, 0, 128 * n, xlb, 5120 + 1024 * n); }
  const unsigned wvo = (8 * c + ((lane >> 2) & 7)) * 9216u + hh * 64 + (lane & 3) * 16;
  const unsigned wso = (288 * nt + fl0) * 2, wlb = c * 1024;
  const u32x4 dft = mkdesc(&FT, has_dft ? sizeof(FT) : 0);
  const u32x4 dm = c < 2 ? dw1 : dft;
  const unsigned mvo = c < 2 ? (144 * c + (lane >> 2)) * 9216u + (lane & 3) * 16 : lane * 16;
  const unsigned mso = c < 2 ? (288 * nt + flh) * 2 : nt * 36864 + (c & 1) * 9216;
  const unsigned mstep = c < 2 ? 16 * 9216 : 1024, mhf = c < 2 ? 288 * 9216 : 18432, mlb = c * 9216;
#pragma unroll
  for (int n = SET; n < 9; n += 2) GLDS(dm, mvo, mso + n * mstep, 0, mlb, 1024 * n);
  const int cu = (xcd >> 2) * 32 + slot, cg = 18 * bb + cu / 3, g3 = cu % 3;
  u32x4 uu[3];
#pragma unroll
  for (int n = 0; n < 3; n++) BLD("dwordx4", uu[n], dw1, cu < 54 ? (lane >> 3) * 9216u + (lane & 7) * 16 : OOB, (192 * g3 + 24 * wv + 8 * n) * 9216 + cg * 128, "");
  asm volatile("s_waitcnt lgkmcnt(0)" : "+s"(ev)::"memory");
  const unsigned tag = ev * 0x9E3779B1u + 0x7F4A7C15u;
  TS(3, 1);
  if (SET == 0) {
    asm volatile("s_waitcnt vmcnt(18)" : "+v"(xs[0]), "+v"(xs[1]), "+v"(xc)::"memory");
    TS(3, 2);
    float s = blo(xc) + bhi(xc);
#pragma unroll
    for (int i = 0; i < 8; i++) { const unsigned w = xs[i >> 2][i & 3]; s += blo(w) + bhi(w); }
    const float mean = half_sum(s) * (1.0f / 576.0f);
    float v = 0.f;
#pragma unroll
    for (int i = 0; i < 9; i++) { const unsigned w = i < 8 ? xs[(i >> 2) & 1][i & 3] : xc; const float d0 = blo(w) - mean, d1 = bhi(w) - mean; v = __builtin_fmaf(d0, d0, v); v = __builtin_fmaf(d1, d1, v); }
    const float var = half_sum(v) * (1.0f / 576.0f);
    __builtin_amdgcn_raw_buffer_store_b64(u32x2{pk2(mean, __builtin_sqrtf(bfr(bfr(var) + 1e-5f))), tag}, rw, r == 0 ? hh * 8u : OOB, WS_ST + (rt * 128 + 8 * nt + 2 * c) * 8, 16);
  }
  TS(1, 1); TS(3, 3);
  const int q4 = (lane & 15) >> 2, p4 = lane & 3, g = lane >> 4, l15 = lane & 15;
  const unsigned hoff = (r >> 4) * 1024 + hh * 256 + (r & 15) * 16;
  const unsigned hrow = WS_H + (4 * rt + c) * 144 * 2048;
  const unsigned hso = hrow + 9 * nt * 2048;
  unsigned w2o[9];
  constexpr auto ftile = [](int ks) constexpr {
    const int qq = ks < 12 ? ks / 3 : ks < 20 ? (ks - 12) / 2 : (ks - 20) / 4, e = ks < 12 ? ks % 3 : ks < 20 ? 3 + (ks & 1) : 5 + (ks & 3);
    return 9 * qq + ((qq & 1) ? e : (e == 0 ? 8 : e - 1));
  };
  auto w2off = [&](int j) AI {
    const unsigned t = ftile(4 * j) | ftile(4 * j + 1) << 8 | ftile(4 * j + 2) << 16 | ftile(4 * j + 3) << 24;
    return (36 * bb + ((t >> (8 * c)) & 255)) * 36864 + q * 288;
  };
  auto w2stage = [&](unsigned so, int j, int n) AI { GLDS(dw2, w2o[n], so, 0, mlb, (j & 3) * SLOT + 1024 * n); };
  int cnt = 0, mark[12] = {};
  constexpr int HW = 8;
  u32x4 hb[HW][2];
  int mh[36];
  auto hload = [&](int ks) AI {
    const unsigned so = hrow + (36 * bb + ftile(ks)) * 2048;
    BLD("dwordx4", hb[ks % HW][0], dws, lane * 16, so, " sc1");
    BLD("dwordx4", hb[ks % HW][1], dws, lane * 16, so, " offset:1024 sc1");
    cnt += 2; mh[ks] = cnt;
  };
  u32x4 pf;
  int mpf, mst, mx;
  const unsigned pbase = WS_P + ((rt * 4 + q) * 64 + c) * 3072;
  const u32x4 dst = mkdesc(&ST, sizeof(ST));
  const int e2 = rt * 7 + nt - 9;
  const bool has2 = nt >= 9 && e2 < 33;
  const int kc[2] = {lid, 256 + e2};
  u32x4 fa[2][3], fb[2][3], f32[2][3], xx[2][8], wt[2][2];
  auto xload = [&](int z, int i) AI {
    const bool cplx = kc[z] != 0 && kc[z] != 288;
    const unsigned vo = q4 * 256 + g * 64 + p4 * 16;
    if (i < 8) BLD("dwordx4", xx[z][i], dws, (i < 4 || cplx) ? vo : OOB, WS_XS + (kc[z] == 288 ? 1 : 2 * kc[z]) * 4096 + i * 1024, " sc1");
    else BLD("dwordx4", wt[z][i - 8], dws, lane * 16, WS_WT + kc[z] * 8192 + (c * 2 + i - 8) * 1024, " sc1");
  };
  auto tload = [&](u32x4 (&f)[2][3], unsigned so, int i) AI { BLD("dwordx4", f[i >> 1][i & 1], dst, lane * 16, so + (i >> 1) * 3072 + (i & 1) * 1024, ""); };

  {
    constexpr int D = DIST1, R = RING1;
    const unsigned tq = q4 * 64 + (g & 1) * 32 + p4 * 8;
    LAS char* const tbP = smem + hh * 1024 + tq + SET * 512;
    LAS char* const tbM = smem + hh * 512 + tq + SET * 18432;
    const unsigned xa = 2 * SLOT + (4 * c + (r >> 3)) * 5120 + (r & 7) * 128 + ((hh ^ ((r >> 1) & 7)) * 16);
    u32x4 xf[36], ring[R], pm[2], sf, tf, xq[4];
    u32x2 sg, fc, wl[8];
    u32x4 cq[3];
    unsigned bia, w2s, wvv;
    f32x16 acc[6];
    f32x4 cr[2];
    float cs[8], gt[3][8], ge[3][8], k0, k1, sdf;
    u32x4 go[3];
    LAS char* bs[11];
    int mw, mt[5];
    const f32x16 zero = {0, 0, 0, 0, 0, 0, 0, 0, 0, 0, 0, 0, 0, 0, 0, 0};
    constexpr auto isM = [](int k) constexpr { return k == 0 || k == 2; };
    constexpr auto tile = [](int k) constexpr { return k < 4 ? (k & 1) : (k >> 1); };
    constexpr auto half = [](int k) constexpr { return k < 4 ? (k >> 1) : (k & 1); };
    constexpr auto slotof = [](int k) constexpr { return k < 2 ? k : (k + 2) & 3; };
    auto fetch = [&](int F) AI { const int k = F / 18, st = F % 18; ring[F % R] = trd2(bs[k] + st * (isM(k) ? 1024 : 2048), 256); };
    auto xread = [&](int i) AI { if (i < 36) xf[i] = *(LAS u32x4*)(smem + (xa ^ ((i & 3) * 32)) + ((i >> 2) % 5) * 1024); };
    auto perm = [&](int T) AI {
      const int st = T % 18, b = 18 * half(T / 18) + (st & ~1); const unsigned sel = (st & 1) ? 0x07060302u : 0x05040100u;
      u32x4 o;
      o[0] = __builtin_amdgcn_perm(xf[b][1], xf[b][0], sel); o[1] = __builtin_amdgcn_perm(xf[b][3], xf[b][2], sel);
      o[2] = __builtin_amdgcn_perm(xf[b + 1][1], xf[b + 1][0], sel); o[3] = __builtin_amdgcn_perm(xf[b + 1][3], xf[b + 1][2], sel);
      PIN(o); pm[st & 1] = o;
    };
    auto gstep = [&](int t, int u, int s, int z) AI {
      float* T_ = gt[z]; float* E_ = ge[z]; const int i = s >> 1;
      auto v = [&](int n) AI { return acc[t][8 * u + n]; };
      if (s == 0) { T_[0] = v(0) * v(0); PIN(T_[0]); }
      if ((s & 1) == 0) {
        if (i < 8) { T_[i] = v(i) * __builtin_fmaf(T_[i], k1, k0); PIN(T_[i]); }
        if (i >= 1) { E_[i - 1] = __builtin_amdgcn_rcpf(__builtin_fmaf(E_[i - 1], sdf, sdf)); PIN(E_[i - 1]); }
      } else {
        if (i < 8) { E_[i] = __builtin_amdgcn_exp2f(T_[i]); PIN(E_[i]); }
        if (i >= 1) { T_[i - 1] = v(i - 1) * E_[i - 1]; if ((i & 1) == 0) { unsigned w_ = pk2(T_[i - 2], T_[i - 1]); PIN(w_); go[z][(i >> 1) - 1] = w_; } else PIN(T_[i - 1]); }
        if (i + 1 < 8) { T_[i + 1] = v(i + 1) * v(i + 1); PIN(T_[i + 1]); }
      }
      if (s == 17 && (ABL & 2)) { PIN(go[z]); mt[t] = cnt; } else if (s == 17) { st16(rw, hoff, hso + (t == 0 ? flh : fl0 + 64 * (t - 1) + 32 * SET) * 64 + u * 512, go[z]); mt[t] = ++cnt; }
    };
    const int gl = 36 * c + (lane < 35 ? lane : 35);
    auto gather = [&]() AI {
      BLD("dwordx2", sg, dws, r * 8, WS_ST + (rt * 128 + c * 32) * 8, " sc1");
#pragma unroll
      for (int i = 0; i < 3 * (1 - SET); i++) BLD("dwordx4", cq[i], dws, gl * 16, WS_CS + i * 36864 + nt * 2304, " sc1");
    };
    WAITV(4 - SET); TS(1, 2); TS(3, 4); BARX(); TS(3, 5);
    bs[0] = tbM; PIN(bs[0]);
    xread(0); xread(1);
#pragma unroll
    for (int F = 0; F < D; F++) fetch(F);
    xread(2); xread(3);
    if (SET == 1) perm(0);
    SB();
    sfor<180>([&](auto TC_) AI {
      constexpr int T = decltype(TC_)::v, k = T / 18, st = T % 18, F = T + D, t = tile(k);
      if constexpr (T == 5) { WAITL(2); TS(3, 6); BARX(); TS(3, 7); }
      if constexpr (T == 9) { TS(3, 8); }
      if constexpr (T == 14) { TS(3, 9); }
      if constexpr (T == 19) { TS(3, 11); }
      if constexpr (T == 32) { TS(3, 14); }
      if constexpr (T == 26) { TS(3, 12); }
      if constexpr (T == 158) { TS(4, 1); }
      if constexpr (T == 159) { TS(4, 2); }
      if constexpr (T == 162) { TS(4, 3); }
      if constexpr (T == 166) { TS(4, 4); }
      if constexpr (T == 170) { TS(4, 5); }
      if constexpr (T == 171) { TS(4, 6); }
      if constexpr (T == 174) { TS(4, 7); }
      if constexpr (T == 176) { TS(4, 8); }
      if constexpr (T == 177) { TS(4, 9); }
      if constexpr (T == 65) { if (SET == 1) WAITL(4); BARX(); }
      if constexpr (F % 18 == 0) {
        constexpr int kb = F / 18;
        if (kb <= 5 || (kb & 1) == SET) WAITV(cnt - mark[kb]);
        else if (kb == 8 || kb == 10) WAITV(cnt - mt[kb / 2 - 3]);
        if (kb <= 2 || kb == 10) WAITL(0); else if (kb == 3) WAITL(2); else if (kb == 4) WAITL(4);
        TS(1, 2 + kb); if (kb == 1) { TS(3, 10); } if (kb == 2) { TS(3, 15); } BARX();
        if (kb == 7 && SET == 0) { put_flag8(rw, tid, WS_F0 + lid * 4, tag); cnt++; }
        if ((kb == 8 || kb == 10) && SET == 0) { put_flag(rw, tid == 0, (kb == 8 ? WS_FA : WS_F1) + lid * 4, tag); cnt++; }
        if constexpr (kb < 10) { bs[kb] = (isM(kb) ? tbM : tbP) + slotof(kb) * SLOT; PIN(bs[kb]); }
      }
      if constexpr (F < 180) fetch(F);
      if constexpr (SET == 1 && isM(k)) {
        const int a = (st & 1) ? 5 : 0;
        acc[a] = mfma32(pm[st & 1], ring[T % R], (k == 0 && st < 2) ? zero : acc[a]);
        if (st < 17) perm(T + 1);
      } else {
        acc[t] = mfma32(ring[T % R], xf[18 * half(k) + st], (half(k) == 0 && st == 0) ? zero : acc[t]);
      }
      if (SET == 1 && T == 35) perm(36);
      if constexpr (T < 4) { xread(4 + 4 * T); xread(5 + 4 * T); xread(6 + 4 * T); xread(7 + 4 * T); }
      if constexpr (T >= 32 && T < 38) { xread(20 + 3 * (T - 32)); xread(21 + 3 * (T - 32)); xread(22 + 3 * (T - 32)); }
      constexpr int xp = T == 5 ? 0 : T == 7 ? 1 : T == 8 ? 2 : T == 10 ? 3 : T == 11 ? 4 : T == 13 ? 5 : T == 24 ? 6 : T == 25 ? 7 : -1;
      if constexpr (xp >= 0) { GLDS(dx, (xp & 1) ? xvo1 : xvo, 0, 640 + 128 * (xp >> 1), xlb, (xp & 1) * 5120 + 1024 * (xp >> 1)); mark[2] = ++cnt; }
      if constexpr (T < 5 && 2 * T + 1 - SET < 9) { GLDS(dw1, wvo, wso, (2 * T + 1 - SET) * 32 * 9216, wlb, SLOT + 4096 * (2 * T + 1 - SET)); mark[1] = ++cnt; }
      if constexpr (F >= 18 && F < 180) {
        constexpr int kb = F / 18, fs = F % 18;
        constexpr int ks = kb <= 2 ? (fs < 9 ? kb + 1 : -1) : kb == 3 ? (fs < 9 ? 4 : 5) : (fs < 9 ? kb + 2 : -1);
        constexpr int o = (SET + ks) & 1;
        constexpr int n = ks <= 5 ? ((fs % 9) % 2 == o ? fs % 9 : 9) : (o == 0 ? fs : 9);
        if constexpr (ks >= 2 && n < 9 && !((ABL & 1) && ks >= 5 && ks < 10)) {
          if constexpr (ks == 2) GLDS(dm, mvo, mso + mhf + n * mstep, 0, mlb, 1024 * n);
          else if constexpr (ks <= 9) GLDS(dw1, wvo, wso, half(ks) * 288 * 9216 + (tile(ks) - 1) * 128 + n * 32 * 9216, wlb, slotof(ks) * SLOT + 4096 * n);
          else {
            const int e = 64 * n + lane; w2o[n] = (e / 18) * 1152 + (e % 18) * 16;
            if (n == 0) w2s = w2off(ks - 10);
            w2stage(w2s, ks - 10, n);
          }
          mark[ks] = ++cnt;
        }
      }
      if constexpr (T == 10) asm volatile("s_waitcnt vmcnt(%3)" : "+v"(uu[0]), "+v"(uu[1]), "+v"(uu[2]) : "i"(cnt + 1 - SET) : "memory");
      if constexpr (T == 10 || T == 11) {
#pragma unroll
        for (int i = 2 * (T - 10); i < 2 * (T - 10) + 2; i++) {
          cs[2 * i] = blo(uu[0][i]) + blo(uu[1][i]) + blo(uu[2][i]); cs[2 * i + 1] = bhi(uu[0][i]) + bhi(uu[1][i]) + bhi(uu[2][i]);
          PIN(cs[2 * i]); PIN(cs[2 * i + 1]);
        }
      }
      if constexpr (T == 12) {
#pragma unroll
        for (int i = 0; i < 4; i++) { cs[i] = swadd32(cs[i], cs[i + 4]); PIN(cs[i]); }
      }
      if constexpr (T == 13) {
        const float a = dppadd<0x128>(swadd16(cs[0], cs[2])), b = dppadd<0x128>(swadd16(cs[1], cs[3]));
        *(LAS float*)(smem + SP_CS + (lane & 7) * 256 + (lane >> 3) * 32 + wv * 4) = (lane & 8) ? b : a;
      }
      if constexpr (SET == 0 && T == 14) { cr[0] = *(LAS f32x4*)(smem + SP_CS + lane * 32); cr[1] = *(LAS f32x4*)(smem + SP_CS + lane * 32 + 16); }
      if constexpr (SET == 0 && T == 16) {
        const float s = ((cr[0][0] + cr[0][1]) + (cr[0][2] + cr[0][3])) + ((cr[1][0] + cr[1][1]) + (cr[1][2] + cr[1][3]));
        __builtin_amdgcn_raw_buffer_store_b64(u32x2{pk2(s, s - bfr(s)), tag}, rw, (c == 0 && cu < 54) ? lane * 8u : OOB, WS_CS + g3 * 36864 + cg * 512, 16);
        cnt++;
      }
      if constexpr (T == 41) {
        gather();
        if (SET == 0) BLD("dword", bia, mkdesc(b1, 9216), gl * 4, 576 * nt, "");
        else BLD("dwordx2", fc, mkdesc(&ST.fc, 2304), r * 8, (has_dft ? nt : 0) * 256, "");
        cnt += SET == 0 ? 5 : 2;
      }
      if constexpr (T == 48) {
        auto gwait = [&]() AI {
          if (SET == 0) asm volatile("s_waitcnt vmcnt(4)" : "+v"(sg), "+v"(cq[0]), "+v"(cq[1]), "+v"(cq[2]), "+v"(bia)::"memory");
          else asm volatile("s_waitcnt vmcnt(0)" : "+v"(sg), "+v"(fc)::"memory");
          unsigned m = sg[1] ^ tag;
          if (SET == 0) m |= (cq[0][1] ^ tag) | (cq[0][3] ^ tag) | (cq[1][1] ^ tag) | (cq[1][3] ^ tag) | (cq[2][1] ^ tag) | (cq[2][3] ^ tag);
          return __builtin_amdgcn_ballot_w64(m != 0) != 0;
        };
        for (int it = 0; gwait(); it++) {
          if (it >= SPIN) { put_flag(rw, true, WS_ERR, 2); break; }
          __builtin_amdgcn_s_sleep(4);
          gather();
          WAITV(0);
        }
        if (SET == 0) {
          *(LAS u32x4*)(smem + SP_T + gl * 32) = u32x4{cq[0][0], cq[1][0], cq[2][0], bia & 0xffffu};
          *(LAS u32x4*)(smem + SP_T + gl * 32 + 16) = u32x4{cq[0][2], cq[1][2], cq[2][2], bia >> 16};
        }
      }
      if constexpr (T == 51) {
        const unsigned nm = hh ? 0u : ((sg[0] & 0xffffu) ^ 0x8000u) * 0x10001u;
        sf = u32x4{nm, nm, nm, hh ? 0u : sg[0] >> 16};
        sdf = bhi(sg[0]);
        PIN(sf); PIN(sdf);
      }
      if constexpr (SET == 1 && T == 52) *(LAS float*)(smem + SP_RS + c * 128 + r * 4) = __builtin_amdgcn_rcpf(sdf);
      if constexpr (T == (SET == 0 ? 52 : 70)) {
        const float rstd = __builtin_amdgcn_rcpf(sdf), c0 = -2.0f * 0.7978845608028654f * 1.4426950408889634f, c1 = c0 * 0.044715f;
        k0 = c0 * rstd; k1 = c1 * rstd * rstd * rstd;
        PIN(k0); PIN(k1);
      }
      if (SET == 0 && T == 51) tf = *(LAS u32x4*)(smem + SP_T + (flh + r) * 16);
      if (SET == 0 && T == 53) acc[0] = mfma32(tf, sf, acc[0]);
      if constexpr (!isM(k)) {
        if (half(k) == 1 && st == 15) tf = *(LAS u32x4*)(smem + SP_T + (fl0 + 64 * (t - 1) + 32 * SET + r) * 16);
        if (half(k) == 1 && st == 17) acc[t] = mfma32(tf, sf, acc[t]);
      } else if constexpr (SET == 1 && k == 2 && st == 17) {
        acc[0] = mfma32(sf, u32x4{fc[0], 0, 0, 0}, acc[0]);
        acc[5] = mfma32(sf, u32x4{fc[1], 0, 0, 0}, acc[5]);
      }
      constexpr int TW = 26;
      if constexpr (!(ABL & 16) && SET == 0 && ((T >= TW && T < TW + 4) || (T >= TW + 17 && T < TW + 21))) {
        const int wset = 8 * (lid >> 2) + (lane & 7), wp = 5 * c + (lane >> 3), wcol = 72 * q + (q == 3) + 4 * wp;
        const bool on = lane < 40 && wp < 19 && wcol <= 285;
        if constexpr (T < TW + 8) {
          constexpr int i = T - TW;
          if (i == 0) { wvv = ((16 * (wset >> 7) + (wset & 15) + 256 * ((wset >> 4) & 7)) * 289 + wcol) * 2u; PIN(wvv); wvv = on ? wvv : OOB; }
          BLD("dwordx2", wl[2 * i], mkdesc(wr, 2048 * 578), wvv, i * 64 * 578, "");
          BLD("dwordx2", wl[2 * i + 1], mkdesc(wi, 2048 * 578), wvv, i * 64 * 578, "");
          cnt += 2; mw = cnt;
        } else {
          constexpr int j = T - TW - 17;
          if (j == 0) {
            asm volatile("s_waitcnt vmcnt(%8)" : "+v"(wl[0]), "+v"(wl[1]), "+v"(wl[2]), "+v"(wl[3]), "+v"(wl[4]), "+v"(wl[5]), "+v"(wl[6]), "+v"(wl[7]) : "i"(cnt - mw) : "memory");
            wvv = wcol * 8192u + wset * 16; PIN(wvv); wvv = on ? wvv : OOB;
          }
          u32x4 o;
#pragma unroll
          for (int i = 0; i < 4; i++) o[i] = __builtin_amdgcn_perm(wl[2 * i + 1][j >> 1], wl[2 * i][j >> 1], (j & 1) ? 0x07060302u : 0x05040100u);
          st16(rw, wvv, WS_WT + j * 8192, o);
          mw = ++cnt;
        }
      }
      if constexpr (SET == 0) {
        if constexpr (T == 161) { BLD("dwordx4", pf, dws, 0, WS_FA + (lid >> 2) * 16, " sc1"); mpf = ++cnt; }
        if constexpr (T == 168) {
          asm volatile("s_waitcnt vmcnt(%1)" : "+v"(pf) : "i"(cnt - mpf) : "memory");
          poll4(rw, dws, pf, 0, WS_FA + (lid >> 2) * 16, tag);
        }
        if constexpr (T >= 168 && (T - 168) % 3 == 0) hload((T - 168) / 3);
      }
      if constexpr (SET == 1) {
        if constexpr (T >= 168 && T < 176) { tload(T < 172 ? fa : f32, T < 172 ? c * 6144 : 49152, T & 3); cnt++; }
      }
      if constexpr (PREF && (SET == 0 ? (T == 77 || T == 113) : T == 107)) {
        const int u = wv * 32 + (rt & 7) * 4 + q;
        const u32x4 dp = u < 162 ? dw2 : u < 185 ? mkdesc(&GT, sizeof(GT)) : dst;
        GLDS1(dp, (c >> 1) == (T == 113) ? lane * 128u : OOB, u < 162 ? bb * 1327104 + u * 8192 : u < 185 ? bb * 184320 + (u - 162) * 8192 : (u - 185) * 8192, SP_PF);
        cnt++;
      }
      if constexpr (SET == 0) {
        if constexpr (T >= 55 && T < 73) gstep(0, 0, T - 55, 2);
        if constexpr (T >= 73 && T < 109 && ((T - 73) & 1) == 0) gstep(0, 1, (T - 73) >> 1, 2);
      } else if constexpr (T >= 55 && T < 61) {
        constexpr int u = (T - 55) / 3, ph = (T - 55) % 3;
        const int bc = 32 * nt + r;
        const float b1c = bc == 144 ? 0.f : 1.f, b2c = bc < 144 ? -1.f : 1.f, a2c = -b1c * b2c;
        if (ph == 0) { cr[0] = *(LAS f32x4*)(smem + SP_RS + c * 128 + u * 64 + hh * 16); cr[1] = *(LAS f32x4*)(smem + SP_RS + c * 128 + u * 64 + 32 + hh * 16); }
        else {
          float o1[4], o2[4];
#pragma unroll
          for (int i = 0; i < 4; i++) {
            const float e = acc[0][8 * u + 4 * (ph - 1) + i] * cr[ph - 1][i], o = acc[5][8 * u + 4 * (ph - 1) + i] * cr[ph - 1][i];
            o1[i] = __builtin_fmaf(b1c, o, e); o2[i] = __builtin_fmaf(a2c, e, b2c * o);
          }
          LAS char* p = smem + r * 272 + (2 * u + hh) * 64 + c * 16 + (ph - 1) * 8;
          *(LAS u32x2*)p = u32x2{pk2(o1[0], o1[1]), pk2(o1[2], o1[3])};
          *(LAS u32x2*)(p + 32 * 272) = u32x2{pk2(o2[0], o2[1]), pk2(o2[2], o2[3])};
        }
      } else if constexpr (T == 65) {
#pragma unroll
        for (int i = 0; i < 4; i++) xq[i] = *(LAS u32x4*)(smem + (16 * c + 4 * i + g) * 272 + l15 * 16);
      } else if constexpr (T >= 67 && T < 71) {
        const int bc = 32 * nt + 16 * (c & 1) + 4 * (T - 67) + g;
        const int p1 = 2 * bc - (bc > 144 ? 287 : 0);
        int p2 = (bc < 144 ? 576 : 865) - 2 * bc; p2 = bc == 0 ? 1 : p2; p2 = bc == 144 ? 289 : p2;
        unsigned vo = (c < 2 ? p1 : p2) * 4096u + l15 * 16; PIN(vo);
        st16(rw, (has_dft && !(ABL & 32)) ? vo : OOB, WS_XS + bh * 2048 + aq * 256, xq[T - 67]);
        cnt++;
      }
      if constexpr (T >= 73 && (ABL & 4)) { if constexpr ((T - 73) % 36 == 0) PIN(acc[(T - 73) / 36 + 1]); } else if constexpr (T >= 73) {
        const int pt = (T - 73) / 36 + 1, s = (T - 73) % 36;
        if (s < 18) gstep(pt, 0, s, 0);
        if (s >= 16 && s < 34) gstep(pt, 1, s - 16, 1);
      }
      SB();
    });
    TS(1, 13); TS(4, 10);
#pragma unroll
    for (int s = 0; s < 18; s++) {
      if (s % 4 == 1 && s < 16 && SET == 0) hload(4 + s / 4);
      if (s == 6 && SET == 1) { BLD("dwordx4", pf, dws, lane * 16, WS_F0 + xcd * 1024, " sc1"); mpf = ++cnt; }
      gstep(4, 0, s, 0); gstep(4, 1, s, 1);
    }
    mst = cnt;
  }
  TS(1, 14); TS(2, 1); TS(4, 11);
  if constexpr (SET == 0) {
    constexpr int D = DIST2, R = RING2;
    const unsigned tb = (16 * (g >> 1) + 4 * (g & 1) + q4) * 288 + p4 * 8;
    const unsigned zb = 2 * SLOT + (8 * (g >> 1) + 2 * (g & 1) + (q4 >> 1)) * 512 + (2 * (p4 >> 1) + (q4 & 1)) * 64 + ((c ^ (2 * (g & 1) + (q4 >> 1))) * 16) + (p4 & 1) * 8;
    u32x2 ra[R], rb[R], xv[5], bv[5];
    f32x4 acc[9][2];
    LAS char* bs[11];
    const f32x4 z4 = {0, 0, 0, 0};
    auto fad = [&](int F) AI { return bs[F / 36] + ((F % 36) / 9) * 9216 + (F % 9) * 32; };
    bs[0] = smem + tb; PIN(bs[0]);
#pragma unroll
    for (int F = 0; F < D; F++) { ra[F] = trd(fad(F)); rb[F] = trd(fad(F) + 2304); }
    TS(4, 12);
    const unsigned orow = (4 * aq + c + 32 * (32 * bh + 16 * (bb >> 1) + l15)) * 1152u + g * 8, ocol = 288 * q + 160 * (bb & 1);
    SB();
    sfor<369>([&](auto GC_) AI {
      constexpr int G = decltype(GC_)::v, ks = G / 9, dt = G % 9, F = G + D;
      if constexpr (F % 36 == 0) {
        constexpr int j = F / 36;
        if (j == 1) { TS(4, 13); WAITV(cnt - mst); TS(4, 14); }
        if (j < 10) { TS(2, 1 + j); BARX(); }
        if (j == 1) { put_flag(rw, tid == 0, WS_FH + lid * 4, tag); cnt++; }
        bs[j] = smem + (j < 10 ? (j & 3) * SLOT : SP) + tb; PIN(bs[j]);
      }
      if constexpr (dt == 0 && ks < 36) asm volatile("s_waitcnt vmcnt(%2)" : "+v"(hb[ks % HW][0]), "+v"(hb[ks % HW][1]) : "i"(cnt - mh[ks]) : "memory");
      const u32x4 a = {ra[G % R][0], ra[G % R][1], rb[G % R][0], rb[G % R][1]};
      acc[dt][0] = mfma16(a, hb[ks % HW][0], ks == 0 ? z4 : acc[dt][0]);
      SB();
      if constexpr (F < 369) ra[F % R] = trd(fad(F));
      SB();
      acc[dt][1] = mfma16(a, hb[ks % HW][1], ks == 0 ? z4 : acc[dt][1]);
      SB();
      if constexpr (F < 369) rb[F % R] = trd(fad(F) + 2304);
      if constexpr (G == 9 * (10 - HW) + 7 || G == 9 * (18 - HW) + 7) { BLD("dwordx4", pf, dws, 0, (ks + HW < 12 ? WS_F1 : WS_FH) + (lid >> 2) * 16, " sc1"); mpf = ++cnt; }
      if constexpr (dt == 8 && ks + HW < 36) {
        if (ks + HW == 12 || ks + HW == 20) {
          asm volatile("s_waitcnt vmcnt(%1)" : "+v"(pf) : "i"(cnt - mpf) : "memory");
          poll4(rw, dws, pf, 0, (ks + HW == 12 ? WS_F1 : WS_FH) + (lid >> 2) * 16, tag);
        }
        hload(ks + HW);
      }
      if constexpr (ks >= 35 && ks < 40 && (dt == 5 || dt == 6)) {
        constexpr int sp = ks - 35;
        hb[(ks + 1) % HW][dt - 5] = trd2(smem + zb + (dt - 5) * 256 + sp * 8192 - (sp == 4 ? (g >> 1) * 4096 : 0), 2048);
      }
      if constexpr (G >= 270 && G < 320 && G % 5 == 0) {
        constexpr int i = (G - 270) / 10;
        const bool on = i < 4 || (bb & 1) == 0;
        if (G % 10 == 0) BLD("dwordx2", xv[i], dx, on ? orow : OOB, ocol + 32 * i, "");
        else BLD("dwordx2", bv[i], mkdesc(b2, 1152), on ? g * 8 : OOB, ocol + 32 * i, "");
        cnt++;
      }
      SB();
    });
    TS(2, 11);
    auto pk4 = [&](f32x4 a, f32x4 b) AI { return u32x4{pk2(a[0], a[1]), pk2(a[2], a[3]), pk2(b[0], b[1]), pk2(b[2], b[3])}; };
#pragma unroll
    for (int d = 0; d < 4; d++)
#pragma unroll
      for (int i = 0; i < 3 - (d & 1); i++) {
        const int t0 = 5 * (d & 1) + 2 * i;
        st16(rw, d == bb ? OOB : lane * 16 + i * 1024, pbase + (d * 16 + bb * 4) * 3072, pk4(acc[t0][d >> 1], acc[i < 2 ? t0 + 1 : t0][d >> 1]));
      }
    u32x4 pp[3][3];
    auto pload = [&]() AI {
#pragma unroll
      for (int s = 0; s < 3; s++)
#pragma unroll
        for (int i = 0; i < 3; i++) BLD("dwordx4", pp[s][i], dws, (i < 2 || (bb & 1) == 0) ? lane * 16 + i * 1024 : OOB, pbase + (bb * 16 + ((bb + 1 + s) & 3) * 4) * 3072, " sc1");
    };
    asm volatile("s_waitcnt vmcnt(10)" : "+v"(xv[0]), "+v"(xv[1]), "+v"(xv[2]), "+v"(xv[3]), "+v"(xv[4]), "+v"(bv[0]), "+v"(bv[1]), "+v"(bv[2]), "+v"(bv[3]), "+v"(bv[4])::"memory");
    f32x4 own[5];
#pragma unroll
    for (int i = 0; i < 5; i++) {
      const int i2 = i < 4 ? 5 + i : 8;
#pragma unroll
      for (int e = 0; e < 4; e++) {
        const float lo = (bb & 2) ? acc[i][1][e] : acc[i][0][e], hi = (bb & 2) ? acc[i2][1][e] : acc[i2][0][e];
        const unsigned xw = xv[i][e >> 1], bw = bv[i][e >> 1];
        own[i][e] = ((bb & 1) ? hi : lo) + ((e & 1) ? bhi(xw) + bhi(bw) : blo(xw) + blo(bw));
      }
      PIN(own[i]);
    }
    TS(2, 12);
    for (int it = 0;; it++) {
      pload();
      asm volatile("s_waitcnt vmcnt(0)" : "+v"(pp[0][0]), "+v"(pp[0][1]), "+v"(pp[0][2]), "+v"(pp[1][0]), "+v"(pp[1][1]), "+v"(pp[1][2]), "+v"(pp[2][0]), "+v"(pp[2][1]), "+v"(pp[2][2])::"memory");
      unsigned m = 0;
#pragma unroll
      for (int i = 0; i < 36; i++) { const unsigned w = pp[i / 12][(i >> 2) % 3][i & 3]; m = m > w ? m : w; }
      if (__builtin_amdgcn_ballot_w64(m == 0xffffffffu) == 0) break;
      if (it >= SPIN / 16) { put_flag(rw, true, WS_ERR, 4); break; }
    }
    TS(2, 13);
    const rsrc_t ro = mkrsrc(out, 2048 * 1152);
#pragma unroll
    for (int i = 0; i < 5; i++) {
      float v[4];
#pragma unroll
      for (int e = 0; e < 4; e++) {
        const unsigned w0 = pp[0][i >> 1][2 * (i & 1) + (e >> 1)], w1 = pp[1][i >> 1][2 * (i & 1) + (e >> 1)], w2_ = pp[2][i >> 1][2 * (i & 1) + (e >> 1)];
        v[e] = (e & 1) ? (own[i][e] + bhi(w0)) + (bhi(w1) + bhi(w2_)) : (own[i][e] + blo(w0)) + (blo(w1) + blo(w2_));
      }
      __builtin_amdgcn_raw_buffer_store_b64(u32x2{pk2(v[0], v[1]), pk2(v[2], v[3])}, ro, (i < 4 || (bb & 1) == 0) ? orow : OOB, ocol + 32 * i, 0);
    }
    TS(2, 14);
  } else {
    auto stage = [&](int j) AI {
      const unsigned so = w2off(j);
#pragma unroll
      for (int n = 0; n < 9; n++) w2stage(so, j, n);
      cnt += 9; mark[j] = cnt;
    };
    auto bar = [&](int j, int m) AI { WAITV(cnt - m); WAITL(0); TS(2, 1 + j); BARX(); };
    const u32x4 dg = mkdesc(&GT, sizeof(GT));
    f32x4 t1[4], t2[4];
    const f32x4 z4 = {0, 0, 0, 0};
    auto xpin = [&](int z) AI {
#pragma unroll
      for (int i = 0; i < 8; i++) PIN(xx[z][i]);
      PIN(wt[z][0]); PIN(wt[z][1]);
    };
    auto tpin = [&](u32x4 (&f)[2][3]) AI {
#pragma unroll
      for (int t = 0; t < 2; t++) { PIN(f[t][0]); PIN(f[t][1]); f[t][2] = f[t][1] ^ 0x80008000u; }
    };
    asm volatile("s_waitcnt vmcnt(%1)" : "+v"(pf) : "i"(cnt - mpf) : "memory");
    poll4(rw, dws, pf, lane * 16, WS_F0 + xcd * 1024, tag);
#pragma unroll
    for (int i = 0; i < 10; i++) xload(0, i);
    cnt += 10; mx = cnt;
    stage(2);
    {
      const float f1 = (16 * c + l15) * (1.0f / 2048.0f), f2 = (16 * c + 4 * g) * (1.0f / 2048.0f);
#pragma unroll
      for (int at = 0; at < 2; at++)
#pragma unroll
        for (int i = 0; i < 4; i++) {
          const float a1 = (float)(16 * at + 4 * g + i) * f1, a2 = (float)(16 * at + l15) * (f2 + i * (1.0f / 2048.0f));
          t1[2 * at][i] = __builtin_amdgcn_cosf(a1); t1[2 * at + 1][i] = __builtin_amdgcn_sinf(a1);
          t2[2 * at][i] = __builtin_amdgcn_cosf(a2) * (1.0f / 2048.0f); t2[2 * at + 1][i] = __builtin_amdgcn_sinf(a2) * (1.0f / 2048.0f);
        }
#pragma unroll
      for (int i = 0; i < 4; i++) { PIN(t1[i]); PIN(t2[i]); }
    }
    TS(4, 12);
    bar(1, mst);
    if (has2) {
#pragma unroll
      for (int i = 0; i < 10; i++) xload(1, i);
    }
#pragma unroll
    for (int i = 0; i < 4; i++) tload(fb, 24576 + c * 6144, i);
    cnt += 4;
    const int mb = cnt;
    stage(3);
    WAITV(cnt - mx);
    tpin(fa); tpin(f32); xpin(0);
    auto front = [&](int z) AI {
      u32x4 br, bi, yr, yi;
#pragma unroll
      for (int at = 0; at < 2; at++) {
        f32x4 tr = z4, ti = z4;
#pragma unroll
        for (int t = 0; t < 2; t++) {
          const u32x4 xr = xx[z][2 * t + at], xi = xx[z][4 + 2 * t + at];
          tr = mfma16(xr, fa[t][0], tr); ti = mfma16(xi, fa[t][0], ti);
          tr = mfma16(xi, fa[t][1], tr); ti = mfma16(xr, fa[t][2], ti);
        }
        const f32x4 co = t1[2 * at], si = t1[2 * at + 1];
        const f32x4 ur = tr * co + ti * si, ui = ti * co - tr * si;
        br[2 * at] = pk2(ur[0], ur[1]); br[2 * at + 1] = pk2(ur[2], ur[3]);
        bi[2 * at] = pk2(ui[0], ui[1]); bi[2 * at + 1] = pk2(ui[2], ui[3]);
      }
#pragma unroll
      for (int ct = 0; ct < 2; ct++) {
        f32x4 xr = mfma16(f32[ct][0], br, z4), xi = mfma16(f32[ct][0], bi, z4);
        xr = mfma16(f32[ct][1], bi, xr); xi = mfma16(f32[ct][2], br, xi);
        float vr[4], vi[4];
#pragma unroll
        for (int i = 0; i < 4; i++) { const unsigned w = wt[z][ct][i]; const float a = blo(w), b = bhi(w); vr[i] = xr[i] * a - xi[i] * b; vi[i] = xr[i] * b + xi[i] * a; }
        yr[2 * ct] = pk2(vr[0], vr[1]); yr[2 * ct + 1] = pk2(vr[2], vr[3]);
        yi[2 * ct] = pk2(vi[0], vi[1]); yi[2 * ct + 1] = pk2(vi[2], vi[3]);
      }
#pragma unroll
      for (int at = 0; at < 2; at++) {
        f32x4 pr = mfma16(yr, f32[at][0], z4), pi = mfma16(yi, f32[at][0], z4);
        pr = mfma16(yi, f32[at][2], pr); pi = mfma16(yr, f32[at][1], pi);
        const f32x4 co = t2[2 * at], si = t2[2 * at + 1];
        const f32x4 ur = pr * co - pi * si, ui = pi * co + pr * si;
        LAS char* p = smem + SP + z * 8192 + (at * 4 + (c >> 1)) * 1024 + lane * 16 + (c & 1) * 8;
        *(LAS u32x2*)p = u32x2{pk2(ur[0], ur[1]), pk2(ur[2], ur[3])};
        *(LAS u32x2*)(p + 2048) = u32x2{pk2(ui[0], ui[1]), pk2(ui[2], ui[3])};
      }
    };
    front(0);
    TS(4, 14);
    bar(2, mb);
    tpin(fb);
    stage(4);
    auto back = [&](int z) AI {
      const int pair = kc[z] == 288 ? 0 : kc[z];
      const bool on = kc[z] == 0 ? (g & 1) == 0 : (kc[z] == 288 ? (g & 1) == 1 : true);
#pragma unroll
      for (int at = 0; at < 2; at++) {
        f32x4 zr = z4, zi = z4;
#pragma unroll
        for (int t = 0; t < 2; t++) {
          LAS char* p = smem + SP + z * 8192 + (at * 4 + t) * 1024 + lane * 16;
          const u32x4 pr = *(LAS u32x4*)p, pi = *(LAS u32x4*)(p + 2048);
          zr = mfma16(fb[t][0], pr, zr); zi = mfma16(fb[t][0], pi, zi);
          zr = mfma16(fb[t][2], pi, zr); zi = mfma16(fb[t][1], pr, zi);
        }
        const unsigned r0 = pk2(zr[0], zr[1]), r1 = pk2(zr[2], zr[3]);
        const unsigned i0 = kc[z] == 288 ? r0 : pk2(zi[0], zi[1]), i1 = kc[z] == 288 ? r1 : pk2(zi[2], zi[3]);
        const auto s0 = __builtin_amdgcn_permlane16_swap(r0, i0, false, false), s1 = __builtin_amdgcn_permlane16_swap(r1, i1, false, false);
        const unsigned a0 = s0[0], a1 = s1[0], a2 = s0[1], a3 = s1[1];
        st16(rw, on ? q4 * 1024u + g * 64 + ((p4 ^ (pair & 3)) * 16) : OOB, WS_Z + pair * 8192 + at * 4096 + c * 256, u32x4{a0, a1, a2, a3});
      }
    };
    back(0);
    cnt += 2;
    if (has2) { xpin(1); front(1); }
    bar(3, mark[3]);
    if (has2) back(1);
    stage(5);
#pragma unroll
    for (int d = 0; d < 4; d++)
#pragma unroll
      for (int i = 0; i < 3 - (d & 1); i++) st16(rw, d == bb ? OOB : lane * 16 + i * 1024, pbase + (d * 16 + bb * 4) * 3072, u32x4{~0u, ~0u, ~0u, ~0u});
    cnt += 10;
    const int mz = cnt;
    bar(4, mark[4]);
    stage(6);
#pragma unroll
    for (int n = 0; n < 3; n++) { const int pc = n < 2 ? c + 4 * n : 8; GLDS(dg, lane * 16, (nt * 5 + 4) * 9216 + pc * 1024, 0, SP + pc * 1024, 0); }
    cnt += 3;
    bar(5, mz);
    put_flag8(rw, tid - 256, WS_FZ + lid * 4, tag); cnt++;
    stage(7);
    bar(6, mark[6]);
    stage(8);
    bar(7, mark[7]);
#pragma unroll
    for (int n = 0; n < 9; n++) GLDS(dg, lane * 16, (nt * 5 + c) * 9216, 1024 * n, mlb, SLOT + 1024 * n);
    __builtin_amdgcn_s_sleep(4);
    BLD("dwordx4", pf, dws, c == 0 ? lane * 16 : OOB, WS_FZ + xcd * 1024, " sc1");
    asm volatile("s_waitcnt vmcnt(0)" : "+v"(pf)::"memory");
    if (c == 0) poll4(rw, dws, pf, lane * 16, WS_FZ + xcd * 1024, tag);
    bar(8, cnt);
    u32x4 zz[9];
#pragma unroll
    for (int n = 0; n < 9; n++) BLD("dwordx4", zz[n], dws, hh * 8192 + r * 16, WS_Z + (72 * bb + 2 * c + 8 * n) * 8192 + rt * 512, " sc1");
    put_flag(rw, lid == 0 && tid == 256, WS_E, tag);
    WAITV(1);
#pragma unroll
    for (int n = 0; n < 9; n++) { PIN(zz[n]); *(LAS u32x4*)(smem + 2 * SLOT + (c + 4 * n) * 1024 + lane * 16) = zz[n]; }
    bar(9, cnt - 1);
  }
  if (SET == 0) asm volatile(".Lkend:");
  TSDUMP();
  };
  if (wv < 4) body(IC<0>{}); else body(IC<1>{});
}

extern "C" void kernel_launch(void* const* d_in, const int* in_sizes, int n_in, void* d_out, int out_size, void* d_ws, size_t ws_size, hipStream_t stream) {
  (void)hipFuncSetAttribute((const void*)kmain, hipFuncAttributeMaxDynamicSharedMemorySize, LDSZ);
  kmain<<<256, 512, LDSZ, stream>>>((const u16*)d_in[0], (const u16*)d_in[1], (const u16*)d_in[2], (const u16*)d_in[4], (const u16*)d_in[5],
                                    (const u16*)d_in[6], (const u16*)d_in[7], (u16*)d_out, (char*)d_ws);
}
```

```cpp
#include <hip/hip_runtime.h>
#include <hip/hip_bf16.h>
#include <utility>

typedef unsigned short u16;
typedef __attribute__((ext_vector_type(8))) __bf16 bf16x8;
typedef __attribute__((ext_vector_type(2))) __bf16 bf16x2;
typedef __attribute__((ext_vector_type(4))) short s16x4;
typedef __attribute__((ext_vector_type(8))) short s16x8;
typedef __attribute__((ext_vector_type(4))) float f32x4;
typedef __attribute__((ext_vector_type(16))) float f32x16;
typedef __attribute__((ext_vector_type(4))) unsigned u32x4;
typedef __attribute__((ext_vector_type(2))) unsigned u32x2;
#define LAS __attribute__((address_space(3)))
#define GAS __attribute__((address_space(1)))
#define DI __device__ __forceinline__

namespace tab {
constexpr double PI_ = 3.14159265358979323846;
constexpr double tsin(double x) { double t = x, s = x; for (int i = 1; i < 12; i++) { t *= -x * x / ((2 * i) * (2 * i + 1)); s += t; } return s; }
constexpr double tcos(double x) { double t = 1, s = 1; for (int i = 1; i < 12; i++) { t *= -x * x / ((2 * i - 1) * (2 * i)); s += t; } return s; }
constexpr double ccos(int n, int N) {
  n %= N; if (n < 0) n += N;
  if (2 * n > N) n = N - n;
  bool neg = false;
  if (4 * n > N) { n = N / 2 - n; neg = true; }
  double v = (8 * n > N) ? tsin(2 * PI_ * (N / 4 - n) / N) : tcos(2 * PI_ * n / N);
  return neg ? -v : v;
}
constexpr double csin(int n, int N) { return ccos(n - N / 4, N); }
constexpr u16 f2bf(double d) { float f = (float)d; unsigned u = __builtin_bit_cast(unsigned, f); u += 0x7fffu + ((u >> 16) & 1u); return (u16)(u >> 16); }
struct C576 { u16 c[576]; u16 g1[576]; u16 g2[576]; };
constexpr C576 mk576() { C576 r{}; for (int i = 0; i < 576; i++) { double c = ccos(i, 576); r.c[i] = f2bf(c); r.g1[i] = f2bf(c / 576.0); r.g2[i] = f2bf(2.0 * c / 576.0); } return r; }
constexpr C576 c576 = mk576();
struct Blk { u16 v[9216]; };
constexpr int fidx(int bc, int d) {
  int n = bc < 144 ? bc * d : (bc == 144 ? ((d & 1) == 0 ? 144 * d : 144 * d + 144) : (bc - 144) * d + 144);
  return n % 576;
}
constexpr Blk mkF(int idx) {
  Blk r{}; int at = idx >> 1, hf = idx & 1;
  for (int st = 0; st < 18; st++) for (int hh = 0; hh < 2; hh++) for (int j = 0; j < 8; j++) {
    int d = 288 * hf + 32 * (st >> 1) + 16 * (j >> 2) + 8 * hh + 2 * (j & 3) + (st & 1);
    for (int cc = 0; cc < 32; cc++) r.v[((st * 2 + hh) * 8 + j) * 32 + cc] = c576.c[fidx(32 * at + cc, d)];
  }
  return r;
}
template <int I> constexpr Blk fblk = mkF(I);
struct FTab { Blk b[18]; };
template <int... I> constexpr FTab mkFT(std::integer_sequence<int, I...>) { return FTab{{fblk<I>...}}; }
constexpr double bf2d(u16 b) { return (double)__builtin_bit_cast(float, (unsigned)b << 16); }
struct FCB { unsigned v[64]; };
constexpr FCB mkFC(int at) {
  FCB r{};
  for (int e = 0; e < 2; e++) for (int cc = 0; cc < 32; cc++) {
    double s = 0;
    for (int d = e; d < 576; d += 2) s += bf2d(c576.c[fidx(32 * at + cc, d)]);
    u16 hi = f2bf(s), lo = f2bf(s - bf2d(hi));
    r.v[cc * 2 + e] = (unsigned)hi | ((unsigned)lo << 16);
  }
  return r;
}
template <int I> constexpr FCB fcb = mkFC(I);
struct FCT { FCB b[9]; };
template <int... I> constexpr FCT mkFCT(std::integer_sequence<int, I...>) { return FCT{{fcb<I>...}}; }
struct GBlk { u16 v[4608]; };
constexpr GBlk mkG(int idx) {
  GBlk r{}; int step = idx % 5, q = (idx / 5) & 3, b = idx / 20;
  for (int row = 0; row < 32; row++) {
    int rho = 32 * step + row;
    if (rho >= 144) continue;
    int kc = 72 * b + (rho >> 1), ri = rho & 1;
    for (int c = 0; c < 144; c++) {
      int d = 144 * q + c;
      r.v[row * 144 + c] = kc == 0 ? c576.g1[(ri * 288 * d) % 576] : c576.g2[(kc * d + ri * 144) % 576];
    }
  }
  return r;
}
template <int I> constexpr GBlk gblk = mkG(I);
struct GTab { GBlk b[80]; };
template <int... I> constexpr GTab mkGT(std::integer_sequence<int, I...>) { return GTab{{gblk<I>...}}; }
struct C2048 { float c[2048]; };
constexpr C2048 mk2048() { C2048 r{}; for (int i = 0; i < 2048; i++) r.c[i] = (float)ccos(i, 2048); return r; }
constexpr C2048 c2048 = mk2048();
constexpr u16 trig(int n2048, int type) {
  int n = type == 0 ? n2048 : (type == 1 ? n2048 + 1536 : n2048 + 512);
  return f2bf(c2048.c[n & 2047]);
}
struct F64T { u16 v[4][2][3][64][8]; };
constexpr F64T mkF64(int which) {
  F64T r{};
  for (int j = 0; j < 4; j++) for (int t = 0; t < 2; t++) for (int ty = 0; ty < 3; ty++) for (int l = 0; l < 64; l++) for (int jj = 0; jj < 8; jj++) {
    int g = l >> 4, o = 16 * j + (l & 15);
    int k = which == 0 ? 32 * t + 16 * (g >> 1) + 8 * (jj >> 2) + 4 * (g & 1) + (jj & 3) : 32 * t + 16 * (jj >> 2) + 4 * g + (jj & 3);
    r.v[j][t][ty][l][jj] = trig(o * k * 32, ty);
  }
  return r;
}
struct F32T { u16 v[2][3][64][8]; };
constexpr F32T mkF32() {
  F32T r{};
  for (int ct = 0; ct < 2; ct++) for (int ty = 0; ty < 3; ty++) for (int l = 0; l < 64; l++) for (int jj = 0; jj < 8; jj++) {
    int g = l >> 4, o = 16 * ct + (l & 15), k = 16 * (jj >> 2) + 4 * g + (jj & 3);
    r.v[ct][ty][l][jj] = trig(o * k * 64, ty);
  }
  return r;
}
struct TWT { float v[4][4][64][4]; };
constexpr TWT mkTW(int which) {
  TWT r{};
  for (int j = 0; j < 4; j++) for (int n = 0; n < 4; n++) for (int l = 0; l < 64; l++) for (int i = 0; i < 4; i++) {
    int g = l >> 4, at = n >> 1;
    int a = which == 0 ? 16 * at + 4 * g + i : 16 * at + (l & 15);
    int d = which == 0 ? 16 * j + (l & 15) : 16 * j + 4 * g + i;
    float v = c2048.c[(a * d + ((n & 1) ? 1536 : 0)) & 2047];
    r.v[j][n][l][i] = which == 0 ? v : v * (1.0f / 2048.0f);
  }
  return r;
}
constexpr F64T f64a = mkF64(0), f64b = mkF64(1);
constexpr F32T f32t = mkF32();
constexpr TWT tw1 = mkTW(0), tw2 = mkTW(1);
struct Small { F64T a, b; F32T f; TWT t1, t2; FCT fc; };
}
#if defined(__HIP_DEVICE_COMPILE__)
__device__ __attribute__((aligned(128))) static const tab::FTab FT = tab::mkFT(std::make_integer_sequence<int, 18>{});
__device__ __attribute__((aligned(128))) static const tab::GTab GT = tab::mkGT(std::make_integer_sequence<int, 80>{});
__device__ __attribute__((aligned(128))) static const tab::Small ST = {tab::f64a, tab::f64b, tab::f32t, tab::tw1, tab::tw2, tab::mkFCT(std::make_integer_sequence<int, 9>{})};
#else
__device__ static const tab::FTab FT = {};
__device__ static const tab::GTab GT = {};
__device__ static const tab::Small ST = {};
#endif
typedef unsigned long long u64;
DI float bfr(float x) { return (float)(__bf16)x; }
DI unsigned pk2(float a, float b) { bf16x2 c; c[0] = (__bf16)a; c[1] = (__bf16)b; return __builtin_bit_cast(unsigned, c); }
DI float blo(unsigned u) { return __builtin_bit_cast(float, u << 16); }
DI float bhi(unsigned u) { return __builtin_bit_cast(float, u & 0xffff0000u); }
DI f32x16 mfma32(u32x4 a, u32x4 b, f32x16 c) { return __builtin_amdgcn_mfma_f32_32x32x16_bf16(__builtin_bit_cast(bf16x8, a), __builtin_bit_cast(bf16x8, b), c, 0, 0, 0); }
DI f32x4 mfma16(u32x4 a, u32x4 b, f32x4 c) { return __builtin_amdgcn_mfma_f32_16x16x32_bf16(__builtin_bit_cast(bf16x8, a), __builtin_bit_cast(bf16x8, b), c, 0, 0, 0); }
DI u32x2 trd(LAS char* p) { return __builtin_bit_cast(u32x2, __builtin_amdgcn_ds_read_tr16_b64_v4i16((LAS s16x4*)p)); }
DI u32x4 trd2(LAS char* p, int off2) { u32x2 a = trd(p), b = trd(p + off2); u32x4 r; r[0] = a[0]; r[1] = a[1]; r[2] = b[0]; r[3] = b[1]; return r; }
template <int C> DI float dppadd(float v) { return v + __builtin_bit_cast(float, __builtin_amdgcn_mov_dpp(__builtin_bit_cast(int, v), C, 0xf, 0xf, true)); }
DI u32x4 mkdesc(const void* p, unsigned bytes) { u64 a = (u64)p; return u32x4{(unsigned)a, (unsigned)(a >> 32) & 0xffffu, bytes, 0x00020000u}; }
typedef __amdgpu_buffer_rsrc_t rsrc_t;
DI rsrc_t mkrsrc(const void* p, unsigned bytes) { return __builtin_amdgcn_make_buffer_rsrc((void*)p, 0, bytes, 0x00020000); }
DI void st16(rsrc_t r, unsigned voff, unsigned soff, u32x4 v) {
  __builtin_amdgcn_raw_buffer_store_b128(v, r, voff, soff, 16);
  asm volatile("s_nop 1" ::"v"(v) : "memory");
}
DI u32x4 ld16(rsrc_t r, unsigned voff, unsigned soff) { return __builtin_amdgcn_raw_buffer_load_b128(r, voff, soff, 16); }
#define GLDS(d, voff, sbase, simm, lbase, limm) do { unsigned t_; \
  asm volatile("s_add_i32 m0, %1, %2\n\ts_add_i32 %0, %3, %4\n\tbuffer_load_dwordx4 %5, %6, %0 offen lds" \
               : "=&s"(t_) : "s"(lbase), "i"(limm), "s"(sbase), "i"(simm), "v"(voff), "s"(d) : "memory", "scc"); } while (0)
#define GLDS1(d, voff, soff, lb) asm volatile("s_mov_b32 m0, %0\n\tbuffer_load_dword %1, %2, %3 offen lds" ::"i"(lb), "v"(voff), "s"(d), "s"(soff) : "memory")
#define WAITV(n) asm volatile("s_waitcnt vmcnt(%0)" ::"i"(n) : "memory")
#define WAITL(n) asm volatile("s_waitcnt lgkmcnt(%0)" ::"i"(n) : "memory")
#define PIN(x) asm volatile("" : "+v"(x))
#define BARX() asm volatile("s_barrier" ::: "memory")
#define SB() __builtin_amdgcn_sched_barrier(0)
#define AI __attribute__((always_inline))
#if defined(__HIP_DEVICE_COMPILE__)
#define NOPK __attribute__((target("no-packed-fp32-ops")))
#else
#define NOPK
#endif
template <int N> struct IC { static constexpr int v = N; };
template <class F, int... I> DI void sfor_(F&& f, std::integer_sequence<int, I...>) { (f(IC<I>{}), ...); }
template <int N, class F> DI void sfor(F&& f) { sfor_(f, std::make_integer_sequence<int, N>{}); }

#define BLD(op, dst, d, voff, soff, pol) asm volatile("buffer_load_" op " %0, %1, %2, %3 offen" pol : "=v"(dst) : "v"(voff), "s"(d), "s"(soff) : "memory")
DI float asf(unsigned u) { return __builtin_bit_cast(float, u); }
DI unsigned asu(float f) { return __builtin_bit_cast(unsigned, f); }
DI float swadd32(float a, float b) { auto s = __builtin_amdgcn_permlane32_swap(asu(a), asu(b), false, false); const unsigned x = s[0], y = s[1]; return asf(x) + asf(y); }
DI float swadd16(float a, float b) { auto s = __builtin_amdgcn_permlane16_swap(asu(a), asu(b), false, false); const unsigned x = s[0], y = s[1]; return asf(x) + asf(y); }
DI float half_sum(float v) {
  v = dppadd<0xB1>(v); v = dppadd<0x4E>(v); v = dppadd<0x141>(v); v = dppadd<0x140>(v);
  return swadd16(v, v);
}

constexpr int SLOT = 36864, SP = 4 * SLOT, LDSZ = 163840;
constexpr int SP_T = SP + 8192, SP_CS = SP_T + 4608, SP_RS = SP_T + 6656, SP_PF = SP_T + 7168;
constexpr unsigned WS_E = 0, WS_F1 = 1024, WS_FH = 2048, WS_FA = 3072, WS_ERR = 5120, WS_ST = 8192, WS_F0 = 24576, WS_FZ = 32768;
constexpr unsigned WS_CS = 40960;
constexpr unsigned WS_H = WS_CS + 3 * 36864;
constexpr unsigned WS_XS = WS_H + 64u * 144 * 2048;
constexpr unsigned WS_Z = WS_XS + 576u * 4096;
constexpr unsigned WS_WT = WS_Z + 576u * 4096;
constexpr unsigned WS_P = WS_WT + 289u * 8192;
constexpr unsigned WS_END = WS_P + 4096u * 5120;
constexpr int SPIN = 200000;
constexpr unsigned OOB = 0x80000000u;
DI void put_flag(rsrc_t rw, bool on, unsigned off, unsigned tag) { __builtin_amdgcn_raw_buffer_store_b32(tag, rw, on ? 0u : OOB, off, 16); }
DI void put_flag8(rsrc_t rw, unsigned t, unsigned off, unsigned tag) { __builtin_amdgcn_raw_buffer_store_b32(tag, rw, t < 8 ? t * 1024 : OOB, off, 16); }
DI bool bad4(u32x4 v, unsigned tag) { return __builtin_amdgcn_ballot_w64(v[0] != tag || v[1] != tag || v[2] != tag || v[3] != tag) != 0; }
DI void poll4(rsrc_t rw, u32x4 dws, u32x4 v, unsigned voff, unsigned soff, unsigned tag) {
  for (int it = 0; bad4(v, tag); it++) {
    if (it >= SPIN) { put_flag(rw, true, WS_ERR, 1); break; }
    __builtin_amdgcn_s_sleep(4);
    BLD("dwordx4", v, dws, voff, soff, " sc1");
    asm volatile("s_waitcnt vmcnt(0)" : "+v"(v)::"memory");
  }
}
#define DIST1 4
#define RING1 5
#define DIST2 4
#define RING2 5
#define ABL 0
#define PREF 1
#define PREFC 1
#define PREF2 103
__global__ __launch_bounds__(512, 2) NOPK void kmain(const u16* __restrict__ x, const u16* __restrict__ wr, const u16* __restrict__ wi,
                                                     const u16* __restrict__ w1, const u16* __restrict__ b1, const u16* __restrict__ w2,
                                                     const u16* __restrict__ b2, u16* __restrict__ out, char* __restrict__ ws) {
  extern __shared__ __attribute__((aligned(16))) char smem_[];
  LAS char* smem = (LAS char*)smem_;
  const int tid = threadIdx.x, lane = tid & 63;
  const int wv = __builtin_amdgcn_readfirstlane(tid >> 6);
  auto body = [&](auto SETC) AI {
  constexpr int SET = decltype(SETC)::v;
  const int id = blockIdx.x, xcd = id & 7, slot = id >> 3;
  const int rt = (xcd >> 2) * 8 + (slot >> 2), bb = xcd & 3, q = slot & 3, nt = bb * 4 + q, lid = rt * 16 + nt;
  const int aq = rt >> 1, bh = rt & 1, c = wv & 3;
  const int fl0 = (nt & 1) * 32, flh = (nt & 1) ? 0 : 256;
  const bool has_dft = nt < 9;
  const rsrc_t rw = mkrsrc(ws, WS_END);
  const u32x4 dws = mkdesc(ws, WS_END), dx = mkdesc(x, 2048 * 1152), dw1 = mkdesc(w1, 576 * 9216), dw2 = mkdesc(w2, 4608 * 1152);
  const int r = lane & 31, hh = lane >> 5;
  unsigned ev, xc;
  u32x4 xs[2];
  asm volatile("s_load_dword %0, %1, 0x0" : "=s"(ev) : "s"(ws) : "memory");
  if (SET == 1 && PREFC) {
    u64 pc; unsigned len;
    asm volatile("s_getpc_b64 %0\n.Lkpc:\n\ts_mov_b32 %1, .Lkend-.Lkpc" : "=s"(pc), "=s"(len));
    GLDS1(mkdesc((const void*)pc, len < 0x40000u ? len : 0u), lane * 128u + (c * 32 + slot) * 8192u, 0, SP_PF);
  }
  if (SET == 0) {
    const int tr = 8 * nt + 2 * c;
    const unsigned ro = (4 * aq + (tr >> 5) + 32 * (32 * bh + (tr & 31))) * 1152u;
    BLD("dwordx4", xs[0], dx, hh * 36864 + r * 16, ro, "");
    BLD("dwordx4", xs[1], dx, hh * 36864 + r * 16, ro, " offset:512");
    BLD("dword", xc, dx, hh * 36864 + r * 4, ro, " offset:1024");
  }
  const unsigned xvo = (4 * aq + (wv >> 1) + 32 * (32 * bh + 16 * (wv & 1) + (lane >> 3))) * 1152u + (((lane & 7) ^ (lane >> 4)) * 16);
  const unsigned xvo1 = (xvo ^ 64) + 8 * 32 * 1152;
  const unsigned xlb = 2 * SLOT + wv * 10240;
#pragma unroll
  for (int n = 0; n < 5; n++) { GLDS(dx, xvo, 0, 128 * n, xlb, 1024 * n); GLDS(dx, xvo1, 0, 128 * n, xlb, 5120 + 1024 * n); }
  const unsigned wvo = (8 * c + ((lane >> 2) & 7)) * 9216u + hh * 64 + (lane & 3) * 16;
  const unsigned wso = (288 * nt + fl0) * 2, wlb = c * 1024;
  const u32x4 dft = mkdesc(&FT, has_dft ? sizeof(FT) : 0);
  const u32x4 dm = c < 2 ? dw1 : dft;
  const unsigned mvo = c < 2 ? (144 * c + (lane >> 2)) * 9216u + (lane & 3) * 16 : lane * 16;
  const unsigned mso = c < 2 ? (288 * nt + flh) * 2 : nt * 36864 + (c & 1) * 9216;
  const unsigned mstep = c < 2 ? 16 * 9216 : 1024, mhf = c < 2 ? 288 * 9216 : 18432, mlb = c * 9216;
#pragma unroll
  for (int n = SET; n < 9; n += 2) GLDS(dm, mvo, mso + n * mstep, 0, mlb, 1024 * n);
  const int cu = (xcd >> 2) * 32 + slot, cg = 18 * bb + cu / 3, g3 = cu % 3;
  u32x4 uu[3];
#pragma unroll
  for (int n = 0; n < 3; n++) BLD("dwordx4", uu[n], dw1, cu < 54 ? (lane >> 3) * 9216u + (lane & 7) * 16 : OOB, (192 * g3 + 24 * wv + 8 * n) * 9216 + cg * 128, "");
  asm volatile("s_waitcnt lgkmcnt(0)" : "+s"(ev)::"memory");
  const unsigned tag = ev * 0x9E3779B1u + 0x7F4A7C15u;
  if (SET == 0) {
    asm volatile("s_waitcnt vmcnt(18)" : "+v"(xs[0]), "+v"(xs[1]), "+v"(xc)::"memory");
    float s = blo(xc) + bhi(xc);
#pragma unroll
    for (int i = 0; i < 8; i++) { const unsigned w = xs[i >> 2][i & 3]; s += blo(w) + bhi(w); }
    const float mean = half_sum(s) * (1.0f / 576.0f);
    float v = 0.f;
#pragma unroll
    for (int i = 0; i < 9; i++) { const unsigned w = i < 8 ? xs[(i >> 2) & 1][i & 3] : xc; const float d0 = blo(w) - mean, d1 = bhi(w) - mean; v = __builtin_fmaf(d0, d0, v); v = __builtin_fmaf(d1, d1, v); }
    const float var = half_sum(v) * (1.0f / 576.0f);
    __builtin_amdgcn_raw_buffer_store_b64(u32x2{pk2(mean, __builtin_sqrtf(bfr(bfr(var) + 1e-5f))), tag}, rw, r == 0 ? hh * 8u : OOB, WS_ST + (rt * 128 + 8 * nt + 2 * c) * 8, 16);
  }
  const int q4 = (lane & 15) >> 2, p4 = lane & 3, g = lane >> 4, l15 = lane & 15;
  const unsigned hoff = (r >> 4) * 1024 + hh * 256 + (r & 15) * 16;
  const unsigned hrow = WS_H + (4 * rt + c) * 144 * 2048;
  const unsigned hso = hrow + 9 * nt * 2048;
  unsigned w2o[9];
  constexpr auto ftile = [](int ks) constexpr {
    const int qq = ks < 12 ? ks / 3 : ks < 20 ? (ks - 12) / 2 : (ks - 20) / 4, e = ks < 12 ? ks % 3 : ks < 20 ? 3 + (ks & 1) : 5 + (ks & 3);
    return 9 * qq + ((qq & 1) ? e : (e == 0 ? 8 : e - 1));
  };
  auto w2off = [&](int j) AI {
    const unsigned t = ftile(4 * j) | ftile(4 * j + 1) << 8 | ftile(4 * j + 2) << 16 | ftile(4 * j + 3) << 24;
    return (36 * bb + ((t >> (8 * c)) & 255)) * 36864 + q * 288;
  };
  auto w2stage = [&](unsigned so, int j, int n) AI { GLDS(dw2, w2o[n], so, 0, mlb, (j & 3) * SLOT + 1024 * n); };
  int cnt = 0, mark[12] = {};
  constexpr int HW = 8;
  u32x4 hb[HW][2];
  int mh[36];
  auto hload = [&](int ks) AI {
    const unsigned so = hrow + (36 * bb + ftile(ks)) * 2048;
    BLD("dwordx4", hb[ks % HW][0], dws, lane * 16, so, " sc1");
    BLD("dwordx4", hb[ks % HW][1], dws, lane * 16, so, " offset:1024 sc1");
    cnt += 2; mh[ks] = cnt;
  };
  u32x4 pf;
  int mpf, mst, mx;
  const unsigned pbase = WS_P + ((rt * 4 + q) * 64 + c) * 3072;
  const u32x4 dst = mkdesc(&ST, sizeof(ST));
  const int e2 = rt * 7 + nt - 9;
  const bool has2 = nt >= 9 && e2 < 33;
  const int kc[2] = {lid, 256 + e2};
  u32x4 fa[2][3], fb[2][3], f32[2][3], xx[2][8], wt[2][2];
  auto xload = [&](int z, int i) AI {
    const bool cplx = kc[z] != 0 && kc[z] != 288;
    const unsigned vo = q4 * 256 + g * 64 + p4 * 16;
    if (i < 8) BLD("dwordx4", xx[z][i], dws, (i < 4 || cplx) ? vo : OOB, WS_XS + (kc[z] == 288 ? 1 : 2 * kc[z]) * 4096 + i * 1024, " sc1");
    else BLD("dwordx4", wt[z][i - 8], dws, lane * 16, WS_WT + kc[z] * 8192 + (c * 2 + i - 8) * 1024, " sc1");
  };
  auto tload = [&](u32x4 (&f)[2][3], unsigned so, int i) AI { BLD("dwordx4", f[i >> 1][i & 1], dst, lane * 16, so + (i >> 1) * 3072 + (i & 1) * 1024, ""); };

  {
    constexpr int D = DIST1, R = RING1;
    const unsigned tq = q4 * 64 + (g & 1) * 32 + p4 * 8;
    LAS char* const tbP = smem + hh * 1024 + tq + SET * 512;
    LAS char* const tbM = smem + hh * 512 + tq + SET * 18432;
    const unsigned xa = 2 * SLOT + (4 * c + (r >> 3)) * 5120 + (r & 7) * 128 + ((hh ^ ((r >> 1) & 7)) * 16);
    u32x4 xf[36], ring[R], pm[2], sf, tf, xq[4];
    u32x2 sg, fc, wl[8];
    u32x4 cq[3];
    unsigned bia, w2s, wvv;
    f32x16 acc[6];
    f32x4 cr[2];
    float cs[8], gt[3][8], ge[3][8], k0, k1, sdf;
    u32x4 go[3];
    LAS char* bs[11];
    int mw, mt[5];
    const f32x16 zero = {0, 0, 0, 0, 0, 0, 0, 0, 0, 0, 0, 0, 0, 0, 0, 0};
    constexpr auto isM = [](int k) constexpr { return k == 0 || k == 2; };
    constexpr auto tile = [](int k) constexpr { return k < 4 ? (k & 1) : (k >> 1); };
    constexpr auto half = [](int k) constexpr { return k < 4 ? (k >> 1) : (k & 1); };
    constexpr auto slotof = [](int k) constexpr { return k < 2 ? k : (k + 2) & 3; };
    auto fetch = [&](int F) AI { const int k = F / 18, st = F % 18; ring[F % R] = trd2(bs[k] + st * (isM(k) ? 1024 : 2048), 256); };
    auto xread = [&](int i) AI { if (i < 36) xf[i] = *(LAS u32x4*)(smem + (xa ^ ((i & 3) * 32)) + ((i >> 2) % 5) * 1024); };
    auto perm = [&](int T) AI {
      const int st = T % 18, b = 18 * half(T / 18) + (st & ~1); const unsigned sel = (st & 1) ? 0x07060302u : 0x05040100u;
      u32x4 o;
      o[0] = __builtin_amdgcn_perm(xf[b][1], xf[b][0], sel); o[1] = __builtin_amdgcn_perm(xf[b][3], xf[b][2], sel);
      o[2] = __builtin_amdgcn_perm(xf[b + 1][1], xf[b + 1][0], sel); o[3] = __builtin_amdgcn_perm(xf[b + 1][3], xf[b + 1][2], sel);
      PIN(o); pm[st & 1] = o;
    };
    auto gstep = [&](int t, int u, int s, int z) AI {
      float* T_ = gt[z]; float* E_ = ge[z]; const int i = s >> 1;
      auto v = [&](int n) AI { return acc[t][8 * u + n]; };
      if (s == 0) { T_[0] = v(0) * v(0); PIN(T_[0]); }
      if ((s & 1) == 0) {
        if (i < 8) { T_[i] = v(i) * __builtin_fmaf(T_[i], k1, k0); PIN(T_[i]); }
        if (i >= 1) { E_[i - 1] = __builtin_amdgcn_rcpf(__builtin_fmaf(E_[i - 1], sdf, sdf)); PIN(E_[i - 1]); }
      } else {
        if (i < 8) { E_[i] = __builtin_amdgcn_exp2f(T_[i]); PIN(E_[i]); }
        if (i >= 1) { T_[i - 1] = v(i - 1) * E_[i - 1]; if ((i & 1) == 0) { unsigned w_ = pk2(T_[i - 2], T_[i - 1]); PIN(w_); go[z][(i >> 1) - 1] = w_; } else PIN(T_[i - 1]); }
        if (i + 1 < 8) { T_[i + 1] = v(i + 1) * v(i + 1); PIN(T_[i + 1]); }
      }
      if (s == 17 && (ABL & 2)) { PIN(go[z]); mt[t] = cnt; } else if (s == 17) { st16(rw, hoff, hso + (t == 0 ? flh : fl0 + 64 * (t - 1) + 32 * SET) * 64 + u * 512, go[z]); mt[t] = ++cnt; }
    };
    const int gl = 36 * c + (lane < 35 ? lane : 35);
    auto gather = [&]() AI {
      BLD("dwordx2", sg, dws, r * 8, WS_ST + (rt * 128 + c * 32) * 8, " sc1");
#pragma unroll
      for (int i = 0; i < 3 * (1 - SET); i++) BLD("dwordx4", cq[i], dws, gl * 16, WS_CS + i * 36864 + nt * 2304, " sc1");
    };
    WAITV(4 - SET); BARX();
    bs[0] = tbM; PIN(bs[0]);
    xread(0); xread(1);
#pragma unroll
    for (int F = 0; F < D; F++) fetch(F);
    xread(2); xread(3);
    if (SET == 1) perm(0);
    SB();
    sfor<180>([&](auto TC_) AI {
      constexpr int T = decltype(TC_)::v, k = T / 18, st = T % 18, F = T + D, t = tile(k);
      if constexpr (T == 5) { WAITL(2); BARX(); }
      if constexpr (T == 9) { }
      if constexpr (T == 14) { }
      if constexpr (T == 19) { }
      if constexpr (T == 32) { }
      if constexpr (T == 26) { }
      if constexpr (T == 158) { }
      if constexpr (T == 159) { }
      if constexpr (T == 162) { }
      if constexpr (T == 166) { }
      if constexpr (T == 170) { }
      if constexpr (T == 171) { }
      if constexpr (T == 174) { }
      if constexpr (T == 176) { }
      if constexpr (T == 177) { }
      if constexpr (T == 65) { if (SET == 1) WAITL(4); BARX(); }
      if constexpr (F % 18 == 0) {
        constexpr int kb = F / 18;
        if (kb <= 5 || (kb & 1) == SET) WAITV(cnt - mark[kb]);
        else if (kb == 8 || kb == 10) WAITV(cnt - mt[kb / 2 - 3]);
        if (kb <= 2 || kb == 10) WAITL(0); else if (kb == 3) WAITL(2); else if (kb == 4) WAITL(4);
        if (kb == 1) { } if (kb == 2) { } BARX();
        if (kb == 7 && SET == 0) { put_flag8(rw, tid, WS_F0 + lid * 4, tag); cnt++; }
        if ((kb == 8 || kb == 10) && SET == 0) { put_flag(rw, tid == 0, (kb == 8 ? WS_FA : WS_F1) + lid * 4, tag); cnt++; }
        if constexpr (kb < 10) { bs[kb] = (isM(kb) ? tbM : tbP) + slotof(kb) * SLOT; PIN(bs[kb]); }
      }
      if constexpr (F < 180) fetch(F);
      if constexpr (SET == 1 && isM(k)) {
        const int a = (st & 1) ? 5 : 0;
        acc[a] = mfma32(pm[st & 1], ring[T % R], (k == 0 && st < 2) ? zero : acc[a]);
        if (st < 17) perm(T + 1);
      } else {
        acc[t] = mfma32(ring[T % R], xf[18 * half(k) + st], (half(k) == 0 && st == 0) ? zero : acc[t]);
      }
      if (SET == 1 && T == 35) perm(36);
      if constexpr (T < 4) { xread(4 + 4 * T); xread(5 + 4 * T); xread(6 + 4 * T); xread(7 + 4 * T); }
      if constexpr (T >= 32 && T < 38) { xread(20 + 3 * (T - 32)); xread(21 + 3 * (T - 32)); xread(22 + 3 * (T - 32)); }
      constexpr int xp = T == 5 ? 0 : T == 7 ? 1 : T == 8 ? 2 : T == 10 ? 3 : T == 11 ? 4 : T == 13 ? 5 : T == 24 ? 6 : T == 25 ? 7 : -1;
      if constexpr (xp >= 0) { GLDS(dx, (xp & 1) ? xvo1 : xvo, 0, 640 + 128 * (xp >> 1), xlb, (xp & 1) * 5120 + 1024 * (xp >> 1)); mark[2] = ++cnt; }
      if constexpr (T < 5 && 2 * T + 1 - SET < 9) { GLDS(dw1, wvo, wso, (2 * T + 1 - SET) * 32 * 9216, wlb, SLOT + 4096 * (2 * T + 1 - SET)); mark[1] = ++cnt; }
      if constexpr (F >= 18 && F < 180) {
        constexpr int kb = F / 18, fs = F % 18;
        constexpr int ks = kb <= 2 ? (fs < 9 ? kb + 1 : -1) : kb == 3 ? (fs < 9 ? 4 : 5) : (fs < 9 ? kb + 2 : -1);
        constexpr int o = (SET + ks) & 1;
        constexpr int n = ks <= 5 ? ((fs % 9) % 2 == o ? fs % 9 : 9) : (o == 0 ? fs : 9);
        if constexpr (ks >= 2 && n < 9 && !((ABL & 1) && ks >= 5 && ks < 10)) {
          if constexpr (ks == 2) GLDS(dm, mvo, mso + mhf + n * mstep, 0, mlb, 1024 * n);
          else if constexpr (ks <= 9) GLDS(dw1, wvo, wso, half(ks) * 288 * 9216 + (tile(ks) - 1) * 128 + n * 32 * 9216, wlb, slotof(ks) * SLOT + 4096 * n);
          else {
            const int e = 64 * n + lane; w2o[n] = (e / 18) * 1152 + (e % 18) * 16;
            if (n == 0) w2s = w2off(ks - 10);
            w2stage(w2s, ks - 10, n);
          }
          mark[ks] = ++cnt;
        }
      }
      if constexpr (T == 10) asm volatile("s_waitcnt vmcnt(%3)" : "+v"(uu[0]), "+v"(uu[1]), "+v"(uu[2]) : "i"(cnt + 1 - SET) : "memory");
      if constexpr (T == 10 || T == 11) {
#pragma unroll
        for (int i = 2 * (T - 10); i < 2 * (T - 10) + 2; i++) {
          cs[2 * i] = blo(uu[0][i]) + blo(uu[1][i]) + blo(uu[2][i]); cs[2 * i + 1] = bhi(uu[0][i]) + bhi(uu[1][i]) + bhi(uu[2][i]);
          PIN(cs[2 * i]); PIN(cs[2 * i + 1]);
        }
      }
      if constexpr (T == 12) {
#pragma unroll
        for (int i = 0; i < 4; i++) { cs[i] = swadd32(cs[i], cs[i + 4]); PIN(cs[i]); }
      }
      if constexpr (T == 13) {
        const float a = dppadd<0x128>(swadd16(cs[0], cs[2])), b = dppadd<0x128>(swadd16(cs[1], cs[3]));
        *(LAS float*)(smem + SP_CS + (lane & 7) * 256 + (lane >> 3) * 32 + wv * 4) = (lane & 8) ? b : a;
      }
      if constexpr (SET == 0 && T == 14) { cr[0] = *(LAS f32x4*)(smem + SP_CS + lane * 32); cr[1] = *(LAS f32x4*)(smem + SP_CS + lane * 32 + 16); }
      if constexpr (SET == 0 && T == 16) {
        const float s = ((cr[0][0] + cr[0][1]) + (cr[0][2] + cr[0][3])) + ((cr[1][0] + cr[1][1]) + (cr[1][2] + cr[1][3]));
        __builtin_amdgcn_raw_buffer_store_b64(u32x2{pk2(s, s - bfr(s)), tag}, rw, (c == 0 && cu < 54) ? lane * 8u : OOB, WS_CS + g3 * 36864 + cg * 512, 16);
        cnt++;
      }
      if constexpr (T == 41) {
        gather();
        if (SET == 0) BLD("dword", bia, mkdesc(b1, 9216), gl * 4, 576 * nt, "");
        else BLD("dwordx2", fc, mkdesc(&ST.fc, 2304), r * 8, (has_dft ? nt : 0) * 256, "");
        cnt += SET == 0 ? 5 : 2;
      }
      if constexpr (T == 48) {
        auto gwait = [&]() AI {
          if (SET == 0) asm volatile("s_waitcnt vmcnt(4)" : "+v"(sg), "+v"(cq[0]), "+v"(cq[1]), "+v"(cq[2]), "+v"(bia)::"memory");
          else asm volatile("s_waitcnt vmcnt(0)" : "+v"(sg), "+v"(fc)::"memory");
          unsigned m = sg[1] ^ tag;
          if (SET == 0) m |= (cq[0][1] ^ tag) | (cq[0][3] ^ tag) | (cq[1][1] ^ tag) | (cq[1][3] ^ tag) | (cq[2][1] ^ tag) | (cq[2][3] ^ tag);
          return __builtin_amdgcn_ballot_w64(m != 0) != 0;
        };
        for (int it = 0; gwait(); it++) {
          if (it >= SPIN) { put_flag(rw, true, WS_ERR, 2); break; }
          __builtin_amdgcn_s_sleep(4);
          gather();
          WAITV(0);
        }
        if (SET == 0) {
          *(LAS u32x4*)(smem + SP_T + gl * 32) = u32x4{cq[0][0], cq[1][0], cq[2][0], bia & 0xffffu};
          *(LAS u32x4*)(smem + SP_T + gl * 32 + 16) = u32x4{cq[0][2], cq[1][2], cq[2][2], bia >> 16};
        }
      }
      if constexpr (T == 51) {
        const unsigned nm = hh ? 0u : ((sg[0] & 0xffffu) ^ 0x8000u) * 0x10001u;
        sf = u32x4{nm, nm, nm, hh ? 0u : sg[0] >> 16};
        sdf = bhi(sg[0]);
        PIN(sf); PIN(sdf);
      }
      if constexpr (SET == 1 && T == 52) *(LAS float*)(smem + SP_RS + c * 128 + r * 4) = __builtin_amdgcn_rcpf(sdf);
      if constexpr (T == (SET == 0 ? 52 : 70)) {
        const float rstd = __builtin_amdgcn_rcpf(sdf), c0 = -2.0f * 0.7978845608028654f * 1.4426950408889634f, c1 = c0 * 0.044715f;
        k0 = c0 * rstd; k1 = c1 * rstd * rstd * rstd;
        PIN(k0); PIN(k1);
      }
      if (SET == 0 && T == 51) tf = *(LAS u32x4*)(smem + SP_T + (flh + r) * 16);
      if (SET == 0 && T == 53) acc[0] = mfma32(tf, sf, acc[0]);
      if constexpr (!isM(k)) {
        if (half(k) == 1 && st == 15) tf = *(LAS u32x4*)(smem + SP_T + (fl0 + 64 * (t - 1) + 32 * SET + r) * 16);
        if (half(k) == 1 && st == 17) acc[t] = mfma32(tf, sf, acc[t]);
      } else if constexpr (SET == 1 && k == 2 && st == 17) {
        acc[0] = mfma32(sf, u32x4{fc[0], 0, 0, 0}, acc[0]);
        acc[5] = mfma32(sf, u32x4{fc[1], 0, 0, 0}, acc[5]);
      }
      constexpr int TW = 26;
      if constexpr (!(ABL & 16) && SET == 0 && ((T >= TW && T < TW + 4) || (T >= TW + 17 && T < TW + 21))) {
        const int wset = 8 * (lid >> 2) + (lane & 7), wp = 5 * c + (lane >> 3), wcol = 72 * q + (q == 3) + 4 * wp;
        const bool on = lane < 40 && wp < 19 && wcol <= 285;
        if constexpr (T < TW + 8) {
          constexpr int i = T - TW;
          if (i == 0) { wvv = ((16 * (wset >> 7) + (wset & 15) + 256 * ((wset >> 4) & 7)) * 289 + wcol) * 2u; PIN(wvv); wvv = on ? wvv : OOB; }
          BLD("dwordx2", wl[2 * i], mkdesc(wr, 2048 * 578), wvv, i * 64 * 578, "");
          BLD("dwordx2", wl[2 * i + 1], mkdesc(wi, 2048 * 578), wvv, i * 64 * 578, "");
          cnt += 2; mw = cnt;
        } else {
          constexpr int j = T - TW - 17;
          if (j == 0) {
            asm volatile("s_waitcnt vmcnt(%8)" : "+v"(wl[0]), "+v"(wl[1]), "+v"(wl[2]), "+v"(wl[3]), "+v"(wl[4]), "+v"(wl[5]), "+v"(wl[6]), "+v"(wl[7]) : "i"(cnt - mw) : "memory");
            wvv = wcol * 8192u + wset * 16; PIN(wvv); wvv = on ? wvv : OOB;
          }
          u32x4 o;
#pragma unroll
          for (int i = 0; i < 4; i++) o[i] = __builtin_amdgcn_perm(wl[2 * i + 1][j >> 1], wl[2 * i][j >> 1], (j & 1) ? 0x07060302u : 0x05040100u);
          st16(rw, wvv, WS_WT + j * 8192, o);
          mw = ++cnt;
        }
      }
      if constexpr (SET == 0) {
        if constexpr (T == 161) { BLD("dwordx4", pf, dws, 0, WS_FA + (lid >> 2) * 16, " sc1"); mpf = ++cnt; }
        if constexpr (T == 168) {
          asm volatile("s_waitcnt vmcnt(%1)" : "+v"(pf) : "i"(cnt - mpf) : "memory");
          poll4(rw, dws, pf, 0, WS_FA + (lid >> 2) * 16, tag);
        }
        if constexpr (T >= 168 && (T - 168) % 3 == 0) hload((T - 168) / 3);
      }
      if constexpr (SET == 1) {
        if constexpr (T >= 168 && T < 176) { tload(T < 172 ? fa : f32, T < 172 ? c * 6144 : 49152, T & 3); cnt++; }
      }
      if constexpr (PREF && (SET == 0 ? (T == 77 || T == 113) : T == 107)) {
        const int u = wv * 32 + (rt & 7) * 4 + q;
        const u32x4 dp = u < 162 ? dw2 : u < 185 ? mkdesc(&GT, sizeof(GT)) : dst;
        GLDS1(dp, (c >> 1) == (T == 113) ? lane * 128u : OOB, u < 162 ? bb * 1327104 + u * 8192 : u < 185 ? bb * 184320 + (u - 162) * 8192 : (u - 185) * 8192, SP_PF);
        cnt++;
      }
      if constexpr (SET == 0) {
        if constexpr (T >= 55 && T < 73) gstep(0, 0, T - 55, 2);
        if constexpr (T >= 73 && T < 109 && ((T - 73) & 1) == 0) gstep(0, 1, (T - 73) >> 1, 2);
      } else if constexpr (T >= 55 && T < 61) {
        constexpr int u = (T - 55) / 3, ph = (T - 55) % 3;
        const int bc = 32 * nt + r;
        const float b1c = bc == 144 ? 0.f : 1.f, b2c = bc < 144 ? -1.f : 1.f, a2c = -b1c * b2c;
        if (ph == 0) { cr[0] = *(LAS f32x4*)(smem + SP_RS + c * 128 + u * 64 + hh * 16); cr[1] = *(LAS f32x4*)(smem + SP_RS + c * 128 + u * 64 + 32 + hh * 16); }
        else {
          float o1[4], o2[4];
#pragma unroll
          for (int i = 0; i < 4; i++) {
            const float e = acc[0][8 * u + 4 * (ph - 1) + i] * cr[ph - 1][i], o = acc[5][8 * u + 4 * (ph - 1) + i] * cr[ph - 1][i];
            o1[i] = __builtin_fmaf(b1c, o, e); o2[i] = __builtin_fmaf(a2c, e, b2c * o);
          }
          LAS char* p = smem + r * 272 + (2 * u + hh) * 64 + c * 16 + (ph - 1) * 8;
          *(LAS u32x2*)p = u32x2{pk2(o1[0], o1[1]), pk2(o1[2], o1[3])};
          *(LAS u32x2*)(p + 32 * 272) = u32x2{pk2(o2[0], o2[1]), pk2(o2[2], o2[3])};
        }
      } else if constexpr (T == 65) {
#pragma unroll
        for (int i = 0; i < 4; i++) xq[i] = *(LAS u32x4*)(smem + (16 * c + 4 * i + g) * 272 + l15 * 16);
      } else if constexpr (T >= 67 && T < 71) {
        const int bc = 32 * nt + 16 * (c & 1) + 4 * (T - 67) + g;
        const int p1 = 2 * bc - (bc > 144 ? 287 : 0);
        int p2 = (bc < 144 ? 576 : 865) - 2 * bc; p2 = bc == 0 ? 1 : p2; p2 = bc == 144 ? 289 : p2;
        unsigned vo = (c < 2 ? p1 : p2) * 4096u + l15 * 16; PIN(vo);
        st16(rw, (has_dft && !(ABL & 32)) ? vo : OOB, WS_XS + bh * 2048 + aq * 256, xq[T - 67]);
        cnt++;
      }
      if constexpr (T >= 73 && (ABL & 4)) { if constexpr ((T - 73) % 36 == 0) PIN(acc[(T - 73) / 36 + 1]); } else if constexpr (T >= 73) {
        const int pt = (T - 73) / 36 + 1, s = (T - 73) % 36;
        if (s < 18) gstep(pt, 0, s, 0);
        if (s >= 16 && s < 34) gstep(pt, 1, s - 16, 1);
      }
      SB();
    });
#pragma unroll
    for (int s = 0; s < 18; s++) {
      if (s % 4 == 1 && s < 16 && SET == 0) hload(4 + s / 4);
      if (s == 6 && SET == 1) { BLD("dwordx4", pf, dws, lane * 16, WS_F0 + xcd * 1024, " sc1"); mpf = ++cnt; }
      gstep(4, 0, s, 0); gstep(4, 1, s, 1);
    }
    mst = cnt;
  }
  if constexpr (SET == 0) {
    constexpr int D = DIST2, R = RING2;
    const unsigned tb = (16 * (g >> 1) + 4 * (g & 1) + q4) * 288 + p4 * 8;
    const unsigned zb = 2 * SLOT + (8 * (g >> 1) + 2 * (g & 1) + (q4 >> 1)) * 512 + (2 * (p4 >> 1) + (q4 & 1)) * 64 + ((c ^ (2 * (g & 1) + (q4 >> 1))) * 16) + (p4 & 1) * 8;
    u32x2 ra[R], rb[R], xv[5], bv[5];
    f32x4 acc[9][2];
    LAS char* bs[11];
    const f32x4 z4 = {0, 0, 0, 0};
    auto fad = [&](int F) AI { return bs[F / 36] + ((F % 36) / 9) * 9216 + (F % 9) * 32; };
    bs[0] = smem + tb; PIN(bs[0]);
#pragma unroll
    for (int F = 0; F < D; F++) { ra[F] = trd(fad(F)); rb[F] = trd(fad(F) + 2304); }
    const unsigned orow = (4 * aq + c + 32 * (32 * bh + 16 * (bb >> 1) + l15)) * 1152u + g * 8, ocol = 288 * q + 160 * (bb & 1);
    SB();
    sfor<369>([&](auto GC_) AI {
      constexpr int G = decltype(GC_)::v, ks = G / 9, dt = G % 9, F = G + D;
      if constexpr (F % 36 == 0) {
        constexpr int j = F / 36;
        if (j == 1) { WAITV(cnt - mst); }
        if (j < 10) { BARX(); }
        if (j == 1) { put_flag(rw, tid == 0, WS_FH + lid * 4, tag); cnt++; }
        bs[j] = smem + (j < 10 ? (j & 3) * SLOT : SP) + tb; PIN(bs[j]);
      }
      if constexpr (dt == 0 && ks < 36) asm volatile("s_waitcnt vmcnt(%2)" : "+v"(hb[ks % HW][0]), "+v"(hb[ks % HW][1]) : "i"(cnt - mh[ks]) : "memory");
      const u32x4 a = {ra[G % R][0], ra[G % R][1], rb[G % R][0], rb[G % R][1]};
      acc[dt][0] = mfma16(a, hb[ks % HW][0], ks == 0 ? z4 : acc[dt][0]);
      SB();
      if constexpr (F < 369) ra[F % R] = trd(fad(F));
      SB();
      acc[dt][1] = mfma16(a, hb[ks % HW][1], ks == 0 ? z4 : acc[dt][1]);
      SB();
      if constexpr (F < 369) rb[F % R] = trd(fad(F) + 2304);
      if constexpr (G == 9 * (10 - HW) + 7 || G == 9 * (18 - HW) + 7) { BLD("dwordx4", pf, dws, 0, (ks + HW < 12 ? WS_F1 : WS_FH) + (lid >> 2) * 16, " sc1"); mpf = ++cnt; }
      if constexpr (dt == 8 && ks + HW < 36) {
        if (ks + HW == 12 || ks + HW == 20) {
          asm volatile("s_waitcnt vmcnt(%1)" : "+v"(pf) : "i"(cnt - mpf) : "memory");
          poll4(rw, dws, pf, 0, (ks + HW == 12 ? WS_F1 : WS_FH) + (lid >> 2) * 16, tag);
        }
        hload(ks + HW);
      }
      if constexpr (ks >= 35 && ks < 40 && (dt == 5 || dt == 6)) {
        constexpr int sp = ks - 35;
        hb[(ks + 1) % HW][dt - 5] = trd2(smem + zb + (dt - 5) * 256 + sp * 8192 - (sp == 4 ? (g >> 1) * 4096 : 0), 2048);
      }
      if constexpr (G >= 270 && G < 320 && G % 5 == 0) {
        constexpr int i = (G - 270) / 10;
        const bool on = i < 4 || (bb & 1) == 0;
        if (G % 10 == 0) BLD("dwordx2", xv[i], dx, on ? orow : OOB, ocol + 32 * i, "");
        else BLD("dwordx2", bv[i], mkdesc(b2, 1152), on ? g * 8 : OOB, ocol + 32 * i, "");
        cnt++;
      }
      SB();
    });
    auto pk4 = [&](f32x4 a, f32x4 b) AI { return u32x4{pk2(a[0], a[1]), pk2(a[2], a[3]), pk2(b[0], b[1]), pk2(b[2], b[3])}; };
#pragma unroll
    for (int d = 0; d < 4; d++)
#pragma unroll
      for (int i = 0; i < 3 - (d & 1); i++) {
        const int t0 = 5 * (d & 1) + 2 * i;
        st16(rw, d == bb ? OOB : lane * 16 + i * 1024, pbase + (d * 16 + bb * 4) * 3072, pk4(acc[t0][d >> 1], acc[i < 2 ? t0 + 1 : t0][d >> 1]));
      }
    u32x4 pp[3][3];
    auto pload = [&]() AI {
#pragma unroll
      for (int s = 0; s < 3; s++)
#pragma unroll
        for (int i = 0; i < 3; i++) BLD("dwordx4", pp[s][i], dws, (i < 2 || (bb & 1) == 0) ? lane * 16 + i * 1024 : OOB, pbase + (bb * 16 + ((bb + 1 + s) & 3) * 4) * 3072, " sc1");
    };
    asm volatile("s_waitcnt vmcnt(10)" : "+v"(xv[0]), "+v"(xv[1]), "+v"(xv[2]), "+v"(xv[3]), "+v"(xv[4]), "+v"(bv[0]), "+v"(bv[1]), "+v"(bv[2]), "+v"(bv[3]), "+v"(bv[4])::"memory");
    f32x4 own[5];
#pragma unroll
    for (int i = 0; i < 5; i++) {
      const int i2 = i < 4 ? 5 + i : 8;
#pragma unroll
      for (int e = 0; e < 4; e++) {
        const float lo = (bb & 2) ? acc[i][1][e] : acc[i][0][e], hi = (bb & 2) ? acc[i2][1][e] : acc[i2][0][e];
        const unsigned xw = xv[i][e >> 1], bw = bv[i][e >> 1];
        own[i][e] = ((bb & 1) ? hi : lo) + ((e & 1) ? bhi(xw) + bhi(bw) : blo(xw) + blo(bw));
      }
      PIN(own[i]);
    }
    for (int it = 0;; it++) {
      pload();
      asm volatile("s_waitcnt vmcnt(0)" : "+v"(pp[0][0]), "+v"(pp[0][1]), "+v"(pp[0][2]), "+v"(pp[1][0]), "+v"(pp[1][1]), "+v"(pp[1][2]), "+v"(pp[2][0]), "+v"(pp[2][1]), "+v"(pp[2][2])::"memory");
      unsigned m = 0;
#pragma unroll
      for (int i = 0; i < 36; i++) { const unsigned w = pp[i / 12][(i >> 2) % 3][i & 3]; m = m > w ? m : w; }
      if (__builtin_amdgcn_ballot_w64(m == 0xffffffffu) == 0) break;
      if (it >= SPIN / 16) { put_flag(rw, true, WS_ERR, 4); break; }
    }
    const rsrc_t ro = mkrsrc(out, 2048 * 1152);
#pragma unroll
    for (int i = 0; i < 5; i++) {
      float v[4];
#pragma unroll
      for (int e = 0; e < 4; e++) {
        const unsigned w0 = pp[0][i >> 1][2 * (i & 1) + (e >> 1)], w1 = pp[1][i >> 1][2 * (i & 1) + (e >> 1)], w2_ = pp[2][i >> 1][2 * (i & 1) + (e >> 1)];
        v[e] = (e & 1) ? (own[i][e] + bhi(w0)) + (bhi(w1) + bhi(w2_)) : (own[i][e] + blo(w0)) + (blo(w1) + blo(w2_));
      }
      __builtin_amdgcn_raw_buffer_store_b64(u32x2{pk2(v[0], v[1]), pk2(v[2], v[3])}, ro, (i < 4 || (bb & 1) == 0) ? orow : OOB, ocol + 32 * i, 16);
    }
  } else {
    auto stage = [&](int j) AI {
      const unsigned so = w2off(j);
#pragma unroll
      for (int n = 0; n < 9; n++) w2stage(so, j, n);
      cnt += 9; mark[j] = cnt;
    };
    auto bar = [&](int j, int m) AI { WAITV(cnt - m); WAITL(0); BARX(); };
    const u32x4 dg = mkdesc(&GT, sizeof(GT));
    f32x4 t1[4], t2[4];
    const f32x4 z4 = {0, 0, 0, 0};
    auto xpin = [&](int z) AI {
#pragma unroll
      for (int i = 0; i < 8; i++) PIN(xx[z][i]);
      PIN(wt[z][0]); PIN(wt[z][1]);
    };
    auto tpin = [&](u32x4 (&f)[2][3]) AI {
#pragma unroll
      for (int t = 0; t < 2; t++) { PIN(f[t][0]); PIN(f[t][1]); f[t][2] = f[t][1] ^ 0x80008000u; }
    };
    asm volatile("s_waitcnt vmcnt(%1)" : "+v"(pf) : "i"(cnt - mpf) : "memory");
    poll4(rw, dws, pf, lane * 16, WS_F0 + xcd * 1024, tag);
#pragma unroll
    for (int i = 0; i < 10; i++) xload(0, i);
    cnt += 10; mx = cnt;
    stage(2);
    {
      const float f1 = (16 * c + l15) * (1.0f / 2048.0f), f2 = (16 * c + 4 * g) * (1.0f / 2048.0f);
#pragma unroll
      for (int at = 0; at < 2; at++)
#pragma unroll
        for (int i = 0; i < 4; i++) {
          const float a1 = (float)(16 * at + 4 * g + i) * f1, a2 = (float)(16 * at + l15) * (f2 + i * (1.0f / 2048.0f));
          t1[2 * at][i] = __builtin_amdgcn_cosf(a1); t1[2 * at + 1][i] = __builtin_amdgcn_sinf(a1);
          t2[2 * at][i] = __builtin_amdgcn_cosf(a2) * (1.0f / 2048.0f); t2[2 * at + 1][i] = __builtin_amdgcn_sinf(a2) * (1.0f / 2048.0f);
        }
#pragma unroll
      for (int i = 0; i < 4; i++) { PIN(t1[i]); PIN(t2[i]); }
    }
    bar(1, mst);
    if (has2) {
#pragma unroll
      for (int i = 0; i < 10; i++) xload(1, i);
    }
#pragma unroll
    for (int i = 0; i < 4; i++) tload(fb, 24576 + c * 6144, i);
    cnt += 4;
    const int mb = cnt;
    stage(3);
    WAITV(cnt - mx);
    tpin(fa); tpin(f32); xpin(0);
    auto front = [&](int z) AI {
      u32x4 br, bi, yr, yi;
#pragma unroll
      for (int at = 0; at < 2; at++) {
        f32x4 tr = z4, ti = z4;
#pragma unroll
        for (int t = 0; t < 2; t++) {
          const u32x4 xr = xx[z][2 * t + at], xi = xx[z][4 + 2 * t + at];
          tr = mfma16(xr, fa[t][0], tr); ti = mfma16(xi, fa[t][0], ti);
          tr = mfma16(xi, fa[t][1], tr); ti = mfma16(xr, fa[t][2], ti);
        }
        const f32x4 co = t1[2 * at], si = t1[2 * at + 1];
        const f32x4 ur = tr * co + ti * si, ui = ti * co - tr * si;
        br[2 * at] = pk2(ur[0], ur[1]); br[2 * at + 1] = pk2(ur[2], ur[3]);
        bi[2 * at] = pk2(ui[0], ui[1]); bi[2 * at + 1] = pk2(ui[2], ui[3]);
      }
#pragma unroll
      for (int ct = 0; ct < 2; ct++) {
        f32x4 xr = mfma16(f32[ct][0], br, z4), xi = mfma16(f32[ct][0], bi, z4);
        xr = mfma16(f32[ct][1], bi, xr); xi = mfma16(f32[ct][2], br, xi);
        float vr[4], vi[4];
#pragma unroll
        for (int i = 0; i < 4; i++) { const unsigned w = wt[z][ct][i]; const float a = blo(w), b = bhi(w); vr[i] = xr[i] * a - xi[i] * b; vi[i] = xr[i] * b + xi[i] * a; }
        yr[2 * ct] = pk2(vr[0], vr[1]); yr[2 * ct + 1] = pk2(vr[2], vr[3]);
        yi[2 * ct] = pk2(vi[0], vi[1]); yi[2 * ct + 1] = pk2(vi[2], vi[3]);
      }
#pragma unroll
      for (int at = 0; at < 2; at++) {
        f32x4 pr = mfma16(yr, f32[at][0], z4), pi = mfma16(yi, f32[at][0], z4);
        pr = mfma16(yi, f32[at][2], pr); pi = mfma16(yr, f32[at][1], pi);
        const f32x4 co = t2[2 * at], si = t2[2 * at + 1];
        const f32x4 ur = pr * co - pi * si, ui = pi * co + pr * si;
        LAS char* p = smem + SP + z * 8192 + (at * 4 + (c >> 1)) * 1024 + lane * 16 + (c & 1) * 8;
        *(LAS u32x2*)p = u32x2{pk2(ur[0], ur[1]), pk2(ur[2], ur[3])};
        *(LAS u32x2*)(p + 2048) = u32x2{pk2(ui[0], ui[1]), pk2(ui[2], ui[3])};
      }
    };
    front(0);
    bar(2, mb);
    tpin(fb);
    stage(4);
    auto back = [&](int z) AI {
      const int pair = kc[z] == 288 ? 0 : kc[z];
      const bool on = kc[z] == 0 ? (g & 1) == 0 : (kc[z] == 288 ? (g & 1) == 1 : true);
#pragma unroll
      for (int at = 0; at < 2; at++) {
        f32x4 zr = z4, zi = z4;
#pragma unroll
        for (int t = 0; t < 2; t++) {
          LAS char* p = smem + SP + z * 8192 + (at * 4 + t) * 1024 + lane * 16;
          const u32x4 pr = *(LAS u32x4*)p, pi = *(LAS u32x4*)(p + 2048);
          zr = mfma16(fb[t][0], pr, zr); zi = mfma16(fb[t][0], pi, zi);
          zr = mfma16(fb[t][2], pi, zr); zi = mfma16(fb[t][1], pr, zi);
        }
        const unsigned r0 = pk2(zr[0], zr[1]), r1 = pk2(zr[2], zr[3]);
        const unsigned i0 = kc[z] == 288 ? r0 : pk2(zi[0], zi[1]), i1 = kc[z] == 288 ? r1 : pk2(zi[2], zi[3]);
        const auto s0 = __builtin_amdgcn_permlane16_swap(r0, i0, false, false), s1 = __builtin_amdgcn_permlane16_swap(r1, i1, false, false);
        const unsigned a0 = s0[0], a1 = s1[0], a2 = s0[1], a3 = s1[1];
        st16(rw, on ? q4 * 1024u + g * 64 + ((p4 ^ (pair & 3)) * 16) : OOB, WS_Z + pair * 8192 + at * 4096 + c * 256, u32x4{a0, a1, a2, a3});
      }
    };
    back(0);
    cnt += 2;
    if (has2) { xpin(1); front(1); }
    bar(3, mark[3]);
    if (has2) back(1);
    stage(5);
#pragma unroll
    for (int d = 0; d < 4; d++)
#pragma unroll
      for (int i = 0; i < 3 - (d & 1); i++) st16(rw, d == bb ? OOB : lane * 16 + i * 1024, pbase + (d * 16 + bb * 4) * 3072, u32x4{~0u, ~0u, ~0u, ~0u});
    cnt += 10;
    const int mz = cnt;
    bar(4, mark[4]);
    stage(6);
#pragma unroll
    for (int n = 0; n < 3; n++) { const int pc = n < 2 ? c + 4 * n : 8; GLDS(dg, lane * 16, (nt * 5 + 4) * 9216 + pc * 1024, 0, SP + pc * 1024, 0); }
    cnt += 3;
    bar(5, mz);
    put_flag8(rw, tid - 256, WS_FZ + lid * 4, tag); cnt++;
    stage(7);
    bar(6, mark[6]);
    stage(8);
    bar(7, mark[7]);
#pragma unroll
    for (int n = 0; n < 9; n++) GLDS(dg, lane * 16, (nt * 5 + c) * 9216, 1024 * n, mlb, SLOT + 1024 * n);
    __builtin_amdgcn_s_sleep(4);
    BLD("dwordx4", pf, dws, c == 0 ? lane * 16 : OOB, WS_FZ + xcd * 1024, " sc1");
    asm volatile("s_waitcnt vmcnt(0)" : "+v"(pf)::"memory");
    if (c == 0) poll4(rw, dws, pf, lane * 16, WS_FZ + xcd * 1024, tag);
    bar(8, cnt);
    u32x4 zz[9];
#pragma unroll
    for (int n = 0; n < 9; n++) BLD("dwordx4", zz[n], dws, hh * 8192 + r * 16, WS_Z + (72 * bb + 2 * c + 8 * n) * 8192 + rt * 512, " sc1");
    put_flag(rw, lid == 0 && tid == 256, WS_E, tag);
    WAITV(1);
#pragma unroll
    for (int n = 0; n < 9; n++) { PIN(zz[n]); *(LAS u32x4*)(smem + 2 * SLOT + (c + 4 * n) * 1024 + lane * 16) = zz[n]; }
    bar(9, cnt - 1);
  }
  if (SET == 0) asm volatile(".Lkend:");
  };
  if (wv < 4) body(IC<0>{}); else body(IC<1>{});
}

extern "C" void kernel_launch(void* const* d_in, const int* in_sizes, int n_in, void* d_out, int out_size, void* d_ws, size_t ws_size, hipStream_t stream) {
  (void)hipFuncSetAttribute((const void*)kmain, hipFuncAttributeMaxDynamicSharedMemorySize, LDSZ);
  kmain<<<256, 512, LDSZ, stream>>>((const u16*)d_in[0], (const u16*)d_in[1], (const u16*)d_in[2], (const u16*)d_in[4], (const u16*)d_in[5],
                                    (const u16*)d_in[6], (const u16*)d_in[7], (u16*)d_out, (char*)d_ws);
}
```

```cpp
#include <hip/hip_runtime.h>
#include <hip/hip_bf16.h>
#include <utility>

typedef unsigned short u16;
typedef __attribute__((ext_vector_type(8))) __bf16 bf16x8;
typedef __attribute__((ext_vector_type(2))) __bf16 bf16x2;
typedef __attribute__((ext_vector_type(4))) short s16x4;
typedef __attribute__((ext_vector_type(8))) short s16x8;
typedef __attribute__((ext_vector_type(4))) float f32x4;
typedef __attribute__((ext_vector_type(16))) float f32x16;
typedef __attribute__((ext_vector_type(4))) unsigned u32x4;
typedef __attribute__((ext_vector_type(2))) unsigned u32x2;
#define LAS __attribute__((address_space(3)))
#define GAS __attribute__((address_space(1)))
#define DI __device__ __forceinline__

namespace tab {
constexpr double PI_ = 3.14159265358979323846;
constexpr double tsin(double x) { double t = x, s = x; for (int i = 1; i < 12; i++) { t *= -x * x / ((2 * i) * (2 * i + 1)); s += t; } return s; }
constexpr double tcos(double x) { double t = 1, s = 1; for (int i = 1; i < 12; i++) { t *= -x * x / ((2 * i - 1) * (2 * i)); s += t; } return s; }
constexpr double ccos(int n, int N) {
  n %= N; if (n < 0) n += N;
  if (2 * n > N) n = N - n;
  bool neg = false;
  if (4 * n > N) { n = N / 2 - n; neg = true; }
  double v = (8 * n > N) ? tsin(2 * PI_ * (N / 4 - n) / N) : tcos(2 * PI_ * n / N);
  return neg ? -v : v;
}
constexpr double csin(int n, int N) { return ccos(n - N / 4, N); }
constexpr u16 f2bf(double d) { float f = (float)d; unsigned u = __builtin_bit_cast(unsigned, f); u += 0x7fffu + ((u >> 16) & 1u); return (u16)(u >> 16); }
struct C576 { u16 c[576]; u16 g1[576]; u16 g2[576]; };
constexpr C576 mk576() { C576 r{}; for (int i = 0; i < 576; i++) { double c = ccos(i, 576); r.c[i] = f2bf(c); r.g1[i] = f2bf(c / 576.0); r.g2[i] = f2bf(2.0 * c / 576.0); } return r; }
constexpr C576 c576 = mk576();
struct Blk { u16 v[9216]; };
constexpr int fidx(int bc, int d) {
  int n = bc < 144 ? bc * d : (bc == 144 ? ((d & 1) == 0 ? 144 * d : 144 * d + 144) : (bc - 144) * d + 144);
  return n % 576;
}
constexpr Blk mkF(int idx) {
  Blk r{}; int at = idx >> 1, hf = idx & 1;
  for (int st = 0; st < 18; st++) for (int hh = 0; hh < 2; hh++) for (int j = 0; j < 8; j++) {
    int d = 288 * hf + 32 * (st >> 1) + 16 * (j >> 2) + 8 * hh + 2 * (j & 3) + (st & 1);
    for (int cc = 0; cc < 32; cc++) r.v[((st * 2 + hh) * 8 + j) * 32 + cc] = c576.c[fidx(32 * at + cc, d)];
  }
  return r;
}
template <int I> constexpr Blk fblk = mkF(I);
struct FTab { Blk b[18]; };
template <int... I> constexpr FTab mkFT(std::integer_sequence<int, I...>) { return FTab{{fblk<I>...}}; }
constexpr double bf2d(u16 b) { return (double)__builtin_bit_cast(float, (unsigned)b << 16); }
struct FCB { unsigned v[64]; };
constexpr FCB mkFC(int at) {
  FCB r{};
  for (int e = 0; e < 2; e++) for (int cc = 0; cc < 32; cc++) {
    double s = 0;
    for (int d = e; d < 576; d += 2) s += bf2d(c576.c[fidx(32 * at + cc, d)]);
    u16 hi = f2bf(s), lo = f2bf(s - bf2d(hi));
    r.v[cc * 2 + e] = (unsigned)hi | ((unsigned)lo << 16);
  }
  return r;
}
template <int I> constexpr FCB fcb = mkFC(I);
struct FCT { FCB b[9]; };
template <int... I> constexpr FCT mkFCT(std::integer_sequence<int, I...>) { return FCT{{fcb<I>...}}; }
struct GBlk { u16 v[4608]; };
constexpr GBlk mkG(int idx) {
  GBlk r{}; int step = idx % 5, q = (idx / 5) & 3, b = idx / 20;
  for (int row = 0; row < 32; row++) {
    int rho = 32 * step + row;
    if (rho >= 144) continue;
    int kc = 72 * b + (rho >> 1), ri = rho & 1;
    for (int c = 0; c < 144; c++) {
      int d = 144 * q + c;
      r.v[row * 144 + c] = kc == 0 ? c576.g1[(ri * 288 * d) % 576] : c576.g2[(kc * d + ri * 144) % 576];
    }
  }
  return r;
}
template <int I> constexpr GBlk gblk = mkG(I);
struct GTab { GBlk b[80]; };
template <int... I> constexpr GTab mkGT(std::integer_sequence<int, I...>) { return GTab{{gblk<I>...}}; }
struct C2048 { float c[2048]; };
constexpr C2048 mk2048() { C2048 r{}; for (int i = 0; i < 2048; i++) r.c[i] = (float)ccos(i, 2048); return r; }
constexpr C2048 c2048 = mk2048();
constexpr u16 trig(int n2048, int type) {
  int n = type == 0 ? n2048 : (type == 1 ? n2048 + 1536 : n2048 + 512);
  return f2bf(c2048.c[n & 2047]);
}
struct F64T { u16 v[4][2][3][64][8]; };
constexpr F64T mkF64(int which) {
  F64T r{};
  for (int j = 0; j < 4; j++) for (int t = 0; t < 2; t++) for (int ty = 0; ty < 3; ty++) for (int l = 0; l < 64; l++) for (int jj = 0; jj < 8; jj++) {
    int g = l >> 4, o = 16 * j + (l & 15);
    int k = which == 0 ? 32 * t + 16 * (g >> 1) + 8 * (jj >> 2) + 4 * (g & 1) + (jj & 3) : 32 * t + 16 * (jj >> 2) + 4 * g + (jj & 3);
    r.v[j][t][ty][l][jj] = trig(o * k * 32, ty);
  }
  return r;
}
struct F32T { u16 v[2][3][64][8]; };
constexpr F32T mkF32() {
  F32T r{};
  for (int ct = 0; ct < 2; ct++) for (int ty = 0; ty < 3; ty++) for (int l = 0; l < 64; l++) for (int jj = 0; jj < 8; jj++) {
    int g = l >> 4, o = 16 * ct + (l & 15), k = 16 * (jj >> 2) + 4 * g + (jj & 3);
    r.v[ct][ty][l][jj] = trig(o * k * 64, ty);
  }
  return r;
}
struct TWT { float v[4][4][64][4]; };
constexpr TWT mkTW(int which) {
  TWT r{};
  for (int j = 0; j < 4; j++) for (int n = 0; n < 4; n++) for (int l = 0; l < 64; l++) for (int i = 0; i < 4; i++) {
    int g = l >> 4, at = n >> 1;
    int a = which == 0 ? 16 * at + 4 * g + i : 16 * at + (l & 15);
    int d = which == 0 ? 16 * j + (l & 15) : 16 * j + 4 * g + i;
    float v = c2048.c[(a * d + ((n & 1) ? 1536 : 0)) & 2047];
    r.v[j][n][l][i] = which == 0 ? v : v * (1.0f / 2048.0f);
  }
  return r;
}
constexpr F64T f64a = mkF64(0), f64b = mkF64(1);
constexpr F32T f32t = mkF32();
constexpr TWT tw1 = mkTW(0), tw2 = mkTW(1);
struct Small { F64T a, b; F32T f; TWT t1, t2; FCT fc; };
}
#if defined(__HIP_DEVICE_COMPILE__)
__device__ __attribute__((aligned(128))) static const tab::FTab FT = tab::mkFT(std::make_integer_sequence<int, 18>{});
__device__ __attribute__((aligned(128))) static const tab::GTab GT = tab::mkGT(std::make_integer_sequence<int, 80>{});
__device__ __attribute__((aligned(128))) static const tab::Small ST = {tab::f64a, tab::f64b, tab::f32t, tab::tw1, tab::tw2, tab::mkFCT(std::make_integer_sequence<int, 9>{})};
#else
__device__ static const tab::FTab FT = {};
__device__ static const tab::GTab GT = {};
__device__ static const tab::Small ST = {};
#endif
typedef unsigned long long u64;
DI float bfr(float x) { return (float)(__bf16)x; }
DI unsigned pk2(float a, float b) { bf16x2 c; c[0] = (__bf16)a; c[1] = (__bf16)b; return __builtin_bit_cast(unsigned, c); }
DI float blo(unsigned u) { return __builtin_bit_cast(float, u << 16); }
DI float bhi(unsigned u) { return __builtin_bit_cast(float, u & 0xffff0000u); }
DI f32x16 mfma32(u32x4 a, u32x4 b, f32x16 c) { return __builtin_amdgcn_mfma_f32_32x32x16_bf16(__builtin_bit_cast(bf16x8, a), __builtin_bit_cast(bf16x8, b), c, 0, 0, 0); }
DI f32x4 mfma16(u32x4 a, u32x4 b, f32x4 c) { return __builtin_amdgcn_mfma_f32_16x16x32_bf16(__builtin_bit_cast(bf16x8, a), __builtin_bit_cast(bf16x8, b), c, 0, 0, 0); }
DI u32x2 trd(LAS char* p) { return __builtin_bit_cast(u32x2, __builtin_amdgcn_ds_read_tr16_b64_v4i16((LAS s16x4*)p)); }
DI u32x4 trd2(LAS char* p, int off2) { u32x2 a = trd(p), b = trd(p + off2); u32x4 r; r[0] = a[0]; r[1] = a[1]; r[2] = b[0]; r[3] = b[1]; return r; }
template <int C> DI float dppadd(float v) { return v + __builtin_bit_cast(float, __builtin_amdgcn_mov_dpp(__builtin_bit_cast(int, v), C, 0xf, 0xf, true)); }
DI u32x4 mkdesc(const void* p, unsigned bytes) { u64 a = (u64)p; return u32x4{(unsigned)a, (unsigned)(a >> 32) & 0xffffu, bytes, 0x00020000u}; }
typedef __amdgpu_buffer_rsrc_t rsrc_t;
DI rsrc_t mkrsrc(const void* p, unsigned bytes) { return __builtin_amdgcn_make_buffer_rsrc((void*)p, 0, bytes, 0x00020000); }
DI void st16(rsrc_t r, unsigned voff, unsigned soff, u32x4 v) {
  __builtin_amdgcn_raw_buffer_store_b128(v, r, voff, soff, 16);
  asm volatile("s_nop 1" ::"v"(v) : "memory");
}
template <int AUX> DI void st16x(rsrc_t r, unsigned voff, unsigned soff, u32x4 v) {
  __builtin_amdgcn_raw_buffer_store_b128(v, r, voff, soff, AUX);
  asm volatile("s_nop 1" ::"v"(v) : "memory");
}
DI u32x4 ld16(rsrc_t r, unsigned voff, unsigned soff) { return __builtin_amdgcn_raw_buffer_load_b128(r, voff, soff, 16); }
#define GLDS(d, voff, sbase, simm, lbase, limm) GLDSP(d, voff, sbase, simm, lbase, limm, "")
#define GLDSP(d, voff, sbase, simm, lbase, limm, pol) do { unsigned t_; \
  asm volatile("s_add_i32 m0, %1, %2\n\ts_add_i32 %0, %3, %4\n\tbuffer_load_dwordx4 %5, %6, %0 offen" pol " lds" \
               : "=&s"(t_) : "s"(lbase), "i"(limm), "s"(sbase), "i"(simm), "v"(voff), "s"(d) : "memory", "scc"); } while (0)
#define GLDS1(d, voff, soff, lb) asm volatile("s_mov_b32 m0, %0\n\tbuffer_load_dword %1, %2, %3 offen lds" ::"i"(lb), "v"(voff), "s"(d), "s"(soff) : "memory")
#define WAITV(n) asm volatile("s_waitcnt vmcnt(%0)" ::"i"(n) : "memory")
#define WAITL(n) asm volatile("s_waitcnt lgkmcnt(%0)" ::"i"(n) : "memory")
#define PIN(x) asm volatile("" : "+v"(x))
#define BARX() asm volatile("s_barrier" ::: "memory")
#define SB() __builtin_amdgcn_sched_barrier(0)
#define AI __attribute__((always_inline))
#if defined(__HIP_DEVICE_COMPILE__)
#define NOPK __attribute__((target("no-packed-fp32-ops")))
#else
#define NOPK
#endif
template <int N> struct IC { static constexpr int v = N; };
template <class F, int... I> DI void sfor_(F&& f, std::integer_sequence<int, I...>) { (f(IC<I>{}), ...); }
template <int N, class F> DI void sfor(F&& f) { sfor_(f, std::make_integer_sequence<int, N>{}); }

#define BLD(op, dst, d, voff, soff, pol) asm volatile("buffer_load_" op " %0, %1, %2, %3 offen" pol : "=v"(dst) : "v"(voff), "s"(d), "s"(soff) : "memory")
DI float asf(unsigned u) { return __builtin_bit_cast(float, u); }
DI unsigned asu(float f) { return __builtin_bit_cast(unsigned, f); }
DI float swadd32(float a, float b) { auto s = __builtin_amdgcn_permlane32_swap(asu(a), asu(b), false, false); const unsigned x = s[0], y = s[1]; return asf(x) + asf(y); }
DI float swadd16(float a, float b) { auto s = __builtin_amdgcn_permlane16_swap(asu(a), asu(b), false, false); const unsigned x = s[0], y = s[1]; return asf(x) + asf(y); }
DI float half_sum(float v) {
  v = dppadd<0xB1>(v); v = dppadd<0x4E>(v); v = dppadd<0x141>(v); v = dppadd<0x140>(v);
  return swadd16(v, v);
}

constexpr int SLOT = 36864, SP = 4 * SLOT, LDSZ = 163840;
constexpr int SP_T = SP + 8192, SP_CS = SP_T + 4608, SP_RS = SP_T + 6656, SP_PF = SP_T + 7168;
constexpr unsigned WS_E = 0, WS_F1 = 1024, WS_FH = 2048, WS_FA = 3072, WS_F5 = 4096, WS_ERR = 5120, WS_ST = 8192, WS_F0 = 24576, WS_FZ = 32768;
constexpr unsigned WS_CS = 40960;
constexpr unsigned WS_H = WS_CS + 3 * 36864;
constexpr unsigned WS_XS = WS_H + 64u * 144 * 2048;
constexpr unsigned WS_Z = WS_XS + 576u * 4096;
constexpr unsigned WS_WT = WS_Z + 576u * 4096;
constexpr unsigned WS_FI = WS_WT + 289u * 8192;
#define PSEP 0
#define STAMPW 0
constexpr unsigned WS_END = WS_FI + 32768 + PSEP;
constexpr int SPIN = 200000;
constexpr unsigned OOB = 0x80000000u;
DI void put_flag(rsrc_t rw, bool on, unsigned off, unsigned tag) { __builtin_amdgcn_raw_buffer_store_b32(tag, rw, on ? 0u : OOB, off, 16); }
DI void put_flag8(rsrc_t rw, unsigned t, unsigned off, unsigned tag) { __builtin_amdgcn_raw_buffer_store_b32(tag, rw, t < 8 ? t * 1024 : OOB, off, 16); }
DI bool bad4(u32x4 v, unsigned tag) { return __builtin_amdgcn_ballot_w64(v[0] != tag || v[1] != tag || v[2] != tag || v[3] != tag) != 0; }
DI void poll4(rsrc_t rw, u32x4 dws, u32x4 v, unsigned voff, unsigned soff, unsigned tag) {
  for (int it = 0; bad4(v, tag); it++) {
    if (it >= SPIN) { put_flag(rw, true, WS_ERR, 1); break; }
    __builtin_amdgcn_s_sleep(4);
    BLD("dwordx4", v, dws, voff, soff, " sc1");
    asm volatile("s_waitcnt vmcnt(0)" : "+v"(v)::"memory");
  }
}
#define DIST1 4
#define RING1 5
#define DIST2 4
#define RING2 5
#define ABL 0
#define PREF 3
#define PREFC 1
#define PREF2 103
#define PFA 26
#define PFB 42
#define PFC 43
#define HAUX 16
#define WNT ""
#define OAUX 16
#define PRIO 2
#define OTOUCH 1
#define TSLEEP 24
#define TWS 26
#define PFW 27
__global__ __launch_bounds__(512, 2) NOPK void kmain(const u16* __restrict__ x, const u16* __restrict__ wr, const u16* __restrict__ wi,
                                                     const u16* __restrict__ w1, const u16* __restrict__ b1, const u16* __restrict__ w2,
                                                     const u16* __restrict__ b2, u16* __restrict__ out, char* __restrict__ ws) {
  extern __shared__ __attribute__((aligned(16))) char smem_[];
  LAS char* smem = (LAS char*)smem_;
  const int tid = threadIdx.x, lane = tid & 63;
  const int wv = __builtin_amdgcn_readfirstlane(tid >> 6);
  auto body = [&](auto SETC) AI {
  constexpr int SET = decltype(SETC)::v;
  const int id = blockIdx.x, xcd = id & 7, slot = id >> 3;
  const int rt = (xcd >> 2) * 8 + (slot >> 2), bb = xcd & 3, q = slot & 3, nt = bb * 4 + q, lid = rt * 16 + nt;
  const int aq = rt >> 1, bh = rt & 1, c = wv & 3;
  const int fl0 = (nt & 1) * 32, flh = (nt & 1) ? 0 : 256;
  const bool has_dft = nt < 9;
  const rsrc_t rw = mkrsrc(ws, WS_END);
  const u32x4 dws = mkdesc(ws, WS_END), dx = mkdesc(x, 2048 * 1152), dw1 = mkdesc(w1, 576 * 9216), dw2 = mkdesc(w2, 4608 * 1152);
  const int r = lane & 31, hh = lane >> 5;
  unsigned ev, xc;
  u32x4 xs[2];
  asm volatile("s_load_dword %0, %1, 0x0" : "=s"(ev) : "s"(ws) : "memory");
  if (SET == 1 && PREFC) {
    u64 pc; unsigned len;
    asm volatile("s_getpc_b64 %0\n.Lkpc:\n\ts_mov_b32 %1, .Lkend-.Lkpc" : "=s"(pc), "=s"(len));
    GLDS1(mkdesc((const void*)pc, len < 0x40000u ? len : 0u), lane * 128u + (c * 32 + slot) * 8192u, 0, SP_PF);
  }
  if (SET == 0) {
    const int tr = 8 * nt + 2 * c;
    const unsigned ro = (4 * aq + (tr >> 5) + 32 * (32 * bh + (tr & 31))) * 1152u;
    BLD("dwordx4", xs[0], dx, hh * 36864 + r * 16, ro, "");
    BLD("dwordx4", xs[1], dx, hh * 36864 + r * 16, ro, " offset:512");
    BLD("dword", xc, dx, hh * 36864 + r * 4, ro, " offset:1024");
  }
  const unsigned xvo = (4 * aq + (wv >> 1) + 32 * (32 * bh + 16 * (wv & 1) + (lane >> 3))) * 1152u + (((lane & 7) ^ (lane >> 4)) * 16);
  const unsigned xvo1 = (xvo ^ 64) + 8 * 32 * 1152;
  const unsigned xlb = 2 * SLOT + wv * 10240;
#pragma unroll
  for (int n = 0; n < 5; n++) { GLDS(dx, xvo, 0, 128 * n, xlb, 1024 * n); GLDS(dx, xvo1, 0, 128 * n, xlb, 5120 + 1024 * n); }
  const unsigned wvo = (8 * c + ((lane >> 2) & 7)) * 9216u + hh * 64 + (lane & 3) * 16;
  const unsigned wso = (288 * nt + fl0) * 2, wlb = c * 1024;
  const u32x4 dft = mkdesc(&FT, has_dft ? sizeof(FT) : 0);
  const u32x4 dm = c < 2 ? dw1 : dft;
  const unsigned mvo = c < 2 ? (144 * c + (lane >> 2)) * 9216u + (lane & 3) * 16 : lane * 16;
  const unsigned mso = c < 2 ? (288 * nt + flh) * 2 : nt * 36864 + (c & 1) * 9216;
  const unsigned mstep = c < 2 ? 16 * 9216 : 1024, mhf = c < 2 ? 288 * 9216 : 18432, mlb = c * 9216;
#pragma unroll
  for (int n = SET; n < 9; n += 2) GLDS(dm, mvo, mso + n * mstep, 0, mlb, 1024 * n);
  const int cu = (xcd >> 2) * 32 + slot, cg = 18 * bb + cu / 3, g3 = cu % 3;
  u32x4 uu[3];
#pragma unroll
  for (int n = 0; n < 3; n++) BLD("dwordx4", uu[n], dw1, (cu < 54 && !(ABL & 64)) ? (lane >> 3) * 9216u + (lane & 7) * 16 : OOB, (192 * g3 + 24 * wv + 8 * n) * 9216 + cg * 128, "");
  if (PREF == 2) {
    const int u = (7 - wv) * 32 + slot, xh = xcd >> 2, v = xcd * 37 + u - 106;
    const u32x4 dp = u < 81 ? dw2 : u < 93 ? mkdesc(&GT, sizeof(GT)) : u < 94 ? mkdesc(&ST, sizeof(ST)) : u < 106 ? mkdesc(&FT, sizeof(FT))
                   : mkdesc(v < 148 ? wr : wi, u < 143 ? 2048 * 578 : 0);
    const unsigned so = u < 81 ? bb * 1327104 + (xh * 81 + u) * 8192 : u < 93 ? bb * 184320 + (xh * 11 + u - 81) * 8192 : u < 94 ? xcd * 8192
                      : u < 106 ? (bb * 4 + (u - 94) / 3) * 36864 + 18432 + ((u - 94) % 3) * 6144 : (v < 148 ? v : v - 148) * 8192;
    GLDS1(dp, lane * 128u, so, SP_PF);
  }
  asm volatile("s_waitcnt lgkmcnt(0)" : "+s"(ev)::"memory");
  const unsigned tag = ev * 0x9E3779B1u + 0x7F4A7C15u;
  if (SET == 0) {
    asm volatile("s_waitcnt vmcnt(%3)" : "+v"(xs[0]), "+v"(xs[1]), "+v"(xc) : "i"(18 + (PREF == 2)) : "memory");
    float s = blo(xc) + bhi(xc);
#pragma unroll
    for (int i = 0; i < 8; i++) { const unsigned w = xs[i >> 2][i & 3]; s += blo(w) + bhi(w); }
    const float mean = half_sum(s) * (1.0f / 576.0f);
    float v = 0.f;
#pragma unroll
    for (int i = 0; i < 9; i++) { const unsigned w = i < 8 ? xs[(i >> 2) & 1][i & 3] : xc; const float d0 = blo(w) - mean, d1 = bhi(w) - mean; v = __builtin_fmaf(d0, d0, v); v = __builtin_fmaf(d1, d1, v); }
    const float var = half_sum(v) * (1.0f / 576.0f);
    __builtin_amdgcn_raw_buffer_store_b64(u32x2{pk2(mean, __builtin_sqrtf(bfr(bfr(var) + 1e-5f))), tag}, rw, r == 0 ? hh * 8u : OOB, WS_ST + (rt * 128 + 8 * nt + 2 * c) * 8, 16);
  }
  const int q4 = (lane & 15) >> 2, p4 = lane & 3, g = lane >> 4, l15 = lane & 15;
  const unsigned hoff = (r >> 4) * 1024 + hh * 256 + (r & 15) * 16;
  const unsigned hrow = WS_H + (4 * rt + c) * 144 * 2048;
  const unsigned hso = hrow + 9 * nt * 2048;
  unsigned w2o[9];
  constexpr auto ftile = [](int ks) constexpr {
    const int qq = ks < 12 ? ks / 3 : ks < 20 ? (ks - 12) / 2 : (ks - 20) / 4, e = ks < 12 ? ks % 3 : ks < 20 ? 3 + (ks & 1) : 5 + (ks & 3);
    return 9 * qq + ((qq & 1) ? e : (e == 0 ? 8 : e - 1));
  };
  auto w2off = [&](int j) AI {
    const unsigned t = ftile(4 * j) | ftile(4 * j + 1) << 8 | ftile(4 * j + 2) << 16 | ftile(4 * j + 3) << 24;
    return (36 * bb + ((t >> (8 * c)) & 255)) * 36864 + q * 288;
  };
  auto w2stage = [&](unsigned so, int j, int n) AI { GLDS(dw2, w2o[n], so, 0, mlb, (j & 3) * SLOT + 1024 * n); };
  int cnt = 0, mark[12] = {};
  constexpr int HW = 8;
  u32x4 hb[HW][2];
  int mh[36];
  auto hload = [&](int ks) AI {
    const unsigned so = hrow + (36 * bb + ftile(ks)) * 2048;
    BLD("dwordx4", hb[ks % HW][0], dws, lane * 16, so, " sc1");
    BLD("dwordx4", hb[ks % HW][1], dws, lane * 16, so, " offset:1024 sc1");
    cnt += 2; mh[ks] = cnt;
  };
  u32x4 pf;
  int mpf, mst, mx;
  auto pslot = [&](int b, int s) AI { return PSEP + hrow + 9 * (4 * b + q) * 2048 + (s == 0 ? 0 : s == 1 ? 2048 + (b & 1) * 1024 : 5120); };
  const unsigned fio = WS_FI + ((rt * 4 + q) * 4 + c) * 128;
  const u32x4 dst = mkdesc(&ST, sizeof(ST));
  const int e2 = rt * 7 + nt - 9;
  const bool has2 = nt >= 9 && e2 < 33;
  const int kc[2] = {lid, 256 + e2};
  u32x4 fa[2][3], fb[2][3], f32[2][3], xx[2][8], wt[2][2];
  auto xload = [&](int z, int i) AI {
    const bool cplx = kc[z] != 0 && kc[z] != 288;
    const unsigned vo = q4 * 256 + g * 64 + p4 * 16;
    if (i < 8) BLD("dwordx4", xx[z][i], dws, (i < 4 || cplx) ? vo : OOB, WS_XS + (kc[z] == 288 ? 1 : 2 * kc[z]) * 4096 + i * 1024, " sc1");
    else BLD("dwordx4", wt[z][i - 8], dws, lane * 16, WS_WT + kc[z] * 8192 + (c * 2 + i - 8) * 1024, " sc1");
  };
  auto tload = [&](u32x4 (&f)[2][3], unsigned so, int i) AI { BLD("dwordx4", f[i >> 1][i & 1], dst, lane * 16, so + (i >> 1) * 3072 + (i & 1) * 1024, ""); };

  {
    constexpr int D = DIST1, R = RING1;
    const unsigned tq = q4 * 64 + (g & 1) * 32 + p4 * 8;
    LAS char* const tbP = smem + hh * 1024 + tq + SET * 512;
    LAS char* const tbM = smem + hh * 512 + tq + SET * 18432;
    const unsigned xa = 2 * SLOT + (4 * c + (r >> 3)) * 5120 + (r & 7) * 128 + ((hh ^ ((r >> 1) & 7)) * 16);
    u32x4 xf[36], ring[R], pm[2], sf, tf, xq[4];
    u32x2 sg, fc, wl[8];
    u32x4 cq[3];
    unsigned bia, w2s, wvv;
    f32x16 acc[6];
    f32x4 cr[2];
    float cs[8], gt[3][8], ge[3][8], k0, k1, sdf;
    u32x4 go[3];
    LAS char* bs[11];
    int mw, mt[5];
    const f32x16 zero = {0, 0, 0, 0, 0, 0, 0, 0, 0, 0, 0, 0, 0, 0, 0, 0};
    constexpr auto isM = [](int k) constexpr { return k == 0 || k == 2; };
    constexpr auto tile = [](int k) constexpr { return k < 4 ? (k & 1) : (k >> 1); };
    constexpr auto half = [](int k) constexpr { return k < 4 ? (k >> 1) : (k & 1); };
    constexpr auto slotof = [](int k) constexpr { return k < 2 ? k : (k + 2) & 3; };
    auto fetch = [&](int F) AI { const int k = F / 18, st = F % 18; ring[F % R] = trd2(bs[k] + st * (isM(k) ? 1024 : 2048), 256); };
    auto xread = [&](int i) AI { if (i < 36) xf[i] = *(LAS u32x4*)(smem + (xa ^ ((i & 3) * 32)) + ((i >> 2) % 5) * 1024); };
    auto perm = [&](int T) AI {
      const int st = T % 18, b = 18 * half(T / 18) + (st & ~1); const unsigned sel = (st & 1) ? 0x07060302u : 0x05040100u;
      u32x4 o;
      o[0] = __builtin_amdgcn_perm(xf[b][1], xf[b][0], sel); o[1] = __builtin_amdgcn_perm(xf[b][3], xf[b][2], sel);
      o[2] = __builtin_amdgcn_perm(xf[b + 1][1], xf[b + 1][0], sel); o[3] = __builtin_amdgcn_perm(xf[b + 1][3], xf[b + 1][2], sel);
      PIN(o); pm[st & 1] = o;
    };
    auto gstep = [&](int t, int u, int s, int z) AI {
      float* T_ = gt[z]; float* E_ = ge[z]; const int i = s >> 1;
      auto v = [&](int n) AI { return acc[t][8 * u + n]; };
      if (s == 0) { T_[0] = v(0) * v(0); PIN(T_[0]); }
      if ((s & 1) == 0) {
        if (i < 8) { T_[i] = v(i) * __builtin_fmaf(T_[i], k1, k0); PIN(T_[i]); }
        if (i >= 1) { E_[i - 1] = __builtin_amdgcn_rcpf(__builtin_fmaf(E_[i - 1], sdf, sdf)); PIN(E_[i - 1]); }
      } else {
        if (i < 8) { E_[i] = __builtin_amdgcn_exp2f(T_[i]); PIN(E_[i]); }
        if (i >= 1) { T_[i - 1] = v(i - 1) * E_[i - 1]; if ((i & 1) == 0) { unsigned w_ = pk2(T_[i - 2], T_[i - 1]); PIN(w_); go[z][(i >> 1) - 1] = w_; } else PIN(T_[i - 1]); }
        if (i + 1 < 8) { T_[i + 1] = v(i + 1) * v(i + 1); PIN(T_[i + 1]); }
      }
      if (s == 17 && (ABL & 2)) { PIN(go[z]); mt[t] = cnt; } else if (s == 17) { st16x<HAUX>(rw, hoff, hso + (t == 0 ? flh : fl0 + 64 * (t - 1) + 32 * SET) * 64 + u * 512, go[z]); mt[t] = ++cnt; }
    };
    const int gl = 36 * c + (lane < 35 ? lane : 35);
    auto gather = [&]() AI {
      BLD("dwordx2", sg, dws, r * 8, WS_ST + (rt * 128 + c * 32) * 8, " sc1");
#pragma unroll
      for (int i = 0; i < 3 * (1 - SET); i++) BLD("dwordx4", cq[i], dws, gl * 16, WS_CS + i * 36864 + nt * 2304, " sc1");
    };
    WAITV(4 - SET + (PREF == 2)); BARX();
    bs[0] = tbM; PIN(bs[0]);
    xread(0); xread(1);
#pragma unroll
    for (int F = 0; F < D; F++) fetch(F);
    xread(2); xread(3);
    if (SET == 1) perm(0);
    SB();
    sfor<180>([&](auto TC_) AI {
      constexpr int T = decltype(TC_)::v, k = T / 18, st = T % 18, F = T + D, t = tile(k);
      if constexpr (T == 5) { WAITL(2); BARX(); }
      if constexpr (T == 9) { }
      if constexpr (T == 14) { }
      if constexpr (T == 19) { }
      if constexpr (T == 32) { }
      if constexpr (T == 26) { }
      if constexpr (T == 26) { }
      if constexpr (T == 32) { }
      if constexpr (T == 33) { }
      if constexpr (T == 41) { }
      if constexpr (T == 43) { }
      if constexpr (T == 44) { }
      if constexpr (T == 48) { }
      if constexpr (T == 49) { }
      if constexpr (T == 50) { }
      if constexpr (T == 51) { }
      if constexpr (T == 55) { }
      if constexpr (T == 61) { }
      if constexpr (T == 65) { }
      if constexpr (T == 66) { }
      if constexpr (T == 68) { }
      if constexpr (T == 158) { }
      if constexpr (T == 159) { }
      if constexpr (T == 162) { }
      if constexpr (T == 166) { }
      if constexpr (T == 170) { }
      if constexpr (T == 171) { }
      if constexpr (T == 174) { }
      if constexpr (T == 176) { }
      if constexpr (T == 177) { }
      if constexpr (T == 65) { if (SET == 1) WAITL(4); BARX(); }
      if constexpr (F % 18 == 0) {
        constexpr int kb = F / 18;
        if (kb <= 5 || (kb & 1) == SET) WAITV(cnt - mark[kb]);
        else if (kb == 6 || kb == 10) WAITV(cnt - mx);
        else if (kb == 7) WAITV(cnt - mw);
        else if (kb == 8) WAITV(cnt - mt[1]);
        if (kb <= 2 || kb == 10) WAITL(0); else if (kb == 3) WAITL(2); else if (kb == 4) WAITL(4);
        if (kb == 1) { } if (kb == 2) { } BARX();
        if (kb == (TWS > 50 ? 7 : 6) && SET == 0) { put_flag8(rw, tid, WS_F0 + lid * 4, tag); cnt++; }
        if ((kb == 8 || kb == 10) && SET == 0) { put_flag(rw, tid == 0, (kb == 8 ? WS_FA : WS_F1) + lid * 4, tag); cnt++; }
        if constexpr (kb < 10) { bs[kb] = (isM(kb) ? tbM : tbP) + slotof(kb) * SLOT; PIN(bs[kb]); }
      }
      if constexpr (F < 180) fetch(F);
      if constexpr (SET == 1 && isM(k)) {
        const int a = (st & 1) ? 5 : 0;
        acc[a] = mfma32(pm[st & 1], ring[T % R], (k == 0 && st < 2) ? zero : acc[a]);
        if (st < 17) perm(T + 1);
      } else {
        acc[t] = mfma32(ring[T % R], xf[18 * half(k) + st], (half(k) == 0 && st == 0) ? zero : acc[t]);
      }
      if (SET == 1 && T == 35) perm(36);
      if constexpr (T < 4) { xread(4 + 4 * T); xread(5 + 4 * T); xread(6 + 4 * T); xread(7 + 4 * T); }
      if constexpr (T >= 32 && T < 38) { xread(20 + 3 * (T - 32)); xread(21 + 3 * (T - 32)); xread(22 + 3 * (T - 32)); }
      constexpr int xp = T == 5 ? 0 : T == 7 ? 1 : T == 8 ? 2 : T == 10 ? 3 : T == 11 ? 4 : T == 13 ? 5 : T == 24 ? 6 : T == 25 ? 7 : -1;
      if constexpr (xp >= 0) { GLDS(dx, (xp & 1) ? xvo1 : xvo, 0, 640 + 128 * (xp >> 1), xlb, (xp & 1) * 5120 + 1024 * (xp >> 1)); mark[2] = ++cnt; }
      if constexpr (T < 5 && 2 * T + 1 - SET < 9) { GLDS(dw1, wvo, wso, (2 * T + 1 - SET) * 32 * 9216, wlb, SLOT + 4096 * (2 * T + 1 - SET)); mark[1] = ++cnt; }
      if constexpr (F >= 18 && F < 180) {
        constexpr int kb = F / 18, fs = F % 18;
        constexpr int ks = kb <= 2 ? (fs < 9 ? kb + 1 : -1) : kb == 3 ? (fs < 9 ? 4 : 5) : (fs < 9 ? kb + 2 : -1);
        constexpr int o = (SET + ks) & 1;
        constexpr int n = ks <= 5 ? ((fs % 9) % 2 == o ? fs % 9 : 9) : (o == 0 ? fs : 9);
        if constexpr (ks >= 2 && n < 9 && !((ABL & 1) && ks >= 5 && ks < 10)) {
          if constexpr (ks == 2) GLDS(dm, mvo, mso + mhf + n * mstep, 0, mlb, 1024 * n);
          else if constexpr (ks <= 9) GLDS(dw1, wvo, wso, half(ks) * 288 * 9216 + (tile(ks) - 1) * 128 + n * 32 * 9216, wlb, slotof(ks) * SLOT + 4096 * n);
          else {
            const int e = 64 * n + lane; w2o[n] = (e / 18) * 1152 + (e % 18) * 16;
            if (n == 0) w2s = w2off(ks - 10);
            w2stage(w2s, ks - 10, n);
          }
          mark[ks] = ++cnt;
        }
      }
      if constexpr (T == 10) asm volatile("s_waitcnt vmcnt(%3)" : "+v"(uu[0]), "+v"(uu[1]), "+v"(uu[2]) : "i"(cnt + 1 - SET + (PREF == 2)) : "memory");
      if constexpr (T == 10 || T == 11) {
#pragma unroll
        for (int i = 2 * (T - 10); i < 2 * (T - 10) + 2; i++) {
          cs[2 * i] = blo(uu[0][i]) + blo(uu[1][i]) + blo(uu[2][i]); cs[2 * i + 1] = bhi(uu[0][i]) + bhi(uu[1][i]) + bhi(uu[2][i]);
          PIN(cs[2 * i]); PIN(cs[2 * i + 1]);
        }
      }
      if constexpr (T == 12) {
#pragma unroll
        for (int i = 0; i < 4; i++) { cs[i] = swadd32(cs[i], cs[i + 4]); PIN(cs[i]); }
      }
      if constexpr (T == 13) {
        const float a = dppadd<0x128>(swadd16(cs[0], cs[2])), b = dppadd<0x128>(swadd16(cs[1], cs[3]));
        *(LAS float*)(smem + SP_CS + (lane & 7) * 256 + (lane >> 3) * 32 + wv * 4) = (lane & 8) ? b : a;
      }
      if constexpr (SET == 0 && T == 14) { cr[0] = *(LAS f32x4*)(smem + SP_CS + lane * 32); cr[1] = *(LAS f32x4*)(smem + SP_CS + lane * 32 + 16); }
      if constexpr (SET == 0 && T == 16) {
        const float s = ((cr[0][0] + cr[0][1]) + (cr[0][2] + cr[0][3])) + ((cr[1][0] + cr[1][1]) + (cr[1][2] + cr[1][3]));
        __builtin_amdgcn_raw_buffer_store_b64(u32x2{pk2(s, s - bfr(s)), tag}, rw, (c == 0 && cu < 54) ? lane * 8u : OOB, WS_CS + g3 * 36864 + cg * 512, 16);
        cnt++;
      }
      if constexpr (T == 41) {
        gather();
        if (SET == 0) BLD("dword", bia, mkdesc(b1, 9216), gl * 4, 576 * nt, "");
        else BLD("dwordx2", fc, mkdesc(&ST.fc, 2304), r * 8, (has_dft ? nt : 0) * 256, "");
        cnt += SET == 0 ? 5 : 2; mpf = cnt;
      }
      if constexpr (T == 48) {
        auto gwait = [&]() AI {
          if (SET == 0) asm volatile("s_waitcnt vmcnt(%5)" : "+v"(sg), "+v"(cq[0]), "+v"(cq[1]), "+v"(cq[2]), "+v"(bia) : "i"(cnt - mpf) : "memory");
          else asm volatile("s_waitcnt vmcnt(%2)" : "+v"(sg), "+v"(fc) : "i"(cnt - mpf) : "memory");
          unsigned m = sg[1] ^ tag;
          if (SET == 0) m |= (cq[0][1] ^ tag) | (cq[0][3] ^ tag) | (cq[1][1] ^ tag) | (cq[1][3] ^ tag) | (cq[2][1] ^ tag) | (cq[2][3] ^ tag);
          return __builtin_amdgcn_ballot_w64(m != 0) != 0;
        };
        for (int it = 0; gwait(); it++) {
          if (it >= SPIN) { put_flag(rw, true, WS_ERR, 2); break; }
          __builtin_amdgcn_s_sleep(4);
          gather();
          WAITV(0);
        }
        if (SET == 0) {
          *(LAS u32x4*)(smem + SP_T + gl * 32) = u32x4{cq[0][0], cq[1][0], cq[2][0], bia & 0xffffu};
          *(LAS u32x4*)(smem + SP_T + gl * 32 + 16) = u32x4{cq[0][2], cq[1][2], cq[2][2], bia >> 16};
        }
      }
      if constexpr (T == 51) {
        const unsigned nm = hh ? 0u : ((sg[0] & 0xffffu) ^ 0x8000u) * 0x10001u;
        sf = u32x4{nm, nm, nm, hh ? 0u : sg[0] >> 16};
        sdf = bhi(sg[0]);
        PIN(sf); PIN(sdf);
      }
      if constexpr (SET == 1 && T == 52) *(LAS float*)(smem + SP_RS + c * 128 + r * 4) = __builtin_amdgcn_rcpf(sdf);
      if constexpr (T == (SET == 0 ? 52 : 70)) {
        const float rstd = __builtin_amdgcn_rcpf(sdf), c0 = -2.0f * 0.7978845608028654f * 1.4426950408889634f, c1 = c0 * 0.044715f;
        k0 = c0 * rstd; k1 = c1 * rstd * rstd * rstd;
        PIN(k0); PIN(k1);
      }
      if (SET == 0 && T == 51) tf = *(LAS u32x4*)(smem + SP_T + (flh + r) * 16);
      if (SET == 0 && T == 53) acc[0] = mfma32(tf, sf, acc[0]);
      if constexpr (!isM(k)) {
        if (half(k) == 1 && st == (k == 9 ? 13 : 15)) tf = *(LAS u32x4*)(smem + SP_T + (fl0 + 64 * (t - 1) + 32 * SET + r) * 16);
        if (half(k) == 1 && st == 17) acc[t] = mfma32(tf, sf, acc[t]);
      } else if constexpr (SET == 1 && k == 2 && st == 17) {
        acc[0] = mfma32(sf, u32x4{fc[0], 0, 0, 0}, acc[0]);
        acc[5] = mfma32(sf, u32x4{fc[1], 0, 0, 0}, acc[5]);
      }
      constexpr int TW = TWS;
      if constexpr (!(ABL & 16) && SET == 0 && ((T >= TW && T < TW + 4) || (T >= TW + 17 && T < TW + 21))) {
        const int wset = 8 * (lid >> 2) + (lane & 7), wp = 5 * c + (lane >> 3), wcol = 72 * q + (q == 3) + 4 * wp;
        const bool on = lane < 40 && wp < 19 && wcol <= 285;
        if constexpr (T < TW + 8) {
          constexpr int i = T - TW;
          if (i == 0) { wvv = ((16 * (wset >> 7) + (wset & 15) + 256 * ((wset >> 4) & 7)) * 289 + wcol) * 2u; PIN(wvv); wvv = on ? wvv : OOB; }
          BLD("dwordx2", wl[2 * i], mkdesc(wr, 2048 * 578), wvv, i * 64 * 578, WNT);
          BLD("dwordx2", wl[2 * i + 1], mkdesc(wi, 2048 * 578), wvv, i * 64 * 578, WNT);
          cnt += 2; mw = cnt;
        } else {
          constexpr int j = T - TW - 17;
          if (j == 0) {
            asm volatile("s_waitcnt vmcnt(%8)" : "+v"(wl[0]), "+v"(wl[1]), "+v"(wl[2]), "+v"(wl[3]), "+v"(wl[4]), "+v"(wl[5]), "+v"(wl[6]), "+v"(wl[7]) : "i"(cnt - mw) : "memory");
            wvv = wcol * 8192u + wset * 16; PIN(wvv); wvv = on ? wvv : OOB;
          }
          u32x4 o;
#pragma unroll
          for (int i = 0; i < 4; i++) o[i] = __builtin_amdgcn_perm(wl[2 * i + 1][j >> 1], wl[2 * i][j >> 1], (j & 1) ? 0x07060302u : 0x05040100u);
          st16(rw, wvv, WS_WT + j * 8192, o);
          mw = ++cnt;
        }
      }
      if constexpr (SET == 0) {
        if constexpr (T == 161) { BLD("dwordx4", pf, dws, 0, WS_FA + (lid >> 2) * 16, " sc1"); mpf = ++cnt; }
        if constexpr (T == 168) {
          asm volatile("s_waitcnt vmcnt(%1)" : "+v"(pf) : "i"(cnt - mpf) : "memory");
          poll4(rw, dws, pf, 0, WS_FA + (lid >> 2) * 16, tag);
        }
        if constexpr (T >= 168 && (T - 168) % 3 == 0) hload((T - 168) / 3);
      }
      if constexpr (SET == 1) {
        if constexpr (T == 141) { BLD("dwordx4", pf, dws, lane * 16, WS_F0 + xcd * 1024, " sc1"); mpf = ++cnt; }
        if constexpr (T == 150) {
          asm volatile("s_waitcnt vmcnt(%1)" : "+v"(pf) : "i"(cnt - mpf) : "memory");
          poll4(rw, dws, pf, lane * 16, WS_F0 + xcd * 1024, tag);
        }
        if constexpr (T == 151 || T == 152) { GLDSP(dws, q4 * 256 + g * 64 + p4 * 16, kc[0] * 8192 + c * 2048, WS_XS + (T - 151) * 1024, c * 2048, SP + (T - 151) * 1024, " sc1"); mx = ++cnt; }
        if constexpr (T >= 168 && T < 176) { tload(T < 172 ? fa : f32, T < 172 ? c * 6144 : 49152, T & 3); cnt++; }
      }
      if constexpr (PREF == 1 && (SET == 0 ? (T == 77 || T == 113) : T == 107)) {
        const int u = wv * 32 + (rt & 7) * 4 + q;
        const u32x4 dp = u < 162 ? dw2 : u < 185 ? mkdesc(&GT, sizeof(GT)) : dst;
        GLDS1(dp, (c >> 1) == (T == 113) ? lane * 128u : OOB, u < 162 ? bb * 1327104 + u * 8192 : u < 185 ? bb * 184320 + (u - 162) * 8192 : (u - 185) * 8192, SP_PF);
        cnt++;
      }
      if constexpr (PREF == 3 && SET == 1 && (T == PFA || T == PFB || T == PFC)) {
        const int u = c * 32 + slot, xh = xcd >> 2;
        const u32x4 dp = u < 1 ? dst : u < 82 ? dw2 : mkdesc(&GT, sizeof(GT));
        GLDS1(dp, c == (T == PFA ? 0 : T == PFB ? 1 : 2) ? lane * 128u : OOB, u < 1 ? xcd * 8192 : u < 82 ? bb * 1327104 + (xh * 81 + u - 1) * 8192 : bb * 184320 + (xh * 9 + u - 82) * 8192, SP_PF);
        cnt++;
      }
      if constexpr (TWS > 50 && SET == 1 && (T == PFW || T == PFW + 1)) {
        const int w0 = 8 * (lid >> 2);
        GLDS1(mkdesc(T == PFW ? wr : wi, 2048 * 578), (c == 3 && lane < 38) ? lane * 128u : OOB, (16 * (w0 >> 7) + (w0 & 15) + 256 * ((w0 >> 4) & 7) + 64 * q) * 578, SP_PF);
        cnt++;
      }
      if constexpr (SET == 0) {
        if constexpr (T >= 55 && T < 73) gstep(0, 0, T - 55, 2);
        if constexpr (T >= 73 && T < 109 && ((T - 73) & 1) == 0) gstep(0, 1, (T - 73) >> 1, 2);
      } else if constexpr (T >= 55 && T < 61) {
        constexpr int u = (T - 55) / 3, ph = (T - 55) % 3;
        const int bc = 32 * nt + r;
        const float b1c = bc == 144 ? 0.f : 1.f, b2c = bc < 144 ? -1.f : 1.f, a2c = -b1c * b2c;
        if (ph == 0) { cr[0] = *(LAS f32x4*)(smem + SP_RS + c * 128 + u * 64 + hh * 16); cr[1] = *(LAS f32x4*)(smem + SP_RS + c * 128 + u * 64 + 32 + hh * 16); }
        else {
          float o1[4], o2[4];
#pragma unroll
          for (int i = 0; i < 4; i++) {
            const float e = acc[0][8 * u + 4 * (ph - 1) + i] * cr[ph - 1][i], o = acc[5][8 * u + 4 * (ph - 1) + i] * cr[ph - 1][i];
            o1[i] = __builtin_fmaf(b1c, o, e); o2[i] = __builtin_fmaf(a2c, e, b2c * o);
          }
          LAS char* p = smem + r * 272 + (2 * u + hh) * 64 + c * 16 + (ph - 1) * 8;
          *(LAS u32x2*)p = u32x2{pk2(o1[0], o1[1]), pk2(o1[2], o1[3])};
          *(LAS u32x2*)(p + 32 * 272) = u32x2{pk2(o2[0], o2[1]), pk2(o2[2], o2[3])};
        }
      } else if constexpr (T == 65) {
#pragma unroll
        for (int i = 0; i < 4; i++) xq[i] = *(LAS u32x4*)(smem + (16 * c + 4 * i + g) * 272 + l15 * 16);
      } else if constexpr (T >= 67 && T < 71) {
        const int bc = 32 * nt + 16 * (c & 1) + 4 * (T - 67) + g;
        const int p1 = 2 * bc - (bc > 144 ? 287 : 0);
        int p2 = (bc < 144 ? 576 : 865) - 2 * bc; p2 = bc == 0 ? 1 : p2; p2 = bc == 144 ? 289 : p2;
        unsigned vo = (c < 2 ? p1 : p2) * 4096u + l15 * 16; PIN(vo);
        st16(rw, (has_dft && !(ABL & 32)) ? vo : OOB, WS_XS + bh * 2048 + aq * 256, xq[T - 67]);
        mx = ++cnt;
      }
      if constexpr (T >= 73 && (ABL & 4)) { if constexpr ((T - 73) % 36 == 0) PIN(acc[(T - 73) / 36 + 1]); } else if constexpr (T >= 73) {
        const int pt = (T - 73) / 36 + 1, s = (T - 73) % 36;
        if (s < 18) gstep(pt, 0, s, 0);
        if (s >= 16 && s < 34) gstep(pt, 1, s - 16, 1);
      }
      SB();
    });
    if (SET == 1 && PRIO) __builtin_amdgcn_s_setprio(1);
#pragma unroll
    for (int s = 0; s < 18; s++) {
      if (s % 4 == 1 && s < 16 && SET == 0) hload(4 + s / 4);
      if (s >= 10 && s < 12 && SET == 1) { xload(0, s - 2); mx = ++cnt; }
      if ((s == 0 || s == 5) && SET == 1 && has2) {
#pragma unroll
        for (int i = 0; i < 5; i++) xload(1, s + i);
      }
      gstep(4, 0, s, 0); gstep(4, 1, s, 1);
    }
    mst = cnt;
  }
  if (SET == 1 && PRIO == 2) __builtin_amdgcn_s_setprio(0);
  if constexpr (SET == 0) {
    constexpr int D = DIST2, R = RING2;
    const unsigned tb = (16 * (g >> 1) + 4 * (g & 1) + q4) * 288 + p4 * 8;
    const unsigned zb = 2 * SLOT + (8 * (g >> 1) + 2 * (g & 1) + (q4 >> 1)) * 512 + (2 * (p4 >> 1) + (q4 & 1)) * 64 + ((c ^ (2 * (g & 1) + (q4 >> 1))) * 16) + (p4 & 1) * 8;
    u32x2 ra[R], rb[R];
    f32x4 acc[9][2];
    LAS char* bs[11];
    const f32x4 z4 = {0, 0, 0, 0};
    auto fad = [&](int F) AI { return bs[F / 36] + ((F % 36) / 9) * 9216 + (F % 9) * 32; };
    bs[0] = smem + tb; PIN(bs[0]);
#pragma unroll
    for (int F = 0; F < D; F++) { ra[F] = trd(fad(F)); rb[F] = trd(fad(F) + 2304); }
    SB();
    sfor<369>([&](auto GC_) AI {
      constexpr int G = decltype(GC_)::v, ks = G / 9, dt = G % 9, F = G + D;
      if constexpr (F % 36 == 0) {
        constexpr int j = F / 36;
        if (j == 1) { WAITV(cnt - mst); }
        if (j == 6 || j == 7) WAITL(0);
        if (j < 10) { BARX(); }
        if (j == 1) { put_flag(rw, tid == 0, WS_FH + lid * 4, tag); cnt++; }
        bs[j] = smem + (j < 10 ? (j & 3) * SLOT : SP) + tb; PIN(bs[j]);
      }
      if constexpr (dt == 0 && ks < 36) asm volatile("s_waitcnt vmcnt(%2)" : "+v"(hb[ks % HW][0]), "+v"(hb[ks % HW][1]) : "i"(cnt - mh[ks]) : "memory");
      const u32x4 a = {ra[G % R][0], ra[G % R][1], rb[G % R][0], rb[G % R][1]};
      acc[dt][0] = mfma16(a, hb[ks % HW][0], ks == 0 ? z4 : acc[dt][0]);
      SB();
      if constexpr (F < 369) ra[F % R] = trd(fad(F));
      SB();
      acc[dt][1] = mfma16(a, hb[ks % HW][1], ks == 0 ? z4 : acc[dt][1]);
      SB();
      if constexpr (F < 369) rb[F % R] = trd(fad(F) + 2304);
      if constexpr (G == 9 * (10 - HW) + 7 || G == 9 * (18 - HW) + 7) { BLD("dwordx4", pf, dws, 0, (ks + HW < 12 ? WS_F1 : WS_FH) + (lid >> 2) * 16, " sc1"); mpf = ++cnt; }
      if constexpr (dt == 8 && ks + HW < 36) {
        if (ks + HW == 12 || ks + HW == 20) {
          asm volatile("s_waitcnt vmcnt(%1)" : "+v"(pf) : "i"(cnt - mpf) : "memory");
          poll4(rw, dws, pf, 0, (ks + HW == 12 ? WS_F1 : WS_FH) + (lid >> 2) * 16, tag);
        }
        hload(ks + HW);
      }
      if constexpr (ks >= 35 && ks < 40 && (dt == 5 || dt == 6)) {
        constexpr int sp = ks - 35;
        hb[(ks + 1) % HW][dt - 5] = trd2(smem + zb + (dt - 5) * 256 + sp * 8192 - (sp == 4 ? (g >> 1) * 4096 : 0), 2048);
      }
      SB();
    });
    auto pk4 = [&](f32x4 a, f32x4 b) AI { return u32x4{pk2(a[0], a[1]), pk2(a[2], a[3]), pk2(b[0], b[1]), pk2(b[2], b[3])}; };
#pragma unroll
    for (int d = 0; d < 4; d++)
      if (d != bb) {
        const unsigned so = pslot(bb, (d - bb - 1) & 3);
        const int t0 = 5 * (d & 1);
        st16(rw, lane * 16, so, pk4(acc[t0][d >> 1], acc[t0 + 1][d >> 1]));
        st16(rw, lane * 16 + 1024, so, pk4(acc[t0 + 2][d >> 1], acc[t0 + 3][d >> 1]));
        const f32x4 a = acc[4][d >> 1];
        if ((d & 1) == 0) __builtin_amdgcn_raw_buffer_store_b64(u32x2{pk2(a[0], a[1]), pk2(a[2], a[3])}, rw, lane * 8 + 2048, so, 16);
      }
#pragma unroll
    for (int d = 0; d < 4; d++)
      if (d == bb) {
#pragma unroll
        for (int i = 0; i < 5 - (d & 1); i++) *(LAS f32x4*)(smem + 3 * SLOT + c * 5120 + i * 1024 + lane * 16) = acc[5 * (d & 1) + i][d >> 1];
      }
    WAITV(0);
    put_flag(rw, lane == 0, fio + bb * 4, tag);
    WAITL(0); BARX();
  } else {
    auto stage = [&](int j) AI {
      const unsigned so = w2off(j);
#pragma unroll
      for (int n = 0; n < 9; n++) w2stage(so, j, n);
      cnt += 9; mark[j] = cnt;
    };
    auto bar = [&](int j, int m) AI { WAITV(cnt - m); WAITL(0); if (j == 1) { } BARX(); };
    const u32x4 dg = mkdesc(&GT, sizeof(GT));
    f32x4 t1[4], t2[4];
    const f32x4 z4 = {0, 0, 0, 0};
    auto xpin = [&](int z) AI {
#pragma unroll
      for (int i = 0; i < 8; i++) PIN(xx[z][i]);
      PIN(wt[z][0]); PIN(wt[z][1]);
    };
    auto tpin = [&](u32x4 (&f)[2][3]) AI {
#pragma unroll
      for (int t = 0; t < 2; t++) { PIN(f[t][0]); PIN(f[t][1]); f[t][2] = f[t][1] ^ 0x80008000u; }
    };
    unsigned so;
    auto io = [&](int j, int n) AI {
      if (j < 0) return;
      SB();
      if (n == 0) so = w2off(j);
      w2stage(so, j, n); mark[j] = ++cnt;
      SB();
    };
#pragma unroll
    for (int i = 0; i < 8; i++) xx[0][i] = *(LAS u32x4*)(smem + SP + i * 1024 + lane * 16);
#pragma unroll
    for (int i = 0; i < 4; i++) tload(fb, 24576 + c * 6144, i);
    cnt += 4;
    const int mb = cnt;
    auto twid = [&](int w) AI {
      float f1 = (16 * c + l15) * (1.0f / 2048.0f), f2 = (16 * c + 4 * g) * (1.0f / 2048.0f);
      PIN(f1); PIN(f2);
#pragma unroll
      for (int at = 0; at < 2; at++)
#pragma unroll
        for (int i = 0; i < 4; i++) {
          const float a1 = (float)(16 * at + 4 * g + i) * f1, a2 = (float)(16 * at + l15) * (f2 + i * (1.0f / 2048.0f));
          if (w == 0) { t1[2 * at][i] = __builtin_amdgcn_cosf(a1); t1[2 * at + 1][i] = __builtin_amdgcn_sinf(a1); }
          else { t2[2 * at][i] = __builtin_amdgcn_cosf(a2) * (1.0f / 2048.0f); t2[2 * at + 1][i] = __builtin_amdgcn_sinf(a2) * (1.0f / 2048.0f); }
        }
#pragma unroll
      for (int i = 0; i < 4; i++) { if (w == 0) PIN(t1[i]); else PIN(t2[i]); }
    };
    twid(0);
#pragma unroll
    for (int i = 4; i < 8; i++) xx[0][i] &= lid == 0 ? 0u : ~0u;
    WAITV(cnt - mx);
    tpin(fa); tpin(f32); xpin(0);
    auto front = [&](int z, int j) AI {
      u32x4 br, bi, yr, yi;
#pragma unroll
      for (int at = 0; at < 2; at++) {
        f32x4 tr = z4, ti = z4;
#pragma unroll
        for (int t = 0; t < 2; t++) {
          const u32x4 xr = xx[z][2 * t + at], xi = xx[z][4 + 2 * t + at];
          tr = mfma16(xr, fa[t][0], tr); ti = mfma16(xi, fa[t][0], ti);
          tr = mfma16(xi, fa[t][1], tr); ti = mfma16(xr, fa[t][2], ti);
          io(j, 4 * at + 2 * t); io(j, 4 * at + 2 * t + 1);
        }
        const f32x4 co = t1[2 * at], si = t1[2 * at + 1];
        const f32x4 ur = tr * co + ti * si, ui = ti * co - tr * si;
        br[2 * at] = pk2(ur[0], ur[1]); br[2 * at + 1] = pk2(ur[2], ur[3]);
        bi[2 * at] = pk2(ui[0], ui[1]); bi[2 * at + 1] = pk2(ui[2], ui[3]);
      }
      if (j >= 0) { io(j, 8); PIN(br); PIN(bi); if (PRIO == 1) __builtin_amdgcn_s_setprio(0); bar(1, mst); j++; io(j, 0); twid(1); }
#pragma unroll
      for (int ct = 0; ct < 2; ct++) {
        f32x4 xr = mfma16(f32[ct][0], br, z4), xi = mfma16(f32[ct][0], bi, z4);
        xr = mfma16(f32[ct][1], bi, xr); xi = mfma16(f32[ct][2], br, xi);
        io(j, 2 * ct + 1); io(j, 2 * ct + 2);
        float vr[4], vi[4];
#pragma unroll
        for (int i = 0; i < 4; i++) { const unsigned w = wt[z][ct][i]; const float a = blo(w), b = bhi(w); vr[i] = xr[i] * a - xi[i] * b; vi[i] = xr[i] * b + xi[i] * a; }
        yr[2 * ct] = pk2(vr[0], vr[1]); yr[2 * ct + 1] = pk2(vr[2], vr[3]);
        yi[2 * ct] = pk2(vi[0], vi[1]); yi[2 * ct + 1] = pk2(vi[2], vi[3]);
      }
#pragma unroll
      for (int at = 0; at < 2; at++) {
        f32x4 pr = mfma16(yr, f32[at][0], z4), pi = mfma16(yi, f32[at][0], z4);
        pr = mfma16(yi, f32[at][2], pr); pi = mfma16(yr, f32[at][1], pi);
        io(j, 5 + 2 * at); io(j, 6 + 2 * at);
        const f32x4 co = t2[2 * at], si = t2[2 * at + 1];
        const f32x4 ur = pr * co - pi * si, ui = pi * co + pr * si;
        LAS char* p = smem + SP + (1 - z) * 8192 + (at * 4 + (c >> 1)) * 1024 + lane * 16 + (c & 1) * 8;
        *(LAS u32x2*)p = u32x2{pk2(ur[0], ur[1]), pk2(ur[2], ur[3])};
        *(LAS u32x2*)(p + 2048) = u32x2{pk2(ui[0], ui[1]), pk2(ui[2], ui[3])};
      }
    };
    front(0, 2);
    if (has2) { xpin(1); front(1, -1); }
    bar(2, mark[2]);
    tpin(fb);
    auto back = [&](int z, int j) AI {
      const int pair = kc[z] == 288 ? 0 : kc[z];
      const bool on = kc[z] == 0 ? (g & 1) == 0 : (kc[z] == 288 ? (g & 1) == 1 : true);
#pragma unroll
      for (int at = 0; at < 2; at++) {
        f32x4 zr = z4, zi = z4;
#pragma unroll
        for (int t = 0; t < 2; t++) {
          LAS char* p = smem + SP + (1 - z) * 8192 + (at * 4 + t) * 1024 + lane * 16;
          const u32x4 pr = *(LAS u32x4*)p, pi = *(LAS u32x4*)(p + 2048);
          io(j, 4 * at + 2 * t);
          zr = mfma16(fb[t][0], pr, zr); zi = mfma16(fb[t][0], pi, zi);
          zr = mfma16(fb[t][2], pi, zr); zi = mfma16(fb[t][1], pr, zi);
          io(j, 4 * at + 2 * t + 1);
        }
        const unsigned r0 = pk2(zr[0], zr[1]), r1 = pk2(zr[2], zr[3]);
        const unsigned i0 = kc[z] == 288 ? r0 : pk2(zi[0], zi[1]), i1 = kc[z] == 288 ? r1 : pk2(zi[2], zi[3]);
        const auto s0 = __builtin_amdgcn_permlane16_swap(r0, i0, false, false), s1 = __builtin_amdgcn_permlane16_swap(r1, i1, false, false);
        const unsigned a0 = s0[0], a1 = s1[0], a2 = s0[1], a3 = s1[1];
        st16(rw, on ? q4 * 1024u + g * 64 + ((p4 ^ (pair & 3)) * 16) : OOB, WS_Z + pair * 8192 + at * 4096 + c * 256, u32x4{a0, a1, a2, a3});
        if (z == 0) cnt++;
      }
    };
    if (has2) back(1, -1);
    back(0, 4);
    io(4, 8);
    bar(3, mark[3]);
    stage(5);
    bar(4, mark[4]);
    put_flag8(rw, tid - 256, WS_FZ + lid * 4, tag); cnt++;
    stage(6);
#pragma unroll
    for (int n = 0; n < 3; n++) { const int pc = n < 2 ? c + 4 * n : 8; GLDS(dg, lane * 16, (nt * 5 + 4) * 9216 + pc * 1024, 0, SP + pc * 1024, 0); }
    cnt += 3;
    bar(5, mark[5]);
    put_flag(rw, tid == 256, WS_F5 + lid * 4, tag); cnt++;
    stage(7);
    bar(6, mark[6]);
    const unsigned pvo = c == 0 ? lane * 16 : c == 1 ? 0u : OOB, pso = c == 0 ? WS_FZ + xcd * 1024 : WS_F5 + (lid >> 2) * 16;
#pragma unroll
    for (int n = 0; n < 9; n++) { io(8, n); if (n == 4) BLD("dwordx4", pf, dws, pvo, pso, " sc1"); }
#pragma unroll
    for (int n = 0; n < 3; n++) GLDS(dg, lane * 16, (nt * 5 + c) * 9216, 1024 * n, mlb, SLOT + 1024 * n);
    asm volatile("s_waitcnt vmcnt(7)" : "+v"(pf)::"memory");
    if (c < 2) poll4(rw, dws, pf, pvo, pso, tag);
    BARX();
#pragma unroll
    for (int n = 0; n < 9; n++) GLDSP(dws, hh * 8192 + r * 16, (72 * bb + 2 * c) * 8192 + rt * 512, WS_Z + 8 * n * 8192, c * 1024, 2 * SLOT + 4 * n * 1024, " sc1");
#pragma unroll
    for (int n = 3; n < 9; n++) GLDS(dg, lane * 16, (nt * 5 + c) * 9216, 1024 * n, mlb, SLOT + 1024 * n);
    put_flag(rw, lid == 0 && tid == 256, WS_E, tag);
    WAITV(16); BARX();
    const unsigned orow = (4 * aq + c + 32 * (32 * bh + 16 * (bb >> 1) + l15)) * 1152u, ocol = 288 * q + 160 * (bb & 1);
    u32x2 xv[5], bv[5];
#pragma unroll
    for (int i = 0; i < 5; i++) {
      const bool on = i < 4 || (bb & 1) == 0;
      BLD("dwordx2", xv[i], dx, on ? orow + g * 8 : OOB, ocol + 32 * i, "");
      BLD("dwordx2", bv[i], mkdesc(b2, 1152), on ? g * 8 : OOB, ocol + 32 * i, "");
    }
    BLD("dwordx4", pf, dws, 0, fio, " sc1");
    if (OTOUCH) GLDS1(mkdesc(out, 2048 * 1152), orow + g * 48, ocol, 3 * SLOT + 24576);
    bar(9, cnt - 12 - OTOUCH);
    asm volatile("s_waitcnt vmcnt(0)" : "+v"(pf), "+v"(xv[0]), "+v"(xv[1]), "+v"(xv[2]), "+v"(xv[3]), "+v"(xv[4]), "+v"(bv[0]), "+v"(bv[1]), "+v"(bv[2]), "+v"(bv[3]), "+v"(bv[4])::"memory");
    f32x4 own[5];
#pragma unroll
    for (int i = 0; i < 5; i++) {
      own[i] = f32x4{blo(xv[i][0]) + blo(bv[i][0]), bhi(xv[i][0]) + bhi(bv[i][0]), blo(xv[i][1]) + blo(bv[i][1]), bhi(xv[i][1]) + bhi(bv[i][1])};
      PIN(own[i]);
    }
    __builtin_amdgcn_s_sleep(TSLEEP);
    poll4(rw, dws, pf, 0, fio, tag);
    u32x4 pp[3][2];
    u32x2 p2[3];
#pragma unroll
    for (int s = 0; s < 3; s++) {
      BLD("dwordx4", pp[s][0], dws, lane * 16, pslot((bb + 1 + s) & 3, 2 - s), " sc1");
      BLD("dwordx4", pp[s][1], dws, lane * 16, pslot((bb + 1 + s) & 3, 2 - s), " offset:1024 sc1");
      BLD("dwordx2", p2[s], dws, (bb & 1) == 0 ? lane * 8 : OOB, pslot((bb + 1 + s) & 3, 2 - s), " offset:2048 sc1");
    }
    BARX();
#pragma unroll
    for (int i = 0; i < 5; i++) { own[i] += *(LAS f32x4*)(smem + 3 * SLOT + c * 5120 + i * 1024 + lane * 16); PIN(own[i]); }
    asm volatile("s_waitcnt vmcnt(0)" : "+v"(pp[0][0]), "+v"(pp[0][1]), "+v"(p2[0]), "+v"(pp[1][0]), "+v"(pp[1][1]), "+v"(p2[1]), "+v"(pp[2][0]), "+v"(pp[2][1]), "+v"(p2[2])::"memory");
    const rsrc_t ro = mkrsrc(out, 2048 * 1152);
    u32x2 ow[5];
#pragma unroll
    for (int i = 0; i < 5; i++) {
      float v[4];
#pragma unroll
      for (int e = 0; e < 4; e++) {
        auto pw = [&](int s) AI { return i < 4 ? pp[s][(i >> 1) & 1][2 * (i & 1) + (e >> 1)] : p2[s][e >> 1]; };
        const unsigned w0 = pw(0), w1 = pw(1), w2_ = pw(2);
        v[e] = (e & 1) ? (own[i][e] + bhi(w0)) + (bhi(w1) + bhi(w2_)) : (own[i][e] + blo(w0)) + (blo(w1) + blo(w2_));
      }
      ow[i] = u32x2{pk2(v[0], v[1]), pk2(v[2], v[3])};
      if (i & 1) {
        const auto s0 = __builtin_amdgcn_permlane16_swap(ow[i - 1][0], ow[i][0], false, false), s1 = __builtin_amdgcn_permlane16_swap(ow[i - 1][1], ow[i][1], false, false);
        const unsigned a0 = s0[0], a1 = s1[0], a2 = s0[1], a3 = s1[1];
        st16x<OAUX>(ro, orow + (g & 1) * 32 + (g >> 1) * 16, ocol + 32 * (i - 1), u32x4{a0, a1, a2, a3});
      }
    }
    __builtin_amdgcn_raw_buffer_store_b64(ow[4], ro, (bb & 1) == 0 ? orow + g * 8 : OOB, ocol + 128, OAUX);
    if (STAMPW) { WAITV(0); }
  }
  if (SET == 0) asm volatile(".Lkend:");
  };
  if (wv < 4) body(IC<0>{}); else body(IC<1>{});
}

extern "C" void kernel_launch(void* const* d_in, const int* in_sizes, int n_in, void* d_out, int out_size, void* d_ws, size_t ws_size, hipStream_t stream) {
  (void)hipFuncSetAttribute((const void*)kmain, hipFuncAttributeMaxDynamicSharedMemorySize, LDSZ);
  kmain<<<256, 512, LDSZ, stream>>>((const u16*)d_in[0], (const u16*)d_in[1], (const u16*)d_in[2], (const u16*)d_in[4], (const u16*)d_in[5],
                                    (const u16*)d_in[6], (const u16*)d_in[7], (u16*)d_out, (char*)d_ws);
}
```

```cpp
#include <hip/hip_runtime.h>
#include <hip/hip_bf16.h>
#include <utility>

typedef unsigned short u16;
typedef __attribute__((ext_vector_type(8))) __bf16 bf16x8;
typedef __attribute__((ext_vector_type(2))) __bf16 bf16x2;
typedef __attribute__((ext_vector_type(4))) short s16x4;
typedef __attribute__((ext_vector_type(8))) short s16x8;
typedef __attribute__((ext_vector_type(4))) float f32x4;
typedef __attribute__((ext_vector_type(16))) float f32x16;
typedef __attribute__((ext_vector_type(4))) unsigned u32x4;
typedef __attribute__((ext_vector_type(2))) unsigned u32x2;
#define LAS __attribute__((address_space(3)))
#define GAS __attribute__((address_space(1)))
#define DI __device__ __forceinline__

namespace tab {
constexpr double PI_ = 3.14159265358979323846;
constexpr double tsin(double x) { double t = x, s = x; for (int i = 1; i < 12; i++) { t *= -x * x / ((2 * i) * (2 * i + 1)); s += t; } return s; }
constexpr double tcos(double x) { double t = 1, s = 1; for (int i = 1; i < 12; i++) { t *= -x * x / ((2 * i - 1) * (2 * i)); s += t; } return s; }
constexpr double ccos(int n, int N) {
  n %= N; if (n < 0) n += N;
  if (2 * n > N) n = N - n;
  bool neg = false;
  if (4 * n > N) { n = N / 2 - n; neg = true; }
  double v = (8 * n > N) ? tsin(2 * PI_ * (N / 4 - n) / N) : tcos(2 * PI_ * n / N);
  return neg ? -v : v;
}
constexpr double csin(int n, int N) { return ccos(n - N / 4, N); }
constexpr u16 f2bf(double d) { float f = (float)d; unsigned u = __builtin_bit_cast(unsigned, f); u += 0x7fffu + ((u >> 16) & 1u); return (u16)(u >> 16); }
struct C576 { u16 c[576]; u16 g1[576]; u16 g2[576]; };
constexpr C576 mk576() { C576 r{}; for (int i = 0; i < 576; i++) { double c = ccos(i, 576); r.c[i] = f2bf(c); r.g1[i] = f2bf(c / 576.0); r.g2[i] = f2bf(2.0 * c / 576.0); } return r; }
constexpr C576 c576 = mk576();
struct Blk { u16 v[9216]; };
constexpr int fidx(int bc, int d) {
  int n = bc < 144 ? bc * d : (bc == 144 ? ((d & 1) == 0 ? 144 * d : 144 * d + 144) : (bc - 144) * d + 144);
  return n % 576;
}
constexpr Blk mkF(int idx) {
  Blk r{}; int at = idx >> 1, hf = idx & 1;
  for (int st = 0; st < 18; st++) for (int hh = 0; hh < 2; hh++) for (int j = 0; j < 8; j++) {
    int d = 288 * hf + 32 * (st >> 1) + 16 * (j >> 2) + 8 * hh + 2 * (j & 3) + (st & 1);
    for (int cc = 0; cc < 32; cc++) r.v[((st * 2 + hh) * 8 + j) * 32 + cc] = c576.c[fidx(32 * at + cc, d)];
  }
  return r;
}
template <int I> constexpr Blk fblk = mkF(I);
struct FTab { Blk b[18]; };
template <int... I> constexpr FTab mkFT(std::integer_sequence<int, I...>) { return FTab{{fblk<I>...}}; }
constexpr double bf2d(u16 b) { return (double)__builtin_bit_cast(float, (unsigned)b << 16); }
struct FCB { unsigned v[64]; };
constexpr FCB mkFC(int at) {
  FCB r{};
  for (int e = 0; e < 2; e++) for (int cc = 0; cc < 32; cc++) {
    double s = 0;
    for (int d = e; d < 576; d += 2) s += bf2d(c576.c[fidx(32 * at + cc, d)]);
    u16 hi = f2bf(s), lo = f2bf(s - bf2d(hi));
    r.v[cc * 2 + e] = (unsigned)hi | ((unsigned)lo << 16);
  }
  return r;
}
template <int I> constexpr FCB fcb = mkFC(I);
struct FCT { FCB b[9]; };
template <int... I> constexpr FCT mkFCT(std::integer_sequence<int, I...>) { return FCT{{fcb<I>...}}; }
struct GBlk { u16 v[4608]; };
constexpr GBlk mkG(int idx) {
  GBlk r{}; int step = idx % 5, q = (idx / 5) & 3, b = idx / 20;
  for (int row = 0; row < 32; row++) {
    int rho = 32 * step + row;
    if (rho >= 144) continue;
    int kc = 72 * b + (rho >> 1), ri = rho & 1;
    for (int c = 0; c < 144; c++) {
      int d = 144 * q + (c + 16 * (2 * b + 3)) % 144;
      r.v[row * 144 + c] = kc == 0 ? c576.g1[(ri * 288 * d) % 576] : c576.g2[(kc * d + ri * 144) % 576];
    }
  }
  return r;
}
template <int I> constexpr GBlk gblk = mkG(I);
struct GTab { GBlk b[80]; };
template <int... I> constexpr GTab mkGT(std::integer_sequence<int, I...>) { return GTab{{gblk<I>...}}; }
struct C2048 { float c[2048]; };
constexpr C2048 mk2048() { C2048 r{}; for (int i = 0; i < 2048; i++) r.c[i] = (float)ccos(i, 2048); return r; }
constexpr C2048 c2048 = mk2048();
constexpr u16 trig(int n2048, int type) {
  int n = type == 0 ? n2048 : (type == 1 ? n2048 + 1536 : n2048 + 512);
  return f2bf(c2048.c[n & 2047]);
}
struct F64T { u16 v[4][2][3][64][8]; };
constexpr F64T mkF64(int which) {
  F64T r{};
  for (int j = 0; j < 4; j++) for (int t = 0; t < 2; t++) for (int ty = 0; ty < 3; ty++) for (int l = 0; l < 64; l++) for (int jj = 0; jj < 8; jj++) {
    int g = l >> 4, o = 16 * j + (l & 15);
    int k = which == 0 ? 32 * t + 16 * (g >> 1) + 8 * (jj >> 2) + 4 * (g & 1) + (jj & 3) : 32 * t + 16 * (jj >> 2) + 4 * g + (jj & 3);
    r.v[j][t][ty][l][jj] = trig(o * k * 32, ty);
  }
  return r;
}
struct F32T { u16 v[2][3][64][8]; };
constexpr F32T mkF32() {
  F32T r{};
  for (int ct = 0; ct < 2; ct++) for (int ty = 0; ty < 3; ty++) for (int l = 0; l < 64; l++) for (int jj = 0; jj < 8; jj++) {
    int g = l >> 4, o = 16 * ct + (l & 15), k = 16 * (jj >> 2) + 4 * g + (jj & 3);
    r.v[ct][ty][l][jj] = trig(o * k * 64, ty);
  }
  return r;
}
struct TWT { float v[4][4][64][4]; };
constexpr TWT mkTW(int which) {
  TWT r{};
  for (int j = 0; j < 4; j++) for (int n = 0; n < 4; n++) for (int l = 0; l < 64; l++) for (int i = 0; i < 4; i++) {
    int g = l >> 4, at = n >> 1;
    int a = which == 0 ? 16 * at + 4 * g + i : 16 * at + (l & 15);
    int d = which == 0 ? 16 * j + (l & 15) : 16 * j + 4 * g + i;
    float v = c2048.c[(a * d + ((n & 1) ? 1536 : 0)) & 2047];
    r.v[j][n][l][i] = which == 0 ? v : v * (1.0f / 2048.0f);
  }
  return r;
}
constexpr F64T f64a = mkF64(0), f64b = mkF64(1);
constexpr F32T f32t = mkF32();
constexpr TWT tw1 = mkTW(0), tw2 = mkTW(1);
struct Small { F64T a, b; F32T f; TWT t1, t2; FCT fc; };
}
#if defined(__HIP_DEVICE_COMPILE__)
__device__ __attribute__((aligned(128))) static const tab::FTab FT = tab::mkFT(std::make_integer_sequence<int, 18>{});
__device__ __attribute__((aligned(128))) static const tab::GTab GT = tab::mkGT(std::make_integer_sequence<int, 80>{});
__device__ __attribute__((aligned(128))) static const tab::Small ST = {tab::f64a, tab::f64b, tab::f32t, tab::tw1, tab::tw2, tab::mkFCT(std::make_integer_sequence<int, 9>{})};
#else
__device__ static const tab::FTab FT = {};
__device__ static const tab::GTab GT = {};
__device__ static const tab::Small ST = {};
#endif
typedef unsigned long long u64;
DI float bfr(float x) { return (float)(__bf16)x; }
DI unsigned pk2(float a, float b) { bf16x2 c; c[0] = (__bf16)a; c[1] = (__bf16)b; return __builtin_bit_cast(unsigned, c); }
DI float blo(unsigned u) { return __builtin_bit_cast(float, u << 16); }
DI float bhi(unsigned u) { return __builtin_bit_cast(float, u & 0xffff0000u); }
DI f32x16 mfma32(u32x4 a, u32x4 b, f32x16 c) { return __builtin_amdgcn_mfma_f32_32x32x16_bf16(__builtin_bit_cast(bf16x8, a), __builtin_bit_cast(bf16x8, b), c, 0, 0, 0); }
DI f32x4 mfma16(u32x4 a, u32x4 b, f32x4 c) { return __builtin_amdgcn_mfma_f32_16x16x32_bf16(__builtin_bit_cast(bf16x8, a), __builtin_bit_cast(bf16x8, b), c, 0, 0, 0); }
DI u32x2 trd(LAS char* p) { return __builtin_bit_cast(u32x2, __builtin_amdgcn_ds_read_tr16_b64_v4i16((LAS s16x4*)p)); }
DI u32x4 trd2(LAS char* p, int off2) { u32x2 a = trd(p), b = trd(p + off2); u32x4 r; r[0] = a[0]; r[1] = a[1]; r[2] = b[0]; r[3] = b[1]; return r; }
template <int C> DI float dppadd(float v) { return v + __builtin_bit_cast(float, __builtin_amdgcn_mov_dpp(__builtin_bit_cast(int, v), C, 0xf, 0xf, true)); }
DI u32x4 mkdesc(const void* p, unsigned bytes) { u64 a = (u64)p; return u32x4{(unsigned)a, (unsigned)(a >> 32) & 0xffffu, bytes, 0x00020000u}; }
typedef __amdgpu_buffer_rsrc_t rsrc_t;
DI rsrc_t mkrsrc(const void* p, unsigned bytes) { return __builtin_amdgcn_make_buffer_rsrc((void*)p, 0, bytes, 0x00020000); }
DI void st16(rsrc_t r, unsigned voff, unsigned soff, u32x4 v) {
  __builtin_amdgcn_raw_buffer_store_b128(v, r, voff, soff, 16);
  asm volatile("s_nop 1" ::"v"(v) : "memory");
}
template <int AUX> DI void st16x(rsrc_t r, unsigned voff, unsigned soff, u32x4 v) {
  __builtin_amdgcn_raw_buffer_store_b128(v, r, voff, soff, AUX);
  asm volatile("s_nop 1" ::"v"(v) : "memory");
}
DI u32x4 ld16(rsrc_t r, unsigned voff, unsigned soff) { return __builtin_amdgcn_raw_buffer_load_b128(r, voff, soff, 16); }
#define GLDS(d, voff, sbase, simm, lbase, limm) GLDSP(d, voff, sbase, simm, lbase, limm, "")
#define GLDSP(d, voff, sbase, simm, lbase, limm, pol) do { unsigned t_; \
  asm volatile("s_add_i32 m0, %1, %2\n\ts_add_i32 %0, %3, %4\n\tbuffer_load_dwordx4 %5, %6, %0 offen" pol " lds" \
               : "=&s"(t_) : "s"(lbase), "i"(limm), "s"(sbase), "i"(simm), "v"(voff), "s"(d) : "memory", "scc"); } while (0)
#define GLDS1(d, voff, soff, lb) asm volatile("s_mov_b32 m0, %0\n\tbuffer_load_dword %1, %2, %3 offen lds" ::"i"(lb), "v"(voff), "s"(d), "s"(soff) : "memory")
#define WAITV(n) asm volatile("s_waitcnt vmcnt(%0)" ::"i"(n) : "memory")
#define WAITL(n) asm volatile("s_waitcnt lgkmcnt(%0)" ::"i"(n) : "memory")
#define PIN(x) asm volatile("" : "+v"(x))
#define BARX() asm volatile("s_barrier" ::: "memory")
#define SB() __builtin_amdgcn_sched_barrier(0)
#define AI __attribute__((always_inline))
#if defined(__HIP_DEVICE_COMPILE__)
#define NOPK __attribute__((target("no-packed-fp32-ops")))
#else
#define NOPK
#endif
template <int N> struct IC { static constexpr int v = N; };
template <class F, int... I> DI void sfor_(F&& f, std::integer_sequence<int, I...>) { (f(IC<I>{}), ...); }
template <int N, class F> DI void sfor(F&& f) { sfor_(f, std::make_integer_sequence<int, N>{}); }

#define BLD(op, dst, d, voff, soff, pol) asm volatile("buffer_load_" op " %0, %1, %2, %3 offen" pol : "=v"(dst) : "v"(voff), "s"(d), "s"(soff) : "memory")
DI float asf(unsigned u) { return __builtin_bit_cast(float, u); }
DI unsigned asu(float f) { return __builtin_bit_cast(unsigned, f); }
DI float swadd32(float a, float b) { auto s = __builtin_amdgcn_permlane32_swap(asu(a), asu(b), false, false); const unsigned x = s[0], y = s[1]; return asf(x) + asf(y); }
DI float swadd16(float a, float b) { auto s = __builtin_amdgcn_permlane16_swap(asu(a), asu(b), false, false); const unsigned x = s[0], y = s[1]; return asf(x) + asf(y); }
DI float half_sum(float v) {
  v = dppadd<0xB1>(v); v = dppadd<0x4E>(v); v = dppadd<0x141>(v); v = dppadd<0x140>(v);
  return swadd16(v, v);
}

constexpr int SLOT = 36864, SP = 4 * SLOT, LDSZ = 163840;
constexpr int SP_T = SP + 8192, SP_CS = SP_T + 4608, SP_RS = SP_T + 6656, SP_PF = SP_T + 7168;
constexpr unsigned WS_E = 0, WS_F1 = 1024, WS_FH = 2048, WS_FA = 3072, WS_F5 = 4096, WS_ERR = 5120, WS_ST = 8192, WS_F0 = 24576, WS_FZ = 32768;
constexpr unsigned WS_CS = 40960;
constexpr unsigned WS_H = WS_CS + 3 * 36864;
constexpr unsigned WS_XS = WS_H + 64u * 144 * 2048;
constexpr unsigned WS_Z = WS_XS + 576u * 4096;
constexpr unsigned WS_WT = WS_Z + 576u * 4096;
#define SELFV 1
#define STAMPW 0
constexpr unsigned WS_END = WS_WT + 289u * 8192;
constexpr int SPIN = 200000;
constexpr unsigned OOB = 0x80000000u;
DI void put_flag(rsrc_t rw, bool on, unsigned off, unsigned tag) { __builtin_amdgcn_raw_buffer_store_b32(tag, rw, on ? 0u : OOB, off, 16); }
DI void put_flag8(rsrc_t rw, unsigned t, unsigned off, unsigned tag) { __builtin_amdgcn_raw_buffer_store_b32(tag, rw, t < 8 ? t * 1024 : OOB, off, 16); }
DI bool bad4(u32x4 v, unsigned tag) { return __builtin_amdgcn_ballot_w64(v[0] != tag || v[1] != tag || v[2] != tag || v[3] != tag) != 0; }
DI void poll4(rsrc_t rw, u32x4 dws, u32x4 v, unsigned voff, unsigned soff, unsigned tag) {
  for (int it = 0; bad4(v, tag); it++) {
    if (it >= SPIN) { put_flag(rw, true, WS_ERR, 1); break; }
    __builtin_amdgcn_s_sleep(4);
    BLD("dwordx4", v, dws, voff, soff, " sc1");
    asm volatile("s_waitcnt vmcnt(0)" : "+v"(v)::"memory");
  }
}
#define DIST1 4
#define RING1 5
#define DIST2 4
#define RING2 5
#define ABL 0
#define PREF 3
#define PREFC 1
#define PREF2 103
#define PFA 26
#define PFB 42
#define PFC 43
#define HAUX 16
#define WNT ""
#define OAUX 16
#define PRIO 2
#define OTOUCH 1
#define TSLEEP 32
#define TWS 26
#define PFW 27
#define TGA 47
#define TGC 56
#define PRI1 0
#define TX0 150
#define UBAR 0
#define TSL2 4
#define B3L 0
#define FSTR 9216
__global__ __launch_bounds__(512, 2) NOPK void kmain(const u16* __restrict__ x, const u16* __restrict__ wr, const u16* __restrict__ wi,
                                                     const u16* __restrict__ w1, const u16* __restrict__ b1, const u16* __restrict__ w2,
                                                     const u16* __restrict__ b2, u16* __restrict__ out, char* __restrict__ ws) {
  extern __shared__ __attribute__((aligned(16))) char smem_[];
  LAS char* smem = (LAS char*)smem_;
  const int tid = threadIdx.x, lane = tid & 63;
  const int wv = __builtin_amdgcn_readfirstlane(tid >> 6);
  if (wv >= 4) {
    u64 pc; int l0;
    asm volatile("s_getpc_b64 %0\n.Lkpc:\n\ts_mov_b32 %1, .Lkend0-.Lkpc\n\ts_max_i32 %1, %1, .Lkend1-.Lkpc\n\ts_max_i32 %1, %1, .Lkend3-.Lkpc" : "=s"(pc), "=s"(l0)::"scc");
    GLDS1(mkdesc((const void*)pc, (unsigned)l0 < 0x40000u ? l0 : 0), lane * 128u + ((wv & 3) * 32 + (blockIdx.x >> 3)) * 8192u, 0, SP_PF);
  }
  auto body = [&](auto SETC) AI {
  constexpr int SET = decltype(SETC)::v & 1;
  constexpr bool ND = decltype(SETC)::v == 3;
  const int id = blockIdx.x, xcd = id & 7, slot = id >> 3;
  const int rt = (xcd >> 2) * 8 + (slot >> 2), bb = xcd & 3, q = slot & 3, nt = bb * 4 + q, lid = rt * 16 + nt;
  const int aq = rt >> 1, bh = rt & 1, c = wv & 3;
  const int fl0 = (nt & 1) * 32, flh = (nt & 1) ? 0 : 256;
  const bool has_dft = nt < 9;
  const rsrc_t rw = mkrsrc(ws, WS_END);
  const u32x4 dws = mkdesc(ws, WS_END), dx = mkdesc(x, 2048 * 1152), dw1 = mkdesc(w1, 576 * 9216), dw2 = mkdesc(w2, 4608 * 1152);
  const int r = lane & 31, hh = lane >> 5;
  unsigned ev, xc;
  u32x4 xs[2];
  asm volatile("s_load_dword %0, %1, 0x0" : "=s"(ev) : "s"(ws) : "memory");
  if (SET == 0) {
    const int tr = 8 * nt + 2 * c;
    const unsigned ro = (4 * aq + (tr >> 5) + 32 * (32 * bh + (tr & 31))) * 1152u;
    BLD("dwordx4", xs[0], dx, hh * 36864 + r * 16, ro, "");
    BLD("dwordx4", xs[1], dx, hh * 36864 + r * 16, ro, " offset:512");
    BLD("dword", xc, dx, hh * 36864 + r * 4, ro, " offset:1024");
  }
  const unsigned xvo = (4 * aq + (wv >> 1) + 32 * (32 * bh + 16 * (wv & 1) + (lane >> 3))) * 1152u + (((lane & 7) ^ (lane >> 4)) * 16);
  const unsigned xvo1 = (xvo ^ 64) + 8 * 32 * 1152;
  const unsigned xlb = 2 * SLOT + wv * 10240;
#pragma unroll
  for (int n = 0; n < 5; n++) { GLDS(dx, xvo, 0, 128 * n, xlb, 1024 * n); GLDS(dx, xvo1, 0, 128 * n, xlb, 5120 + 1024 * n); }
  const unsigned wvo = (8 * c + ((lane >> 2) & 7)) * 9216u + hh * 64 + (lane & 3) * 16;
  const unsigned wso = (288 * nt + fl0) * 2, wlb = c * 1024;
  const u32x4 dft = mkdesc(&FT, has_dft ? sizeof(FT) : 0);
  const u32x4 dm = c < 2 ? dw1 : dft;
  const unsigned mvo = c < 2 ? (144 * c + (lane >> 2)) * 9216u + (lane & 3) * 16 : lane * 16;
  const unsigned mso = c < 2 ? (288 * nt + flh) * 2 : nt * 36864 + (c & 1) * 9216;
  const unsigned mstep = c < 2 ? 16 * 9216 : 1024, mhf = c < 2 ? 288 * 9216 : 18432, mlb = c * 9216;
#pragma unroll
  for (int n = SET; n < 9; n += 2) GLDS(dm, mvo, mso + n * mstep, 0, mlb, 1024 * n);
  const int cu = (xcd >> 2) * 32 + slot, cg = 18 * bb + cu / 3, g3 = cu % 3;
  u32x4 uu[3];
#pragma unroll
  for (int n = 0; n < 3; n++) BLD("dwordx4", uu[n], dw1, (cu < 54 && !(ABL & 64)) ? (lane >> 3) * 9216u + (lane & 7) * 16 : OOB, (192 * g3 + 24 * wv + 8 * n) * 9216 + cg * 128, "");
  if (PREF == 2) {
    const int u = (7 - wv) * 32 + slot, xh = xcd >> 2, v = xcd * 37 + u - 106;
    const u32x4 dp = u < 81 ? dw2 : u < 93 ? mkdesc(&GT, sizeof(GT)) : u < 94 ? mkdesc(&ST, sizeof(ST)) : u < 106 ? mkdesc(&FT, sizeof(FT))
                   : mkdesc(v < 148 ? wr : wi, u < 143 ? 2048 * 578 : 0);
    const unsigned so = u < 81 ? bb * 1327104 + (xh * 81 + u) * 8192 : u < 93 ? bb * 184320 + (xh * 11 + u - 81) * 8192 : u < 94 ? xcd * 8192
                      : u < 106 ? (bb * 4 + (u - 94) / 3) * 36864 + 18432 + ((u - 94) % 3) * 6144 : (v < 148 ? v : v - 148) * 8192;
    GLDS1(dp, lane * 128u, so, SP_PF);
  }
  asm volatile("s_waitcnt lgkmcnt(0)" : "+s"(ev)::"memory");
  const unsigned tag = ev * 0x9E3779B1u + 0x7F4A7C15u;
  if (SET == 0) {
    asm volatile("s_waitcnt vmcnt(%3)" : "+v"(xs[0]), "+v"(xs[1]), "+v"(xc) : "i"(18 + (PREF == 2)) : "memory");
    float s = blo(xc) + bhi(xc);
#pragma unroll
    for (int i = 0; i < 8; i++) { const unsigned w = xs[i >> 2][i & 3]; s += blo(w) + bhi(w); }
    const float mean = half_sum(s) * (1.0f / 576.0f);
    float v = 0.f;
#pragma unroll
    for (int i = 0; i < 9; i++) { const unsigned w = i < 8 ? xs[(i >> 2) & 1][i & 3] : xc; const float d0 = blo(w) - mean, d1 = bhi(w) - mean; v = __builtin_fmaf(d0, d0, v); v = __builtin_fmaf(d1, d1, v); }
    const float var = half_sum(v) * (1.0f / 576.0f);
    __builtin_amdgcn_raw_buffer_store_b64(u32x2{pk2(mean, __builtin_sqrtf(bfr(bfr(var) + 1e-5f))), tag}, rw, r == 0 ? hh * 8u : OOB, WS_ST + (rt * 128 + 8 * nt + 2 * c) * 8, 16);
  }
  const int q4 = (lane & 15) >> 2, p4 = lane & 3, g = lane >> 4, l15 = lane & 15;
  const unsigned hoff = (r >> 4) * 1024 + hh * 256 + (r & 15) * 16;
  const unsigned hrow = WS_H + (4 * rt + c) * 144 * 2048;
  const unsigned hso = hrow + 9 * nt * 2048;
  unsigned w2o[9];
  constexpr auto ftile = [](int ks) constexpr {
    const int qq = ks < 12 ? ks / 3 : ks < 20 ? (ks - 12) / 2 : (ks - 20) / 4, e = ks < 12 ? ks % 3 : ks < 20 ? 3 + (ks & 1) : 5 + (ks & 3);
    return 9 * qq + ((qq & 1) ? e : (e == 0 ? 8 : e - 1));
  };
  auto w2off = [&](int j) AI {
    const unsigned t = ftile(4 * j) | ftile(4 * j + 1) << 8 | ftile(4 * j + 2) << 16 | ftile(4 * j + 3) << 24;
    return (36 * bb + ((t >> (8 * c)) & 255)) * 36864 + q * 288;
  };
  auto w2stage = [&](unsigned so, int j, int n) AI { GLDS(dw2, w2o[n], so, 0, mlb, (j & 3) * SLOT + 1024 * n); };
  int cnt = 0, mark[12] = {};
  constexpr int HW = 8;
  u32x4 hb[HW + 1][2];
  int mh[36];
  auto hload = [&](int ks) AI {
    const unsigned so = hrow + (36 * bb + ftile(ks)) * 2048;
    BLD("dwordx4", hb[ks % HW][0], dws, lane * 16, so, " sc1");
    BLD("dwordx4", hb[ks % HW][1], dws, lane * 16, so, " offset:1024 sc1");
    cnt += 2; mh[ks] = cnt;
  };
  u32x4 pf;
  int mpf, mst, mx;
  const unsigned fio = WS_XS + ((rt * 4 + q) * 4 + c) * FSTR;
  const u32x4 dst = mkdesc(&ST, sizeof(ST));
  const int e2 = q < 2 ? rt * 2 + q : 32;
  const bool has2 = ND && bb == 3 && (q < 2 || lid == 14);
  const int kc[2] = {lid, 256 + e2};
  u32x4 fa[2][3], fb[2][3], f32[2][3], xx[2][8], wt[2][2];
  auto xload = [&](int z, int i) AI {
    const bool cplx = kc[z] != 0 && kc[z] != 288;
    const unsigned vo = q4 * 256 + g * 64 + p4 * 16;
    if (i < 8) BLD("dwordx4", xx[z][i], dws, (i < 4 || cplx) ? vo : OOB, WS_XS + (kc[z] == 288 ? 1 : 2 * kc[z]) * 4096 + i * 1024, " sc1");
    else BLD("dwordx4", wt[z][i - 8], dws, lane * 16, WS_WT + kc[z] * 8192 + (c * 2 + i - 8) * 1024, " sc1");
  };
  auto tload = [&](u32x4 (&f)[2][3], unsigned so, int i) AI { BLD("dwordx4", f[i >> 1][i & 1], dst, lane * 16, so + (i >> 1) * 3072 + (i & 1) * 1024, ""); };

  {
    constexpr int D = DIST1, R = RING1;
    const unsigned tq = q4 * 64 + (g & 1) * 32 + p4 * 8;
    LAS char* const tbP = smem + hh * 1024 + tq + SET * 512;
    LAS char* const tbM = smem + hh * 512 + tq + SET * 18432;
    const unsigned xa = 2 * SLOT + (4 * c + (r >> 3)) * 5120 + (r & 7) * 128 + ((hh ^ ((r >> 1) & 7)) * 16);
    u32x4 xf[36], ring[R], pm[2], sf, tf, xq[4];
    u32x2 sg, fc, wl[8];
    u32x4 cq[3];
    unsigned bia, w2s, wvv;
    f32x16 acc[6];
    f32x4 cr[2];
    float cs[8], gt[3][8], ge[3][8], k0, k1, sdf;
    u32x4 go[3];
    LAS char* bs[11];
    int mw, mt[5];
    const f32x16 zero = {0, 0, 0, 0, 0, 0, 0, 0, 0, 0, 0, 0, 0, 0, 0, 0};
    constexpr auto isM = [](int k) constexpr { return k == 0 || k == 2; };
    constexpr auto tile = [](int k) constexpr { return k < 4 ? (k & 1) : (k >> 1); };
    constexpr auto half = [](int k) constexpr { return k < 4 ? (k >> 1) : (k & 1); };
    constexpr auto slotof = [](int k) constexpr { return k < 2 ? k : (k + 2) & 3; };
    auto fetch = [&](int F) AI { const int k = F / 18, st = F % 18; if (!(ND && isM(k))) ring[F % R] = trd2(bs[k] + st * (isM(k) ? 1024 : 2048), 256); };
    auto xread = [&](int i) AI { if (i < 36) xf[i] = *(LAS u32x4*)(smem + (xa ^ ((i & 3) * 32)) + ((i >> 2) % 5) * 1024); };
    auto perm = [&](int T) AI {
      const int st = T % 18, b = 18 * half(T / 18) + (st & ~1); const unsigned sel = (st & 1) ? 0x07060302u : 0x05040100u;
      u32x4 o;
      o[0] = __builtin_amdgcn_perm(xf[b][1], xf[b][0], sel); o[1] = __builtin_amdgcn_perm(xf[b][3], xf[b][2], sel);
      o[2] = __builtin_amdgcn_perm(xf[b + 1][1], xf[b + 1][0], sel); o[3] = __builtin_amdgcn_perm(xf[b + 1][3], xf[b + 1][2], sel);
      PIN(o); pm[st & 1] = o;
    };
    auto gstep = [&](int t, int u, int s, int z) AI {
      float* T_ = gt[z]; float* E_ = ge[z]; const int i = s >> 1;
      auto v = [&](int n) AI { return acc[t][8 * u + n]; };
      if (s == 0) { T_[0] = v(0) * v(0); PIN(T_[0]); }
      if ((s & 1) == 0) {
        if (i < 8) { T_[i] = v(i) * __builtin_fmaf(T_[i], k1, k0); PIN(T_[i]); }
        if (i >= 1) { E_[i - 1] = __builtin_amdgcn_rcpf(__builtin_fmaf(E_[i - 1], sdf, sdf)); PIN(E_[i - 1]); }
      } else {
        if (i < 8) { E_[i] = __builtin_amdgcn_exp2f(T_[i]); PIN(E_[i]); }
        if (i >= 1) { T_[i - 1] = v(i - 1) * E_[i - 1]; if ((i & 1) == 0) { unsigned w_ = pk2(T_[i - 2], T_[i - 1]); PIN(w_); go[z][(i >> 1) - 1] = w_; } else PIN(T_[i - 1]); }
        if (i + 1 < 8) { T_[i + 1] = v(i + 1) * v(i + 1); PIN(T_[i + 1]); }
      }
      if (s == 17 && (ABL & 2)) { PIN(go[z]); mt[t] = cnt; } else if (s == 17) { st16x<HAUX>(rw, hoff, hso + (t == 0 ? flh : fl0 + 64 * (t - 1) + 32 * SET) * 64 + u * 512, go[z]); mt[t] = ++cnt; }
    };
    const int gl = 36 * c + (lane < 35 ? lane : 35);
    auto gather = [&]() AI {
      BLD("dwordx2", sg, dws, r * 8, WS_ST + (rt * 128 + c * 32) * 8, " sc1");
#pragma unroll
      for (int i = 0; i < 3 * (1 - SET); i++) BLD("dwordx4", cq[i], dws, gl * 16, WS_CS + i * 36864 + nt * 2304, " sc1");
    };
    WAITV(4 - SET + (PREF == 2)); BARX();
    bs[0] = tbM; PIN(bs[0]);
    xread(0); xread(1);
#pragma unroll
    for (int F = 0; F < D; F++) fetch(F);
    xread(2); xread(3);
    if (SET == 1 && !ND) perm(0);
    SB();
    sfor<180>([&](auto TC_) AI {
      constexpr int T = decltype(TC_)::v, k = T / 18, st = T % 18, F = T + D, t = tile(k);
      if constexpr (T == 5) { WAITL(ND ? 0 : 2); BARX(); }
      if constexpr (T == 9) { }
      if constexpr (T == 14) { }
      if constexpr (T == 19) { }
      if constexpr (T == 32) { }
      if constexpr (T == 26) { }
      if constexpr (T == 26) { }
      if constexpr (T == 32) { }
      if constexpr (T == 33) { }
      if constexpr (T == 41) { }
      if constexpr (T == 43) { }
      if constexpr (T == 44) { }
      if constexpr (T == 48) { }
      if constexpr (T == 49) { }
      if constexpr (T == 50) { }
      if constexpr (T == 51) { }
      if constexpr (T == 55) { }
      if constexpr (T == 61) { }
      if constexpr (T == 65) { }
      if constexpr (T == 66) { }
      if constexpr (T == 68) { }
      if constexpr (T == 158) { }
      if constexpr (T == 159) { }
      if constexpr (T == 162) { }
      if constexpr (T == 166) { }
      if constexpr (T == 170) { }
      if constexpr (T == 171) { }
      if constexpr (T == 174) { }
      if constexpr (T == 176) { }
      if constexpr (T == 177) { }
      if constexpr (T == 65) { if (SET == 1) WAITL(4); BARX(); }
      if constexpr (F % 18 == 0) {
        constexpr int kb = F / 18;
        if (kb <= 5 || (kb & 1) == SET) WAITV(cnt - mark[kb]);
        else if ((kb == 6 && !ND) || kb == 10) WAITV(cnt - mx);
        else if (kb == 7) WAITV(cnt - mw);
        else if (kb == 8) WAITV(cnt - mt[1]);
        if (kb <= 2 || kb == 10) WAITL(0); else if (kb == 3) WAITL(B3L); else if (kb == 4) WAITL(4);
        if (kb == 1) { } if (kb == 2) { } BARX();
        if (kb == (TWS > 50 ? 7 : 6) && SET == 0) { put_flag8(rw, tid, WS_F0 + lid * 4, tag); cnt++; }
        if ((kb == 8 || kb == 10) && SET == 0) { put_flag(rw, tid == 0, (kb == 8 ? WS_FA : WS_F1) + lid * 4, tag); cnt++; }
        if constexpr (kb < 10) { bs[kb] = (isM(kb) ? tbM : tbP) + slotof(kb) * SLOT; PIN(bs[kb]); }
      }
      if constexpr (F < 180) fetch(F);
      if constexpr (ND && isM(k)) {
      } else if constexpr (SET == 1 && isM(k)) {
        const int a = (st & 1) ? 5 : 0;
        acc[a] = mfma32(pm[st & 1], ring[T % R], (k == 0 && st < 2) ? zero : acc[a]);
        if (st < 17) perm(T + 1);
      } else {
        acc[t] = mfma32(ring[T % R], xf[18 * half(k) + st], (half(k) == 0 && st == 0) ? zero : acc[t]);
      }
      if (SET == 1 && !ND && T == 35) perm(36);
      if constexpr (T < 4) { xread(4 + 4 * T); xread(5 + 4 * T); xread(6 + 4 * T); xread(7 + 4 * T); }
      if constexpr (T >= 32 && T < 38) { xread(20 + 3 * (T - 32)); xread(21 + 3 * (T - 32)); xread(22 + 3 * (T - 32)); }
      constexpr int xp = T == 5 ? 0 : T == 7 ? 1 : T == 8 ? 2 : T == 10 ? 3 : T == 11 ? 4 : T == 13 ? 5 : T == 24 ? 6 : T == 25 ? 7 : -1;
      if constexpr (xp >= 0) { GLDS(dx, (xp & 1) ? xvo1 : xvo, 0, 640 + 128 * (xp >> 1), xlb, (xp & 1) * 5120 + 1024 * (xp >> 1)); mark[2] = ++cnt; }
      if constexpr (T < 5 && 2 * T + 1 - SET < 9) { GLDS(dw1, wvo, wso, (2 * T + 1 - SET) * 32 * 9216, wlb, SLOT + 4096 * (2 * T + 1 - SET)); mark[1] = ++cnt; }
      if constexpr (F >= 18 && F < 180) {
        constexpr int kb = F / 18, fs = F % 18;
        constexpr int ks = kb <= 2 ? (fs < 9 ? kb + 1 : -1) : kb == 3 ? (fs < 9 ? 4 : 5) : (fs < 9 ? kb + 2 : -1);
        constexpr int o = (SET + ks) & 1;
        constexpr int n = ks <= 5 ? ((fs % 9) % 2 == o ? fs % 9 : 9) : (o == 0 ? fs : 9);
        if constexpr (ks >= 2 && n < 9 && !((ABL & 1) && ks >= 5 && ks < 10)) {
          if constexpr (ks == 2) GLDS(dm, mvo, mso + mhf + n * mstep, 0, mlb, 1024 * n);
          else if constexpr (ks <= 9) GLDS(dw1, wvo, wso, half(ks) * 288 * 9216 + (tile(ks) - 1) * 128 + n * 32 * 9216, wlb, slotof(ks) * SLOT + 4096 * n);
          else {
            const int e = 64 * n + lane; w2o[n] = (e / 18) * 1152 + ((e + 4 * bb + 6) % 18) * 16;
            if (n == 0) w2s = w2off(ks - 10);
            w2stage(w2s, ks - 10, n);
          }
          mark[ks] = ++cnt;
        }
      }
      if constexpr (T == 10) asm volatile("s_waitcnt vmcnt(%3)" : "+v"(uu[0]), "+v"(uu[1]), "+v"(uu[2]) : "i"(cnt + 1 - SET + (PREF == 2)) : "memory");
      if constexpr (T == 10 || T == 11) {
#pragma unroll
        for (int i = 2 * (T - 10); i < 2 * (T - 10) + 2; i++) {
          cs[2 * i] = blo(uu[0][i]) + blo(uu[1][i]) + blo(uu[2][i]); cs[2 * i + 1] = bhi(uu[0][i]) + bhi(uu[1][i]) + bhi(uu[2][i]);
          PIN(cs[2 * i]); PIN(cs[2 * i + 1]);
        }
      }
      if constexpr (T == 12) {
#pragma unroll
        for (int i = 0; i < 4; i++) { cs[i] = swadd32(cs[i], cs[i + 4]); PIN(cs[i]); }
      }
      if constexpr (T == 13) {
        const float a = dppadd<0x128>(swadd16(cs[0], cs[2])), b = dppadd<0x128>(swadd16(cs[1], cs[3]));
        *(LAS float*)(smem + SP_CS + (lane & 7) * 256 + (lane >> 3) * 32 + wv * 4) = (lane & 8) ? b : a;
      }
      if constexpr (SET == 0 && T == 14) { cr[0] = *(LAS f32x4*)(smem + SP_CS + lane * 32); cr[1] = *(LAS f32x4*)(smem + SP_CS + lane * 32 + 16); }
      if constexpr (SET == 0 && T == 16) {
        const float s = ((cr[0][0] + cr[0][1]) + (cr[0][2] + cr[0][3])) + ((cr[1][0] + cr[1][1]) + (cr[1][2] + cr[1][3]));
        __builtin_amdgcn_raw_buffer_store_b64(u32x2{pk2(s, s - bfr(s)), tag}, rw, (c == 0 && cu < 54) ? lane * 8u : OOB, WS_CS + g3 * 36864 + cg * 512, 16);
        cnt++;
      }
      if constexpr (T == 41) {
        gather();
        if (SET == 0) BLD("dword", bia, mkdesc(b1, 9216), gl * 4, 576 * nt, "");
        else BLD("dwordx2", fc, mkdesc(&ST.fc, 2304), r * 8, (ND ? 0 : nt) * 256, "");
        cnt += SET == 0 ? 5 : 2; mpf = cnt;
      }
      if constexpr (T == 48) {
        auto gwait = [&]() AI {
          if (SET == 0) asm volatile("s_waitcnt vmcnt(%5)" : "+v"(sg), "+v"(cq[0]), "+v"(cq[1]), "+v"(cq[2]), "+v"(bia) : "i"(cnt - mpf) : "memory");
          else asm volatile("s_waitcnt vmcnt(%2)" : "+v"(sg), "+v"(fc) : "i"(cnt - mpf) : "memory");
          unsigned m = sg[1] ^ tag;
          if (SET == 0) m |= (cq[0][1] ^ tag) | (cq[0][3] ^ tag) | (cq[1][1] ^ tag) | (cq[1][3] ^ tag) | (cq[2][1] ^ tag) | (cq[2][3] ^ tag);
          return __builtin_amdgcn_ballot_w64(m != 0) != 0;
        };
        for (int it = 0; gwait(); it++) {
          if (it >= SPIN) { put_flag(rw, true, WS_ERR, 2); break; }
          __builtin_amdgcn_s_sleep(4);
          gather();
          WAITV(0);
        }
        if (SET == 0) {
          *(LAS u32x4*)(smem + SP_T + gl * 32) = u32x4{cq[0][0], cq[1][0], cq[2][0], bia & 0xffffu};
          *(LAS u32x4*)(smem + SP_T + gl * 32 + 16) = u32x4{cq[0][2], cq[1][2], cq[2][2], bia >> 16};
        }
      }
      if constexpr (T == 51) {
        const unsigned nm = hh ? 0u : ((sg[0] & 0xffffu) ^ 0x8000u) * 0x10001u;
        sf = u32x4{nm, nm, nm, hh ? 0u : sg[0] >> 16};
        sdf = bhi(sg[0]);
        PIN(sf); PIN(sdf);
      }
      if constexpr (SET == 1 && !ND && T == 52) *(LAS float*)(smem + SP_RS + c * 128 + r * 4) = __builtin_amdgcn_rcpf(sdf);
      if constexpr (T == (SET == 0 ? 52 : 70)) {
        const float rstd = __builtin_amdgcn_rcpf(sdf), c0 = -2.0f * 0.7978845608028654f * 1.4426950408889634f, c1 = c0 * 0.044715f;
        k0 = c0 * rstd; k1 = c1 * rstd * rstd * rstd;
        PIN(k0); PIN(k1);
      }
      if (SET == 0 && T == 51) tf = *(LAS u32x4*)(smem + SP_T + (flh + r) * 16);
      if (SET == 0 && T == 53) acc[0] = mfma32(tf, sf, acc[0]);
      if constexpr (!isM(k)) {
        if (half(k) == 1 && st == (k == 9 ? 13 : 15)) tf = *(LAS u32x4*)(smem + SP_T + (fl0 + 64 * (t - 1) + 32 * SET + r) * 16);
        if (half(k) == 1 && st == 17) acc[t] = mfma32(tf, sf, acc[t]);
      } else if constexpr (SET == 1 && !ND && k == 2 && st == 17) {
        acc[0] = mfma32(sf, u32x4{fc[0], 0, 0, 0}, acc[0]);
        acc[5] = mfma32(sf, u32x4{fc[1], 0, 0, 0}, acc[5]);
      }
      constexpr int TW = TWS;
      if constexpr (!(ABL & 16) && SET == 0 && ((T >= TW && T < TW + 4) || (T >= TW + 17 && T < TW + 21))) {
        const int wset = 8 * (lid >> 2) + (lane & 7), wp = 5 * c + (lane >> 3), wcol = 72 * q + (q == 3) + 4 * wp;
        const bool on = lane < 40 && wp < 19 && wcol <= 285;
        if constexpr (T < TW + 8) {
          constexpr int i = T - TW;
          if (i == 0) { wvv = ((16 * (wset >> 7) + (wset & 15) + 256 * ((wset >> 4) & 7)) * 289 + wcol) * 2u; PIN(wvv); wvv = on ? wvv : OOB; }
          BLD("dwordx2", wl[2 * i], mkdesc(wr, 2048 * 578), wvv, i * 64 * 578, WNT);
          BLD("dwordx2", wl[2 * i + 1], mkdesc(wi, 2048 * 578), wvv, i * 64 * 578, WNT);
          cnt += 2; mw = cnt;
        } else {
          constexpr int j = T - TW - 17;
          if (j == 0) {
            asm volatile("s_waitcnt vmcnt(%8)" : "+v"(wl[0]), "+v"(wl[1]), "+v"(wl[2]), "+v"(wl[3]), "+v"(wl[4]), "+v"(wl[5]), "+v"(wl[6]), "+v"(wl[7]) : "i"(cnt - mw) : "memory");
            wvv = wcol * 8192u + wset * 16; PIN(wvv); wvv = on ? wvv : OOB;
          }
          u32x4 o;
#pragma unroll
          for (int i = 0; i < 4; i++) o[i] = __builtin_amdgcn_perm(wl[2 * i + 1][j >> 1], wl[2 * i][j >> 1], (j & 1) ? 0x07060302u : 0x05040100u);
          st16(rw, wvv, WS_WT + j * 8192, o);
          mw = ++cnt;
        }
      }
      if constexpr (SET == 0) {
        if constexpr (T == 161) { BLD("dwordx4", pf, dws, 0, WS_FA + (lid >> 2) * 16, " sc1"); mpf = ++cnt; }
        if constexpr (T == 168) {
          asm volatile("s_waitcnt vmcnt(%1)" : "+v"(pf) : "i"(cnt - mpf) : "memory");
          poll4(rw, dws, pf, 0, WS_FA + (lid >> 2) * 16, tag);
        }
        if constexpr (T >= 168 && (T - 168) % 3 == 0) hload((T - 168) / 3);
      }
      if constexpr (SET == 1) {
        if constexpr (T == 141) { BLD("dwordx4", pf, dws, lane * 16, WS_F0 + xcd * 1024, " sc1"); mpf = ++cnt; }
        if constexpr (T == 150) {
          asm volatile("s_waitcnt vmcnt(%1)" : "+v"(pf) : "i"(cnt - mpf) : "memory");
          poll4(rw, dws, pf, lane * 16, WS_F0 + xcd * 1024, tag);
        }
        if constexpr (T == 151 || T == 152) { GLDSP(dws, q4 * 256 + g * 64 + p4 * 16, kc[0] * 8192 + c * 2048, WS_XS + (T - 151) * 1024, c * 2048, SP + (T - 151) * 1024, " sc1"); mx = ++cnt; }
        if constexpr (T >= 168 && T < 176) { tload(T < 172 ? fa : f32, T < 172 ? c * 6144 : 49152, T & 3); cnt++; }
      }
      if constexpr (PREF == 1 && (SET == 0 ? (T == 77 || T == 113) : T == 107)) {
        const int u = wv * 32 + (rt & 7) * 4 + q;
        const u32x4 dp = u < 162 ? dw2 : u < 185 ? mkdesc(&GT, sizeof(GT)) : dst;
        GLDS1(dp, (c >> 1) == (T == 113) ? lane * 128u : OOB, u < 162 ? bb * 1327104 + u * 8192 : u < 185 ? bb * 184320 + (u - 162) * 8192 : (u - 185) * 8192, SP_PF);
        cnt++;
      }
      if constexpr (PREF == 3 && SET == 1 && (T == PFA || T == PFB || T == PFC)) {
        const int u = c * 32 + slot, xh = xcd >> 2;
        const u32x4 dp = u < 1 ? dst : u < 82 ? dw2 : u < 95 ? mkdesc(&GT, sizeof(GT)) : mkdesc(b2, 1152);
        GLDS1(dp, c == (T == PFA ? 0 : T == PFB ? 1 : 2) ? lane * 128u : OOB, u < 1 ? xcd * 8192 : u < 82 ? bb * 1327104 + (xh * 81 + u - 1) * 8192 : u < 95 ? bb * 184320 + (xh * 10 + u - 82) * 8192 : 0, SP_PF);
        cnt++;
      }
      if constexpr (TWS > 50 && SET == 1 && (T == PFW || T == PFW + 1)) {
        const int w0 = 8 * (lid >> 2);
        GLDS1(mkdesc(T == PFW ? wr : wi, 2048 * 578), (c == 3 && lane < 38) ? lane * 128u : OOB, (16 * (w0 >> 7) + (w0 & 15) + 256 * ((w0 >> 4) & 7) + 64 * q) * 578, SP_PF);
        cnt++;
      }
      if constexpr (SET == 0) {
        if constexpr (T >= 55 && T < 73) gstep(0, 0, T - 55, 2);
        if constexpr (T >= 73 && T < 109 && ((T - 73) & 1) == 0) gstep(0, 1, (T - 73) >> 1, 2);
      } else if constexpr (ND) {
      } else if constexpr (T >= 55 && T < 61) {
        constexpr int u = (T - 55) / 3, ph = (T - 55) % 3;
        const int bc = 32 * nt + r;
        const float b1c = bc == 144 ? 0.f : 1.f, b2c = bc < 144 ? -1.f : 1.f, a2c = -b1c * b2c;
        if (ph == 0) { cr[0] = *(LAS f32x4*)(smem + SP_RS + c * 128 + u * 64 + hh * 16); cr[1] = *(LAS f32x4*)(smem + SP_RS + c * 128 + u * 64 + 32 + hh * 16); }
        else {
          float o1[4], o2[4];
#pragma unroll
          for (int i = 0; i < 4; i++) {
            const float e = acc[0][8 * u + 4 * (ph - 1) + i] * cr[ph - 1][i], o = acc[5][8 * u + 4 * (ph - 1) + i] * cr[ph - 1][i];
            o1[i] = __builtin_fmaf(b1c, o, e); o2[i] = __builtin_fmaf(a2c, e, b2c * o);
          }
          LAS char* p = smem + r * 272 + (2 * u + hh) * 64 + c * 16 + (ph - 1) * 8;
          *(LAS u32x2*)p = u32x2{pk2(o1[0], o1[1]), pk2(o1[2], o1[3])};
          *(LAS u32x2*)(p + 32 * 272) = u32x2{pk2(o2[0], o2[1]), pk2(o2[2], o2[3])};
        }
      } else if constexpr (T == 65) {
#pragma unroll
        for (int i = 0; i < 4; i++) xq[i] = *(LAS u32x4*)(smem + (16 * c + 4 * i + g) * 272 + l15 * 16);
      } else if constexpr (T >= 67 && T < 71) {
        const int bc = 32 * nt + 16 * (c & 1) + 4 * (T - 67) + g;
        const int p1 = 2 * bc - (bc > 144 ? 287 : 0);
        int p2 = (bc < 144 ? 576 : 865) - 2 * bc; p2 = bc == 0 ? 1 : p2; p2 = bc == 144 ? 289 : p2;
        unsigned vo = (c < 2 ? p1 : p2) * 4096u + l15 * 16; PIN(vo);
        st16(rw, vo, WS_XS + bh * 2048 + aq * 256, xq[T - 67]);
        mx = ++cnt;
      }
      if constexpr (T >= 73 && (ABL & 4)) { if constexpr ((T - 73) % 36 == 0) PIN(acc[(T - 73) / 36 + 1]); } else if constexpr (T >= 73) {
        const int pt = (T - 73) / 36 + 1, s = (T - 73) % 36;
        if (s < 18) gstep(pt, 0, s, 0);
        if (s >= 16 && s < 34) gstep(pt, 1, s - 16, 1);
      }
      SB();
    });
    if (SET == 1 && PRIO) __builtin_amdgcn_s_setprio(1);
#pragma unroll
    for (int s = 0; s < 18; s++) {
      if (s % 4 == 1 && s < 16 && SET == 0) hload(4 + s / 4);
      if (s >= 10 && s < 12 && SET == 1) { xload(0, s - 2); mx = ++cnt; }
      if ((s == 0 || s == 5) && SET == 1 && has2) {
#pragma unroll
        for (int i = 0; i < 5; i++) xload(1, s + i);
      }
      gstep(4, 0, s, 0); gstep(4, 1, s, 1);
    }
    mst = cnt;
  }
  if (SET == 1 && PRIO == 2) __builtin_amdgcn_s_setprio(0);
  if constexpr (SET == 0) {
    constexpr int D = DIST2, R = RING2;
    const unsigned tb = (16 * (g >> 1) + 4 * (g & 1) + q4) * 288 + p4 * 8;
    const unsigned zb = 2 * SLOT + (8 * (g >> 1) + 2 * (g & 1) + (q4 >> 1)) * 512 + (2 * (p4 >> 1) + (q4 & 1)) * 64 + ((c ^ (2 * (g & 1) + (q4 >> 1))) * 16) + (p4 & 1) * 8;
    u32x2 ra[R], rb[R];
    f32x4 acc[9][2];
    LAS char* bs[11];
    const f32x4 z4 = {0, 0, 0, 0};
    constexpr auto ksof = [](int G) constexpr { return G < 288 ? G / 9 : G < 316 ? 32 + (G - 288) / 7 : G < 331 ? 36 + (G - 316) / 3 : G < 351 ? 36 + (G - 331) % 10 / 2 : 32 + (G - 351) / 2; };
    constexpr auto dtof = [](int G) constexpr { return G < 288 ? G % 9 : G < 316 ? (G - 288) % 7 : G < 331 ? (G - 316) % 3 : (G < 341 ? 3 : G < 351 ? 5 : 7) + (G - 331) % 2; };
    auto fad = [&](int F) AI { return bs[ksof(F) / 4] + (ksof(F) % 4) * 9216 + dtof(F) * 32; };
    auto pk4 = [&](f32x4 a, f32x4 b) AI { return u32x4{pk2(a[0], a[1]), pk2(a[2], a[3]), pk2(b[0], b[1]), pk2(b[2], b[3])}; };
    auto keep = [&](int t) AI {
      *(LAS f32x4*)(smem + 3 * SLOT + c * 6144 + (t - 6) * 2048 + lane * 16) = acc[t][0];
      *(LAS f32x4*)(smem + 3 * SLOT + c * 6144 + (t - 6) * 2048 + 1024 + lane * 16) = acc[t][1];
    };
    bs[0] = smem + tb; PIN(bs[0]);
#pragma unroll
    for (int F = 0; F < D; F++) { ra[F] = trd(fad(F)); rb[F] = trd(fad(F) + 2304); }
    SB();
    sfor<369>([&](auto GC_) AI {
      constexpr int G = decltype(GC_)::v, ks = ksof(G), dt = dtof(G), F = G + D, hi = ks < 36 ? ks % HW : ks - 32;
      if constexpr (F < 351 && ksof(F) % 4 == 0 && dtof(F) == 0) {
        constexpr int j = ksof(F) / 4;
        if (j == 1) { WAITV(cnt - mst); }
        if (j == 6 || j == 7) WAITL(0);
        if (j < 10) { BARX(); }
        if (j == 1) { put_flag(rw, tid == 0, WS_FH + lid * 4, tag); cnt++; }
        bs[j] = smem + (j < 10 ? (j & 3) * SLOT : SP) + tb; PIN(bs[j]);
      }
      if constexpr (dt == 0 && ks < 36) asm volatile("s_waitcnt vmcnt(%2)" : "+v"(hb[ks % HW][0]), "+v"(hb[ks % HW][1]) : "i"(cnt - mh[ks]) : "memory");
      const u32x4 a = {ra[G % R][0], ra[G % R][1], rb[G % R][0], rb[G % R][1]};
      acc[dt][0] = mfma16(a, hb[hi][0], ks == 0 ? z4 : acc[dt][0]);
      SB();
      if constexpr (F < 369) ra[F % R] = trd(fad(F));
      SB();
      acc[dt][1] = mfma16(a, hb[hi][1], ks == 0 ? z4 : acc[dt][1]);
      SB();
      if constexpr (F < 369) rb[F % R] = trd(fad(F) + 2304);
      if constexpr (G == 9 * (10 - HW) + 7 || G == 9 * (18 - HW) + 7) { BLD("dwordx4", pf, dws, 0, (ks + HW < 12 ? WS_F1 : WS_FH) + (lid >> 2) * 16, " sc1"); mpf = ++cnt; }
      if constexpr (dt == 8 && ks + HW < 36) {
        if (ks + HW == 12 || ks + HW == 20) {
          asm volatile("s_waitcnt vmcnt(%1)" : "+v"(pf) : "i"(cnt - mpf) : "memory");
          poll4(rw, dws, pf, 0, (ks + HW == 12 ? WS_F1 : WS_FH) + (lid >> 2) * 16, tag);
        }
        hload(ks + HW);
      }
      if constexpr (G >= 313 && G < 328 && (G - 313) % 3 < 2) {
        constexpr int sp = (G - 313) / 3, u = (G - 313) % 3;
        hb[4 + sp][u] = trd2(smem + zb + u * 256 + sp * 8192 - (sp == 4 ? (g >> 1) * 4096 : 0), 2048);
      }
      constexpr int t = G == 330 ? 0 : G == 333 ? 1 : G == 336 ? 2 : G == 341 ? 3 : G == 344 ? 4 : G == 351 ? 5 : G == 353 ? 6 : -1;
      if constexpr (t >= 0) {
        if (t == 6 && bb == 0) keep(6);
        else st16(rw, lane * 16, hso + t * 1024, pk4(acc[t][0], acc[t][1]));
      }
      SB();
    });
    keep(7); keep(8);
    WAITV(0);
    put_flag(rw, lane == 0, fio + bb * 4, tag);
    WAITL(0); BARX();
  } else {
    auto stage = [&](int j) AI {
      const unsigned so = w2off(j);
#pragma unroll
      for (int n = 0; n < 9; n++) w2stage(so, j, n);
      cnt += 9; mark[j] = cnt;
    };
    auto bar = [&](int j, int m) AI { WAITV(cnt - m); WAITL(0); if (j == 1) { } BARX(); };
    const u32x4 dg = mkdesc(&GT, sizeof(GT));
    f32x4 t1[4], t2[4];
    const f32x4 z4 = {0, 0, 0, 0};
    auto xpin = [&](int z) AI {
#pragma unroll
      for (int i = 0; i < 8; i++) PIN(xx[z][i]);
      PIN(wt[z][0]); PIN(wt[z][1]);
    };
    auto tpin = [&](u32x4 (&f)[2][3]) AI {
#pragma unroll
      for (int t = 0; t < 2; t++) { PIN(f[t][0]); PIN(f[t][1]); f[t][2] = f[t][1] ^ 0x80008000u; }
    };
    unsigned so;
    auto io = [&](int j, int n) AI {
      if (j < 0) return;
      SB();
      if (n == 0) so = w2off(j);
      w2stage(so, j, n); mark[j] = ++cnt;
      SB();
    };
#pragma unroll
    for (int i = 0; i < 8; i++) xx[0][i] = *(LAS u32x4*)(smem + SP + i * 1024 + lane * 16);
#pragma unroll
    for (int i = 0; i < 4; i++) tload(fb, 24576 + c * 6144, i);
    cnt += 4;
    const int mb = cnt;
    auto twid = [&](int w) AI {
      float f1 = (16 * c + l15) * (1.0f / 2048.0f), f2 = (16 * c + 4 * g) * (1.0f / 2048.0f);
      PIN(f1); PIN(f2);
#pragma unroll
      for (int at = 0; at < 2; at++)
#pragma unroll
        for (int i = 0; i < 4; i++) {
          const float a1 = (float)(16 * at + 4 * g + i) * f1, a2 = (float)(16 * at + l15) * (f2 + i * (1.0f / 2048.0f));
          if (w == 0) { t1[2 * at][i] = __builtin_amdgcn_cosf(a1); t1[2 * at + 1][i] = __builtin_amdgcn_sinf(a1); }
          else { t2[2 * at][i] = __builtin_amdgcn_cosf(a2) * (1.0f / 2048.0f); t2[2 * at + 1][i] = __builtin_amdgcn_sinf(a2) * (1.0f / 2048.0f); }
        }
#pragma unroll
      for (int i = 0; i < 4; i++) { if (w == 0) PIN(t1[i]); else PIN(t2[i]); }
    };
    twid(0);
#pragma unroll
    for (int i = 4; i < 8; i++) xx[0][i] &= lid == 0 ? 0u : ~0u;
    WAITV(cnt - mx);
    tpin(fa); tpin(f32); xpin(0);
    auto front = [&](int z, int j) AI {
      u32x4 br, bi, yr, yi;
#pragma unroll
      for (int at = 0; at < 2; at++) {
        f32x4 tr = z4, ti = z4;
#pragma unroll
        for (int t = 0; t < 2; t++) {
          const u32x4 xr = xx[z][2 * t + at], xi = xx[z][4 + 2 * t + at];
          tr = mfma16(xr, fa[t][0], tr); ti = mfma16(xi, fa[t][0], ti);
          tr = mfma16(xi, fa[t][1], tr); ti = mfma16(xr, fa[t][2], ti);
          io(j, 4 * at + 2 * t); io(j, 4 * at + 2 * t + 1);
        }
        const f32x4 co = t1[2 * at], si = t1[2 * at + 1];
        const f32x4 ur = tr * co + ti * si, ui = ti * co - tr * si;
        br[2 * at] = pk2(ur[0], ur[1]); br[2 * at + 1] = pk2(ur[2], ur[3]);
        bi[2 * at] = pk2(ui[0], ui[1]); bi[2 * at + 1] = pk2(ui[2], ui[3]);
      }
      if (j >= 0) { io(j, 8); PIN(br); PIN(bi); if (PRIO == 1) __builtin_amdgcn_s_setprio(0); bar(1, mst); j++; io(j, 0); twid(1); }
#pragma unroll
      for (int ct = 0; ct < 2; ct++) {
        f32x4 xr = mfma16(f32[ct][0], br, z4), xi = mfma16(f32[ct][0], bi, z4);
        xr = mfma16(f32[ct][1], bi, xr); xi = mfma16(f32[ct][2], br, xi);
        io(j, 2 * ct + 1); io(j, 2 * ct + 2);
        float vr[4], vi[4];
#pragma unroll
        for (int i = 0; i < 4; i++) { const unsigned w = wt[z][ct][i]; const float a = blo(w), b = bhi(w); vr[i] = xr[i] * a - xi[i] * b; vi[i] = xr[i] * b + xi[i] * a; }
        yr[2 * ct] = pk2(vr[0], vr[1]); yr[2 * ct + 1] = pk2(vr[2], vr[3]);
        yi[2 * ct] = pk2(vi[0], vi[1]); yi[2 * ct + 1] = pk2(vi[2], vi[3]);
      }
#pragma unroll
      for (int at = 0; at < 2; at++) {
        f32x4 pr = mfma16(yr, f32[at][0], z4), pi = mfma16(yi, f32[at][0], z4);
        pr = mfma16(yi, f32[at][2], pr); pi = mfma16(yr, f32[at][1], pi);
        io(j, 5 + 2 * at); io(j, 6 + 2 * at);
        const f32x4 co = t2[2 * at], si = t2[2 * at + 1];
        const f32x4 ur = pr * co - pi * si, ui = pi * co + pr * si;
        LAS char* p = smem + SP + (1 - z) * 8192 + (at * 4 + (c >> 1)) * 1024 + lane * 16 + (c & 1) * 8;
        *(LAS u32x2*)p = u32x2{pk2(ur[0], ur[1]), pk2(ur[2], ur[3])};
        *(LAS u32x2*)(p + 2048) = u32x2{pk2(ui[0], ui[1]), pk2(ui[2], ui[3])};
      }
    };
    front(0, 2);
    if (has2) { xpin(1); front(1, -1); }
    bar(2, mark[2]);
    tpin(fb);
    auto back = [&](int z, int j) AI {
      const int pair = kc[z] == 288 ? 0 : kc[z];
      const bool on = kc[z] == 0 ? (g & 1) == 0 : (kc[z] == 288 ? (g & 1) == 1 : true);
#pragma unroll
      for (int at = 0; at < 2; at++) {
        f32x4 zr = z4, zi = z4;
#pragma unroll
        for (int t = 0; t < 2; t++) {
          LAS char* p = smem + SP + (1 - z) * 8192 + (at * 4 + t) * 1024 + lane * 16;
          const u32x4 pr = *(LAS u32x4*)p, pi = *(LAS u32x4*)(p + 2048);
          io(j, 4 * at + 2 * t);
          zr = mfma16(fb[t][0], pr, zr); zi = mfma16(fb[t][0], pi, zi);
          zr = mfma16(fb[t][2], pi, zr); zi = mfma16(fb[t][1], pr, zi);
          io(j, 4 * at + 2 * t + 1);
        }
        const unsigned r0 = pk2(zr[0], zr[1]), r1 = pk2(zr[2], zr[3]);
        const unsigned i0 = kc[z] == 288 ? r0 : pk2(zi[0], zi[1]), i1 = kc[z] == 288 ? r1 : pk2(zi[2], zi[3]);
        const auto s0 = __builtin_amdgcn_permlane16_swap(r0, i0, false, false), s1 = __builtin_amdgcn_permlane16_swap(r1, i1, false, false);
        const unsigned a0 = s0[0], a1 = s1[0], a2 = s0[1], a3 = s1[1];
        st16(rw, on ? q4 * 1024u + g * 64 + ((p4 ^ (pair & 3)) * 16) : OOB, WS_Z + pair * 8192 + at * 4096 + c * 256, u32x4{a0, a1, a2, a3});
        if (z == 0) cnt++;
      }
    };
    if (has2) back(1, -1);
    back(0, 4);
    io(4, 8);
    bar(3, mark[3]);
    stage(5);
    bar(4, mark[4]);
    put_flag8(rw, tid - 256, WS_FZ + lid * 4, tag); cnt++;
    stage(6);
#pragma unroll
    for (int n = 0; n < 3; n++) { const int pc = n < 2 ? c + 4 * n : 8; GLDS(dg, lane * 16, (nt * 5 + 4) * 9216 + pc * 1024, 0, SP + pc * 1024, 0); }
    cnt += 3;
    bar(5, mark[5]);
    put_flag(rw, tid == 256, WS_F5 + lid * 4, tag); cnt++;
    stage(7);
    bar(6, mark[6]);
    const unsigned pvo = c == 0 ? lane * 16 : c == 1 ? 0u : OOB, pso = c == 0 ? WS_FZ + xcd * 1024 : WS_F5 + (lid >> 2) * 16;
#pragma unroll
    for (int n = 0; n < 9; n++) { io(8, n); if (n == 4) BLD("dwordx4", pf, dws, pvo, pso, " sc1"); }
#pragma unroll
    for (int n = 0; n < 3; n++) GLDS(dg, lane * 16, (nt * 5 + c) * 9216, 1024 * n, mlb, SLOT + 1024 * n);
    asm volatile("s_waitcnt vmcnt(7)" : "+v"(pf)::"memory");
    if (c < 2) poll4(rw, dws, pf, pvo, pso, tag);
    BARX();
#pragma unroll
    for (int n = 0; n < 9; n++) GLDSP(dws, hh * 8192 + r * 16, (72 * bb + 2 * c) * 8192 + rt * 512, WS_Z + 8 * n * 8192, c * 1024, 2 * SLOT + 4 * n * 1024, " sc1");
#pragma unroll
    for (int n = 3; n < 9; n++) GLDS(dg, lane * 16, (nt * 5 + c) * 9216, 1024 * n, mlb, SLOT + 1024 * n);
    put_flag(rw, lid == 0 && tid == 256, WS_E, tag);
    WAITV(16); BARX();
    const unsigned orow = (4 * aq + c + 32 * (32 * bh + l15)) * 1152u, ocol = 288 * q + 64 * bb, v0 = bb == 0 ? 0u : OOB;
    u32x2 xv[3][2], bv[3];
#pragma unroll
    for (int u = 0; u < 3; u++) {
      BLD("dwordx2", xv[u][0], dx, (orow + g * 8) | (u ? 0u : v0), ocol + 32 * u, "");
      BLD("dwordx2", xv[u][1], dx, (orow + g * 8) | (u ? 0u : v0), ocol + 32 * u + 589824, "");
      BLD("dwordx2", bv[u], mkdesc(b2, 1152), g * 8, ocol + 32 * u, "");
    }
    bar(9, cnt - 10);
    asm volatile("s_waitcnt vmcnt(0)" : "+v"(xv[0][0]), "+v"(xv[0][1]), "+v"(xv[1][0]), "+v"(xv[1][1]), "+v"(xv[2][0]), "+v"(xv[2][1]), "+v"(bv[0]), "+v"(bv[1]), "+v"(bv[2])::"memory");
    f32x4 own[3][2];
#pragma unroll
    for (int i = 0; i < 6; i++) {
      const u32x2 a = xv[i >> 1][i & 1], b = bv[i >> 1];
      own[i >> 1][i & 1] = f32x4{blo(a[0]) + blo(b[0]), bhi(a[0]) + bhi(b[0]), blo(a[1]) + blo(b[1]), bhi(a[1]) + bhi(b[1])};
      PIN(own[i >> 1][i & 1]);
    }
    __builtin_amdgcn_s_sleep(TSLEEP);
    poll4(rw, dws, u32x4{~tag, 0, 0, 0}, 0, fio, tag);
    u32x4 pp[3][3];
#pragma unroll
    for (int u = 2; u >= 0; u--)
#pragma unroll
      for (int s = 0; s < 3; s++) {
        const int b = (bb + 1 + s) & 3, d = bb - b;
        BLD("dwordx4", pp[s][u], dws, (lane * 16) | (u ? 0u : v0), hrow + 9 * (4 * b + q) * 2048 + ((d < 0 ? 2 * d + 6 : 2 * d - 3) + u) * 1024, " sc1");
      }
    BARX();
#pragma unroll
    for (int i = 0; i < 6; i++) { own[i >> 1][i & 1] += *(LAS f32x4*)(smem + 3 * SLOT + c * 6144 + i * 1024 + lane * 16); PIN(own[i >> 1][i & 1]); }
    asm volatile("s_waitcnt vmcnt(0)" : "+v"(pp[0][0]), "+v"(pp[0][1]), "+v"(pp[0][2]), "+v"(pp[1][0]), "+v"(pp[1][1]), "+v"(pp[1][2]), "+v"(pp[2][0]), "+v"(pp[2][1]), "+v"(pp[2][2])::"memory");
    const rsrc_t ro = mkrsrc(out, 2048 * 1152);
    auto fin = [&](int u, int h) AI {
      float v[4];
#pragma unroll
      for (int e = 0; e < 4; e++) {
        const unsigned w0 = pp[0][u][2 * h + (e >> 1)], w1 = pp[1][u][2 * h + (e >> 1)], w2_ = pp[2][u][2 * h + (e >> 1)];
        v[e] = (e & 1) ? (own[u][h][e] + bhi(w0)) + (bhi(w1) + bhi(w2_)) : (own[u][h][e] + blo(w0)) + (blo(w1) + blo(w2_));
      }
      return u32x2{pk2(v[0], v[1]), pk2(v[2], v[3])};
    };
#pragma unroll
    for (int h = 0; h < 2; h++) {
      const u32x2 o1 = fin(1, h), o2 = fin(2, h);
      const auto s0 = __builtin_amdgcn_permlane16_swap(o1[0], o2[0], false, false), s1 = __builtin_amdgcn_permlane16_swap(o1[1], o2[1], false, false);
      const unsigned a0 = s0[0], a1 = s1[0], a2 = s0[1], a3 = s1[1];
      st16x<OAUX>(ro, orow + (g & 1) * 32 + (g >> 1) * 16, ocol + 32 + h * 589824, u32x4{a0, a1, a2, a3});
    }
    if (bb == 0) {
      const u32x2 o1 = fin(0, 0), o2 = fin(0, 1);
      const auto s0 = __builtin_amdgcn_permlane16_swap(o1[0], o2[0], false, false), s1 = __builtin_amdgcn_permlane16_swap(o1[1], o2[1], false, false);
      const unsigned a0 = s0[0], a1 = s1[0], a2 = s0[1], a3 = s1[1];
      st16x<OAUX>(ro, orow + (g & 1) * 589824 + (g >> 1) * 16, ocol, u32x4{a0, a1, a2, a3});
    }
    if (STAMPW) { WAITV(0); }
  }
  asm volatile(".Lkend%0:" ::"i"(decltype(SETC)::v));
  };
  if (wv < 4) body(IC<0>{}); else if ((blockIdx.x & 3) * 4 + ((blockIdx.x >> 3) & 3) < 9) body(IC<1>{}); else body(IC<3>{});
}

extern "C" void kernel_launch(void* const* d_in, const int* in_sizes, int n_in, void* d_out, int out_size, void* d_ws, size_t ws_size, hipStream_t stream) {
  (void)hipFuncSetAttribute((const void*)kmain, hipFuncAttributeMaxDynamicSharedMemorySize, LDSZ);
  kmain<<<256, 512, LDSZ, stream>>>((const u16*)d_in[0], (const u16*)d_in[1], (const u16*)d_in[2], (const u16*)d_in[4], (const u16*)d_in[5],
                                    (const u16*)d_in[6], (const u16*)d_in[7], (u16*)d_out, (char*)d_ws);
}
```

```cpp
#include <hip/hip_runtime.h>
#include <hip/hip_bf16.h>
#include <utility>

typedef unsigned short u16;
typedef __attribute__((ext_vector_type(8))) __bf16 bf16x8;
typedef __attribute__((ext_vector_type(2))) __bf16 bf16x2;
typedef __attribute__((ext_vector_type(4))) short s16x4;
typedef __attribute__((ext_vector_type(8))) short s16x8;
typedef __attribute__((ext_vector_type(4))) float f32x4;
typedef __attribute__((ext_vector_type(16))) float f32x16;
typedef __attribute__((ext_vector_type(4))) unsigned u32x4;
typedef __attribute__((ext_vector_type(2))) unsigned u32x2;
#define LAS __attribute__((address_space(3)))
#define GAS __attribute__((address_space(1)))
#define DI __device__ __forceinline__

namespace tab {
constexpr double PI_ = 3.14159265358979323846;
constexpr double tsin(double x) { double t = x, s = x; for (int i = 1; i < 12; i++) { t *= -x * x / ((2 * i) * (2 * i + 1)); s += t; } return s; }
constexpr double tcos(double x) { double t = 1, s = 1; for (int i = 1; i < 12; i++) { t *= -x * x / ((2 * i - 1) * (2 * i)); s += t; } return s; }
constexpr double ccos(int n, int N) {
  n %= N; if (n < 0) n += N;
  if (2 * n > N) n = N - n;
  bool neg = false;
  if (4 * n > N) { n = N / 2 - n; neg = true; }
  double v = (8 * n > N) ? tsin(2 * PI_ * (N / 4 - n) / N) : tcos(2 * PI_ * n / N);
  return neg ? -v : v;
}
constexpr double csin(int n, int N) { return ccos(n - N / 4, N); }
constexpr u16 f2bf(double d) { float f = (float)d; unsigned u = __builtin_bit_cast(unsigned, f); u += 0x7fffu + ((u >> 16) & 1u); return (u16)(u >> 16); }
struct C576 { u16 c[576]; u16 g1[576]; u16 g2[576]; };
constexpr C576 mk576() { C576 r{}; for (int i = 0; i < 576; i++) { double c = ccos(i, 576); r.c[i] = f2bf(c); r.g1[i] = f2bf(c / 576.0); r.g2[i] = f2bf(2.0 * c / 576.0); } return r; }
constexpr C576 c576 = mk576();
struct Blk { u16 v[9216]; };
constexpr int fidx(int bc, int d) {
  int n = bc < 144 ? bc * d : (bc == 144 ? ((d & 1) == 0 ? 144 * d : 144 * d + 144) : (bc - 144) * d + 144);
  return n % 576;
}
constexpr Blk mkF(int idx) {
  Blk r{}; int at = idx >> 1, hf = idx & 1;
  for (int st = 0; st < 18; st++) for (int hh = 0; hh < 2; hh++) for (int j = 0; j < 8; j++) {
    int d = 288 * hf + 32 * (st >> 1) + 16 * (j >> 2) + 8 * hh + 2 * (j & 3) + (st & 1);
    for (int cc = 0; cc < 32; cc++) r.v[((st * 2 + hh) * 8 + j) * 32 + cc] = c576.c[fidx(32 * at + cc, d)];
  }
  return r;
}
template <int I> constexpr Blk fblk = mkF(I);
struct FTab { Blk b[18]; };
template <int... I> constexpr FTab mkFT(std::integer_sequence<int, I...>) { return FTab{{fblk<I>...}}; }
constexpr double bf2d(u16 b) { return (double)__builtin_bit_cast(float, (unsigned)b << 16); }
struct FCB { unsigned v[64]; };
constexpr FCB mkFC(int at) {
  FCB r{};
  for (int e = 0; e < 2; e++) for (int cc = 0; cc < 32; cc++) {
    double s = 0;
    for (int d = e; d < 576; d += 2) s += bf2d(c576.c[fidx(32 * at + cc, d)]);
    u16 hi = f2bf(s), lo = f2bf(s - bf2d(hi));
    r.v[cc * 2 + e] = (unsigned)hi | ((unsigned)lo << 16);
  }
  return r;
}
template <int I> constexpr FCB fcb = mkFC(I);
struct FCT { FCB b[9]; };
template <int... I> constexpr FCT mkFCT(std::integer_sequence<int, I...>) { return FCT{{fcb<I>...}}; }
struct GBlk { u16 v[4608]; };
constexpr GBlk mkG(int idx) {
  GBlk r{}; int step = idx % 5, q = (idx / 5) & 3, b = idx / 20;
  for (int row = 0; row < 32; row++) {
    int rho = 32 * step + row;
    if (rho >= 144) continue;
    int kc = 72 * b + (rho >> 1), ri = rho & 1;
    for (int c = 0; c < 144; c++) {
      int d = 144 * q + (c + 16 * (2 * b + 3)) % 144;
      r.v[row * 144 + c] = kc == 0 ? c576.g1[(ri * 288 * d) % 576] : c576.g2[(kc * d + ri * 144) % 576];
    }
  }
  return r;
}
template <int I> constexpr GBlk gblk = mkG(I);
struct GTab { GBlk b[80]; };
template <int... I> constexpr GTab mkGT(std::integer_sequence<int, I...>) { return GTab{{gblk<I>...}}; }
struct C2048 { float c[2048]; };
constexpr C2048 mk2048() { C2048 r{}; for (int i = 0; i < 2048; i++) r.c[i] = (float)ccos(i, 2048); return r; }
constexpr C2048 c2048 = mk2048();
constexpr u16 trig(int n2048, int type) {
  int n = type == 0 ? n2048 : (type == 1 ? n2048 + 1536 : n2048 + 512);
  return f2bf(c2048.c[n & 2047]);
}
struct F64T { u16 v[4][2][3][64][8]; };
constexpr F64T mkF64(int which) {
  F64T r{};
  for (int j = 0; j < 4; j++) for (int t = 0; t < 2; t++) for (int ty = 0; ty < 3; ty++) for (int l = 0; l < 64; l++) for (int jj = 0; jj < 8; jj++) {
    int g = l >> 4, o = 16 * j + (l & 15);
    int k = which == 0 ? 32 * t + 16 * (g >> 1) + 8 * (jj >> 2) + 4 * (g & 1) + (jj & 3) : 32 * t + 16 * (jj >> 2) + 4 * g + (jj & 3);
    r.v[j][t][ty][l][jj] = trig(o * k * 32, ty);
  }
  return r;
}
struct F32T { u16 v[2][3][64][8]; };
constexpr F32T mkF32() {
  F32T r{};
  for (int ct = 0; ct < 2; ct++) for (int ty = 0; ty < 3; ty++) for (int l = 0; l < 64; l++) for (int jj = 0; jj < 8; jj++) {
    int g = l >> 4, o = 16 * ct + (l & 15), k = 16 * (jj >> 2) + 4 * g + (jj & 3);
    r.v[ct][ty][l][jj] = trig(o * k * 64, ty);
  }
  return r;
}
struct TWT { float v[4][4][64][4]; };
constexpr TWT mkTW(int which) {
  TWT r{};
  for (int j = 0; j < 4; j++) for (int n = 0; n < 4; n++) for (int l = 0; l < 64; l++) for (int i = 0; i < 4; i++) {
    int g = l >> 4, at = n >> 1;
    int a = which == 0 ? 16 * at + 4 * g + i : 16 * at + (l & 15);
    int d = which == 0 ? 16 * j + (l & 15) : 16 * j + 4 * g + i;
    float v = c2048.c[(a * d + ((n & 1) ? 1536 : 0)) & 2047];
    r.v[j][n][l][i] = which == 0 ? v : v * (1.0f / 2048.0f);
  }
  return r;
}
constexpr F64T f64a = mkF64(0), f64b = mkF64(1);
constexpr F32T f32t = mkF32();
constexpr TWT tw1 = mkTW(0), tw2 = mkTW(1);
struct Small { F64T a, b; F32T f; TWT t1, t2; FCT fc; };
}
#if defined(__HIP_DEVICE_COMPILE__)
__device__ __attribute__((aligned(128))) static const tab::FTab FT = tab::mkFT(std::make_integer_sequence<int, 18>{});
__device__ __attribute__((aligned(128))) static const tab::GTab GT = tab::mkGT(std::make_integer_sequence<int, 80>{});
__device__ __attribute__((aligned(128))) static const tab::Small ST = {tab::f64a, tab::f64b, tab::f32t, tab::tw1, tab::tw2, tab::mkFCT(std::make_integer_sequence<int, 9>{})};
#else
__device__ static const tab::FTab FT = {};
__device__ static const tab::GTab GT = {};
__device__ static const tab::Small ST = {};
#endif
typedef unsigned long long u64;
DI float bfr(float x) { return (float)(__bf16)x; }
DI unsigned pk2(float a, float b) { bf16x2 c; c[0] = (__bf16)a; c[1] = (__bf16)b; return __builtin_bit_cast(unsigned, c); }
DI float blo(unsigned u) { return __builtin_bit_cast(float, u << 16); }
DI float bhi(unsigned u) { return __builtin_bit_cast(float, u & 0xffff0000u); }
DI f32x16 mfma32(u32x4 a, u32x4 b, f32x16 c) { return __builtin_amdgcn_mfma_f32_32x32x16_bf16(__builtin_bit_cast(bf16x8, a), __builtin_bit_cast(bf16x8, b), c, 0, 0, 0); }
DI f32x4 mfma16(u32x4 a, u32x4 b, f32x4 c) { return __builtin_amdgcn_mfma_f32_16x16x32_bf16(__builtin_bit_cast(bf16x8, a), __builtin_bit_cast(bf16x8, b), c, 0, 0, 0); }
DI u32x2 trd(LAS char* p) { return __builtin_bit_cast(u32x2, __builtin_amdgcn_ds_read_tr16_b64_v4i16((LAS s16x4*)p)); }
DI u32x4 trd2(LAS char* p, int off2) { u32x2 a = trd(p), b = trd(p + off2); u32x4 r; r[0] = a[0]; r[1] = a[1]; r[2] = b[0]; r[3] = b[1]; return r; }
template <int C> DI float dppadd(float v) { return v + __builtin_bit_cast(float, __builtin_amdgcn_mov_dpp(__builtin_bit_cast(int, v), C, 0xf, 0xf, true)); }
DI u32x4 mkdesc(const void* p, unsigned bytes) { u64 a = (u64)p; return u32x4{(unsigned)a, (unsigned)(a >> 32) & 0xffffu, bytes, 0x00020000u}; }
typedef __amdgpu_buffer_rsrc_t rsrc_t;
DI rsrc_t mkrsrc(const void* p, unsigned bytes) { return __builtin_amdgcn_make_buffer_rsrc((void*)p, 0, bytes, 0x00020000); }
DI void st16(rsrc_t r, unsigned voff, unsigned soff, u32x4 v) {
  __builtin_amdgcn_raw_buffer_store_b128(v, r, voff, soff, 16);
  asm volatile("s_nop 1" ::"v"(v) : "memory");
}
template <int AUX> DI void st16x(rsrc_t r, unsigned voff, unsigned soff, u32x4 v) {
  __builtin_amdgcn_raw_buffer_store_b128(v, r, voff, soff, AUX);
  asm volatile("s_nop 1" ::"v"(v) : "memory");
}
DI u32x4 ld16(rsrc_t r, unsigned voff, unsigned soff) { return __builtin_amdgcn_raw_buffer_load_b128(r, voff, soff, 16); }
#define GLDS(d, voff, sbase, simm, lbase, limm) GLDSP(d, voff, sbase, simm, lbase, limm, "")
#define GLDSP(d, voff, sbase, simm, lbase, limm, pol) do { unsigned t_; \
  asm volatile("s_add_i32 m0, %1, %2\n\ts_add_i32 %0, %3, %4\n\tbuffer_load_dwordx4 %5, %6, %0 offen" pol " lds" \
               : "=&s"(t_) : "s"(lbase), "i"(limm), "s"(sbase), "i"(simm), "v"(voff), "s"(d) : "memory", "scc"); } while (0)
#define GLDS1(d, voff, soff, lb) asm volatile("s_mov_b32 m0, %0\n\tbuffer_load_dword %1, %2, %3 offen lds" ::"i"(lb), "v"(voff), "s"(d), "s"(soff) : "memory")
#define WAITV(n) asm volatile("s_waitcnt vmcnt(%0)" ::"i"(n) : "memory")
#define WAITL(n) asm volatile("s_waitcnt lgkmcnt(%0)" ::"i"(n) : "memory")
#define PIN(x) asm volatile("" : "+v"(x))
#define BARX() asm volatile("s_barrier" ::: "memory")
#define SB() __builtin_amdgcn_sched_barrier(0)
#define AI __attribute__((always_inline))
#if defined(__HIP_DEVICE_COMPILE__)
#define NOPK __attribute__((target("no-packed-fp32-ops")))
#else
#define NOPK
#endif
template <int N> struct IC { static constexpr int v = N; };
template <class F, int... I> DI void sfor_(F&& f, std::integer_sequence<int, I...>) { (f(IC<I>{}), ...); }
template <int N, class F> DI void sfor(F&& f) { sfor_(f, std::make_integer_sequence<int, N>{}); }

#define BLD(op, dst, d, voff, soff, pol) asm volatile("buffer_load_" op " %0, %1, %2, %3 offen" pol : "=v"(dst) : "v"(voff), "s"(d), "s"(soff) : "memory")
DI float asf(unsigned u) { return __builtin_bit_cast(float, u); }
DI unsigned asu(float f) { return __builtin_bit_cast(unsigned, f); }
DI float swadd32(float a, float b) { auto s = __builtin_amdgcn_permlane32_swap(asu(a), asu(b), false, false); const unsigned x = s[0], y = s[1]; return asf(x) + asf(y); }
DI float swadd16(float a, float b) { auto s = __builtin_amdgcn_permlane16_swap(asu(a), asu(b), false, false); const unsigned x = s[0], y = s[1]; return asf(x) + asf(y); }
DI float half_sum(float v) {
  v = dppadd<0xB1>(v); v = dppadd<0x4E>(v); v = dppadd<0x141>(v); v = dppadd<0x140>(v);
  return swadd16(v, v);
}

constexpr int SLOT = 36864, SP = 4 * SLOT, LDSZ = 163840;
constexpr int SP_T = SP + 8192, SP_CS = SP_T + 4608, SP_RS = SP_T + 6656, SP_PF = SP_T + 7168;
constexpr unsigned WS_E = 0, WS_F1 = 1024, WS_FH = 2048, WS_FA = 3072, WS_F5 = 4096, WS_ERR = 5120, WS_ST = 8192, WS_F0 = 24576, WS_FZ = 32768;
constexpr unsigned WS_CS = 40960;
constexpr unsigned WS_H = WS_CS + 3 * 36864;
constexpr unsigned WS_XS = WS_H + 64u * 144 * 2048;
constexpr unsigned WS_Z = WS_XS + 576u * 4096;
constexpr unsigned WS_WT = WS_Z + 576u * 4096;
#define SELFV 1
#define STAMPW 0
constexpr unsigned WS_END = WS_WT + 289u * 8192;
constexpr int SPIN = 200000;
constexpr unsigned OOB = 0x80000000u;
DI void put_flag(rsrc_t rw, bool on, unsigned off, unsigned tag) { __builtin_amdgcn_raw_buffer_store_b32(tag, rw, on ? 0u : OOB, off, 16); }
DI void put_flag8(rsrc_t rw, unsigned t, unsigned off, unsigned tag) { __builtin_amdgcn_raw_buffer_store_b32(tag, rw, t < 8 ? t * 1024 : OOB, off, 16); }
DI bool bad4(u32x4 v, unsigned tag) { return __builtin_amdgcn_ballot_w64(v[0] != tag || v[1] != tag || v[2] != tag || v[3] != tag) != 0; }
#define PSL 2
DI void poll4(rsrc_t rw, u32x4 dws, u32x4 v, unsigned voff, unsigned soff, unsigned tag) {
  for (int it = 0; bad4(v, tag); it++) {
    if (it >= SPIN) { put_flag(rw, true, WS_ERR, 1); break; }
    __builtin_amdgcn_s_sleep(PSL);
    BLD("dwordx4", v, dws, voff, soff, " sc1");
    asm volatile("s_waitcnt vmcnt(0)" : "+v"(v)::"memory");
  }
}
#define DIST1 4
#define RING1 5
#define DIST2 4
#define RING2 5
#define ABL 0
#define PREF 3
#define PREFC 1
#define PREF2 103
#define PFA 26
#define PFB 42
#define PFC 43
#define HAUX 16
#define WNT ""
#define OAUX 16
#define PRIO 2
#define OTOUCH 1
#define TSLEEP 32
#define TWS 26
#define PFW 27
#define TGA 41
#define TGC 56
#define PRI1 0
#define TX0 150
#define UBAR 0
#define TSL2 4
#define B3L 0
#define FSTR 9216
#define WND 1
#define FTCH 0
#define PF0 1
__global__ __launch_bounds__(512, 2) NOPK void kmain(const u16* __restrict__ x, const u16* __restrict__ wr, const u16* __restrict__ wi,
                                                     const u16* __restrict__ w1, const u16* __restrict__ b1, const u16* __restrict__ w2,
                                                     const u16* __restrict__ b2, u16* __restrict__ out, char* __restrict__ ws) {
  extern __shared__ __attribute__((aligned(16))) char smem_[];
  LAS char* smem = (LAS char*)smem_;
  const int tid = threadIdx.x, lane = tid & 63;
  const int wv = __builtin_amdgcn_readfirstlane(tid >> 6);
  if (wv >= 4) {
    u64 pc; int l0;
    asm volatile("s_getpc_b64 %0\n.Lkpc:\n\ts_mov_b32 %1, .Lkend0-.Lkpc\n\ts_max_i32 %1, %1, .Lkend1-.Lkpc\n\ts_max_i32 %1, %1, .Lkend3-.Lkpc" : "=s"(pc), "=s"(l0)::"scc");
    GLDS1(mkdesc((const void*)pc, (unsigned)l0 < 0x40000u ? l0 : 0), lane * 128u + ((wv & 3) * 32 + (blockIdx.x >> 3)) * 8192u, 0, SP_PF);
  }
  {
    int l_ = lane, i_ = blockIdx.x, w_ = wv;
    asm volatile("" : "+v"(l_), "+s"(i_), "+s"(w_));
    const int rt_ = ((i_ & 7) >> 2) * 8 + (i_ >> 5);
    const unsigned vo = (4 * (rt_ >> 1) + (w_ >> 1) + 32 * (32 * (rt_ & 1) + 16 * (w_ & 1) + (l_ >> 3))) * 1152u + (((l_ & 7) ^ (l_ >> 4)) * 16);
    const unsigned vo1 = (vo ^ 64) + 8 * 32 * 1152, lb = 2 * SLOT + w_ * 10240;
    const u32x4 d_ = mkdesc(x, 2048 * 1152);
#pragma unroll
    for (int n = 0; n < 5; n++) { GLDS(d_, vo, 0, 128 * n, lb, 1024 * n); GLDS(d_, vo1, 0, 128 * n, lb, 5120 + 1024 * n); }
  }
  auto body = [&](auto SETC) AI {
  constexpr int SET = decltype(SETC)::v & 1;
  constexpr bool ND = decltype(SETC)::v == 3;
  const int id = blockIdx.x, xcd = id & 7, slot = id >> 3;
  const int rt = (xcd >> 2) * 8 + (slot >> 2), bb = xcd & 3, q = slot & 3, nt = bb * 4 + q, lid = rt * 16 + nt;
  const int aq = rt >> 1, bh = rt & 1, c = wv & 3;
  const int fl0 = (nt & 1) * 32, flh = (nt & 1) ? 0 : 256;
  const bool has_dft = nt < 9;
  const rsrc_t rw = mkrsrc(ws, WS_END);
  const u32x4 dws = mkdesc(ws, WS_END), dx = mkdesc(x, 2048 * 1152), dw1 = mkdesc(w1, 576 * 9216), dw2 = mkdesc(w2, 4608 * 1152);
  const int r = lane & 31, hh = lane >> 5;
  unsigned ev, xc;
  u32x4 xs[2];
  asm volatile("s_load_dword %0, %1, 0x0" : "=s"(ev) : "s"(ws) : "memory");
  if (SET == 0) {
    const int tr = 8 * nt + 2 * c;
    const unsigned ro = (4 * aq + (tr >> 5) + 32 * (32 * bh + (tr & 31))) * 1152u;
    BLD("dwordx4", xs[0], dx, hh * 36864 + r * 16, ro, "");
    BLD("dwordx4", xs[1], dx, hh * 36864 + r * 16, ro, " offset:512");
    BLD("dword", xc, dx, hh * 36864 + r * 4, ro, " offset:1024");
  }
  const unsigned xvo = (4 * aq + (wv >> 1) + 32 * (32 * bh + 16 * (wv & 1) + (lane >> 3))) * 1152u + (((lane & 7) ^ (lane >> 4)) * 16);
  const unsigned xvo1 = (xvo ^ 64) + 8 * 32 * 1152;
  const unsigned xlb = 2 * SLOT + wv * 10240;
  const unsigned wvo = (8 * c + ((lane >> 2) & 7)) * 9216u + hh * 64 + (lane & 3) * 16;
  const unsigned wso = (288 * nt + fl0) * 2, wlb = c * 1024;
  const u32x4 dft = mkdesc(&FT, has_dft ? sizeof(FT) : 0);
  const u32x4 dm = c < 2 ? dw1 : dft;
  const unsigned mvo = c < 2 ? (144 * c + (lane >> 2)) * 9216u + (lane & 3) * 16 : lane * 16;
  const unsigned mso = c < 2 ? (288 * nt + flh) * 2 : nt * 36864 + (c & 1) * 9216;
  const unsigned mstep = c < 2 ? 16 * 9216 : 1024, mhf = c < 2 ? 288 * 9216 : 18432, mlb = c * 9216;
#pragma unroll
  for (int n = SET; n < 9; n += 2) GLDS(dm, mvo, mso + n * mstep, 0, mlb, 1024 * n);
  const int cu = (xcd >> 2) * 32 + slot, cg = 18 * bb + cu / 3, g3 = cu % 3;
  u32x4 uu[3];
  if (UBAR) BARX();
#pragma unroll
  for (int n = 0; n < 3; n++) BLD("dwordx4", uu[n], dw1, (cu < 54 && !(ABL & 64)) ? (lane >> 3) * 9216u + (lane & 7) * 16 : OOB, (192 * g3 + 24 * wv + 8 * n) * 9216 + cg * 128, "");
  if (SET == 1 && PF0) {
    const int r7 = rt & 7;
    GLDS1(c < 3 ? dft : r7 ? mkdesc(&ST.fc, 2304) : mkdesc(b1, 9216), (lane < (c < 3 ? 48 : 6) && r7 <= (c == 3)) ? lane * 128u : OOB,
          c < 3 ? nt * 36864 + 18432 + c * 6144 : r7 ? nt * 256 : nt * 576, SP_PF);
  }
  asm volatile("s_waitcnt lgkmcnt(0)" : "+s"(ev)::"memory");
  const unsigned tag = ev * 0x9E3779B1u + 0x7F4A7C15u;
  if (SET == 0) {
    asm volatile("s_waitcnt vmcnt(%3)" : "+v"(xs[0]), "+v"(xs[1]), "+v"(xc) : "i"(8) : "memory");
    float s = blo(xc) + bhi(xc);
#pragma unroll
    for (int i = 0; i < 8; i++) { const unsigned w = xs[i >> 2][i & 3]; s += blo(w) + bhi(w); }
    const float mean = half_sum(s) * (1.0f / 576.0f);
    float v = 0.f;
#pragma unroll
    for (int i = 0; i < 9; i++) { const unsigned w = i < 8 ? xs[(i >> 2) & 1][i & 3] : xc; const float d0 = blo(w) - mean, d1 = bhi(w) - mean; v = __builtin_fmaf(d0, d0, v); v = __builtin_fmaf(d1, d1, v); }
    const float var = half_sum(v) * (1.0f / 576.0f);
    __builtin_amdgcn_raw_buffer_store_b64(u32x2{pk2(mean, __builtin_sqrtf(bfr(bfr(var) + 1e-5f))), tag}, rw, r == 0 ? hh * 8u : OOB, WS_ST + (rt * 128 + 8 * nt + 2 * c) * 8, 16);
  }
  const int q4 = (lane & 15) >> 2, p4 = lane & 3, g = lane >> 4, l15 = lane & 15;
  const unsigned hoff = (r >> 4) * 1024 + hh * 256 + (r & 15) * 16;
  const unsigned hrow = WS_H + (4 * rt + c) * 144 * 2048;
  const unsigned hso = hrow + 9 * nt * 2048;
  unsigned w2o[9];
  constexpr auto ftile = [](int ks) constexpr {
    const int qq = ks < 12 ? ks / 3 : ks < 20 ? (ks - 12) / 2 : (ks - 20) / 4, e = ks < 12 ? ks % 3 : ks < 20 ? 3 + (ks & 1) : 5 + (ks & 3);
    return 9 * qq + ((qq & 1) ? e : (e == 0 ? 8 : e - 1));
  };
  auto w2off = [&](int j) AI {
    const unsigned t = ftile(4 * j) | ftile(4 * j + 1) << 8 | ftile(4 * j + 2) << 16 | ftile(4 * j + 3) << 24;
    return (36 * bb + ((t >> (8 * c)) & 255)) * 36864 + q * 288;
  };
  auto w2stage = [&](unsigned so, int j, int n) AI { GLDS(dw2, w2o[n], so, 0, mlb, (j & 3) * SLOT + 1024 * n); };
  int cnt = 0, mark[12] = {};
  constexpr int HW = 8;
  u32x4 hb[HW + 1][2];
  int mh[36];
  auto hload = [&](int ks) AI {
    const unsigned so = hrow + (36 * bb + ftile(ks)) * 2048;
    BLD("dwordx4", hb[ks % HW][0], dws, lane * 16, so, " sc1");
    BLD("dwordx4", hb[ks % HW][1], dws, lane * 16, so, " offset:1024 sc1");
    cnt += 2; mh[ks] = cnt;
  };
  u32x4 pf;
  int mpf, mst, mx;
  const unsigned fio = WS_XS + ((rt * 4 + q) * 4 + c) * FSTR;
  const u32x4 dst = mkdesc(&ST, sizeof(ST));
  const int e2 = q < 2 ? rt * 2 + q : 32;
  const bool has2 = ND && bb == 3 && (q < 2 || lid == 14);
  const int kc[2] = {lid, 256 + e2};
  u32x4 fa[2][3], fb[2][3], f32[2][3], xx[2][8], wt[2][2];
  auto xload = [&](int z, int i) AI {
    const bool cplx = kc[z] != 0 && kc[z] != 288;
    const unsigned vo = q4 * 256 + g * 64 + p4 * 16;
    if (i < 8) BLD("dwordx4", xx[z][i], dws, (i < 4 || cplx) ? vo : OOB, WS_XS + (kc[z] == 288 ? 1 : 2 * kc[z]) * 4096 + i * 1024, " sc1");
    else BLD("dwordx4", wt[z][i - 8], dws, lane * 16, WS_WT + kc[z] * 8192 + (c * 2 + i - 8) * 1024, " sc1");
  };
  auto tload = [&](u32x4 (&f)[2][3], unsigned so, int i) AI { BLD("dwordx4", f[i >> 1][i & 1], dst, lane * 16, so + (i >> 1) * 3072 + (i & 1) * 1024, ""); };

  {
    constexpr int D = DIST1, R = RING1;
    const unsigned tq = q4 * 64 + (g & 1) * 32 + p4 * 8;
    LAS char* const tbP = smem + hh * 1024 + tq + SET * 512;
    LAS char* const tbM = smem + hh * 512 + tq + SET * 18432;
    const unsigned xa = 2 * SLOT + (4 * c + (r >> 3)) * 5120 + (r & 7) * 128 + ((hh ^ ((r >> 1) & 7)) * 16);
    u32x4 xf[36], ring[R], pm[2], sf, tf, xq[4];
    u32x2 sg, fc, wl[8];
    u32x4 cq[3];
    unsigned bia, w2s, wvv;
    f32x16 acc[6];
    f32x4 cr[2];
    float cs[8], gt[3][8], ge[3][8], k0, k1, sdf;
    u32x4 go[3];
    LAS char* bs[11];
    int mw, mt[5];
    const f32x16 zero = {0, 0, 0, 0, 0, 0, 0, 0, 0, 0, 0, 0, 0, 0, 0, 0};
    constexpr auto isM = [](int k) constexpr { return k == 0 || k == 2; };
    constexpr auto tile = [](int k) constexpr { return k < 4 ? (k & 1) : (k >> 1); };
    constexpr auto half = [](int k) constexpr { return k < 4 ? (k >> 1) : (k & 1); };
    constexpr auto slotof = [](int k) constexpr { return k < 2 ? k : (k + 2) & 3; };
    auto fetch = [&](int F) AI { const int k = F / 18, st = F % 18; if (!(ND && isM(k))) ring[F % R] = trd2(bs[k] + st * (isM(k) ? 1024 : 2048), 256); };
    auto xread = [&](int i) AI { if (i < 36) xf[i] = *(LAS u32x4*)(smem + (xa ^ ((i & 3) * 32)) + ((i >> 2) % 5) * 1024); };
    auto perm = [&](int T) AI {
      const int st = T % 18, b = 18 * half(T / 18) + (st & ~1); const unsigned sel = (st & 1) ? 0x07060302u : 0x05040100u;
      u32x4 o;
      o[0] = __builtin_amdgcn_perm(xf[b][1], xf[b][0], sel); o[1] = __builtin_amdgcn_perm(xf[b][3], xf[b][2], sel);
      o[2] = __builtin_amdgcn_perm(xf[b + 1][1], xf[b + 1][0], sel); o[3] = __builtin_amdgcn_perm(xf[b + 1][3], xf[b + 1][2], sel);
      PIN(o); pm[st & 1] = o;
    };
    auto gstep = [&](int t, int u, int s, int z) AI {
      float* T_ = gt[z]; float* E_ = ge[z]; const int i = s >> 1;
      auto v = [&](int n) AI { return acc[t][8 * u + n]; };
      if (s == 0) { T_[0] = v(0) * v(0); PIN(T_[0]); }
      if ((s & 1) == 0) {
        if (i < 8) { T_[i] = v(i) * __builtin_fmaf(T_[i], k1, k0); PIN(T_[i]); }
        if (i >= 1) { E_[i - 1] = __builtin_amdgcn_rcpf(__builtin_fmaf(E_[i - 1], sdf, sdf)); PIN(E_[i - 1]); }
      } else {
        if (i < 8) { E_[i] = __builtin_amdgcn_exp2f(T_[i]); PIN(E_[i]); }
        if (i >= 1) { T_[i - 1] = v(i - 1) * E_[i - 1]; if ((i & 1) == 0) { unsigned w_ = pk2(T_[i - 2], T_[i - 1]); PIN(w_); go[z][(i >> 1) - 1] = w_; } else PIN(T_[i - 1]); }
        if (i + 1 < 8) { T_[i + 1] = v(i + 1) * v(i + 1); PIN(T_[i + 1]); }
      }
      if (s == 17 && (ABL & 2)) { PIN(go[z]); mt[t] = cnt; } else if (s == 17) { st16x<HAUX>(rw, hoff, hso + (t == 0 ? flh : fl0 + 64 * (t - 1) + 32 * SET) * 64 + u * 512, go[z]); mt[t] = ++cnt; }
    };
    const int gl = 36 * c + (lane < 35 ? lane : 35);
    auto gather = [&]() AI {
      BLD("dwordx2", sg, dws, r * 8, WS_ST + (rt * 128 + c * 32) * 8, " sc1");
#pragma unroll
      for (int i = 0; i < 3 * (1 - SET); i++) BLD("dwordx4", cq[i], dws, gl * 16, WS_CS + i * 36864 + nt * 2304, " sc1");
    };
    WAITV(4 - SET + SET * PF0); BARX();
    bs[0] = tbM; PIN(bs[0]);
    xread(0); xread(1);
#pragma unroll
    for (int F = 0; F < D; F++) fetch(F);
    xread(2); xread(3);
    if (SET == 1 && !ND) perm(0);
    SB();
    sfor<180>([&](auto TC_) AI {
      constexpr int T = decltype(TC_)::v, k = T / 18, st = T % 18, F = T + D, t = tile(k);
      if constexpr (T == 5) { WAITL(ND ? 0 : 2); BARX(); }
      if constexpr (T == 9) { }
      if constexpr (T == 14) { }
      if constexpr (T == 19) { }
      if constexpr (T == 32) { }
      if constexpr (T == 26) { }
      if constexpr (T == 26) { }
      if constexpr (T == 32) { }
      if constexpr (T == 33) { }
      if constexpr (T == 41) { }
      if constexpr (T == 43) { }
      if constexpr (T == 44) { }
      if constexpr (T == 48) { }
      if constexpr (T == 49) { }
      if constexpr (T == 50) { }
      if constexpr (T == 51) { }
      if constexpr (T == 55) { }
      if constexpr (T == 61) { }
      if constexpr (T == 65) { }
      if constexpr (T == 66) { }
      if constexpr (T == 68) { }
      if constexpr (T == 158) { }
      if constexpr (T == 159) { }
      if constexpr (T == 162) { }
      if constexpr (T == 166) { }
      if constexpr (T == 170) { }
      if constexpr (T == 171) { }
      if constexpr (T == 174) { }
      if constexpr (T == 176) { }
      if constexpr (T == 177) { }
      if constexpr (T == 65) { if (SET == 1) WAITL(4); BARX(); }
      if constexpr (F % 18 == 0) {
        constexpr int kb = F / 18;
        if (kb <= 5 || (kb & 1) == SET) WAITV(cnt - mark[kb]);
        else if ((kb == 6 && !ND) || kb == 10) WAITV(cnt - mx);
        else if (kb == 7) WAITV(cnt - mw);
        else if (kb == 8) WAITV(cnt - mt[1]);
        if (kb <= 2 || kb == 10) WAITL(0); else if (kb == 3) WAITL(B3L); else if (kb == 4) WAITL(4);
        if (kb == 1) { } if (kb == 2) { } BARX();
        if (kb == (TWS > 50 ? 7 : 6) && SET == 0) { put_flag8(rw, tid, WS_F0 + lid * 4, tag); cnt++; }
        if ((kb == 8 || kb == 10) && SET == 0) { put_flag(rw, tid == 0, (kb == 8 ? WS_FA : WS_F1) + lid * 4, tag); cnt++; }
        if constexpr (kb < 10) { bs[kb] = (isM(kb) ? tbM : tbP) + slotof(kb) * SLOT; PIN(bs[kb]); }
      }
      if constexpr (F < 180) fetch(F);
      if constexpr (ND && isM(k)) {
      } else if constexpr (SET == 1 && isM(k)) {
        const int a = (st & 1) ? 5 : 0;
        acc[a] = mfma32(pm[st & 1], ring[T % R], (k == 0 && st < 2) ? zero : acc[a]);
        if (st < 17) perm(T + 1);
      } else {
        acc[t] = mfma32(ring[T % R], xf[18 * half(k) + st], (half(k) == 0 && st == 0) ? zero : acc[t]);
      }
      if (SET == 1 && !ND && T == 35) perm(36);
      if constexpr (T < 4) { xread(4 + 4 * T); xread(5 + 4 * T); xread(6 + 4 * T); xread(7 + 4 * T); }
      if constexpr (T >= 32 && T < 38) { xread(20 + 3 * (T - 32)); xread(21 + 3 * (T - 32)); xread(22 + 3 * (T - 32)); }
      constexpr int xp = T == 5 ? 0 : T == 7 ? 1 : T == 8 ? 2 : T == 10 ? 3 : T == 11 ? 4 : T == 13 ? 5 : T == 24 ? 6 : T == 25 ? 7 : -1;
      if constexpr (xp >= 0) { GLDS(dx, (xp & 1) ? xvo1 : xvo, 0, 640 + 128 * (xp >> 1), xlb, (xp & 1) * 5120 + 1024 * (xp >> 1)); mark[2] = ++cnt; }
      if constexpr (T < 5 && 2 * T + 1 - SET < 9) { GLDS(dw1, wvo, wso, (2 * T + 1 - SET) * 32 * 9216, wlb, SLOT + 4096 * (2 * T + 1 - SET)); mark[1] = ++cnt; }
      if constexpr (F >= 18 && F < 180) {
        constexpr int kb = F / 18, fs = F % 18;
        constexpr int ks = kb <= 2 ? (fs < 9 ? kb + 1 : -1) : kb == 3 ? (fs < 9 ? 4 : 5) : (fs < 9 ? kb + 2 : -1);
        constexpr int o = (SET + ks) & 1;
        constexpr int n = ks <= 5 ? ((fs % 9) % 2 == o ? fs % 9 : 9) : (o == 0 ? fs : 9);
        if constexpr (ks >= 2 && n < 9 && !((ABL & 1) && ks >= 5 && ks < 10)) {
          if constexpr (ks == 2) GLDS(dm, mvo, mso + mhf + n * mstep, 0, mlb, 1024 * n);
          else if constexpr (ks <= 9) GLDS(dw1, wvo, wso, half(ks) * 288 * 9216 + (tile(ks) - 1) * 128 + n * 32 * 9216, wlb, slotof(ks) * SLOT + 4096 * n);
          else {
            const int e = 64 * n + lane; w2o[n] = (e / 18) * 1152 + ((e + 4 * bb + 6) % 18) * 16;
            if (n == 0) w2s = w2off(ks - 10);
            w2stage(w2s, ks - 10, n);
          }
          mark[ks] = ++cnt;
        }
      }
      if constexpr (T == 10) asm volatile("s_waitcnt vmcnt(%3)" : "+v"(uu[0]), "+v"(uu[1]), "+v"(uu[2]) : "i"(cnt + 1 - SET + SET * PF0) : "memory");
      if constexpr (T == 10 || T == 11) {
#pragma unroll
        for (int i = 2 * (T - 10); i < 2 * (T - 10) + 2; i++) {
          cs[2 * i] = blo(uu[0][i]) + blo(uu[1][i]) + blo(uu[2][i]); cs[2 * i + 1] = bhi(uu[0][i]) + bhi(uu[1][i]) + bhi(uu[2][i]);
          PIN(cs[2 * i]); PIN(cs[2 * i + 1]);
        }
      }
      if constexpr (T == 12) {
#pragma unroll
        for (int i = 0; i < 4; i++) { cs[i] = swadd32(cs[i], cs[i + 4]); PIN(cs[i]); }
      }
      if constexpr (T == 13) {
        const float a = dppadd<0x128>(swadd16(cs[0], cs[2])), b = dppadd<0x128>(swadd16(cs[1], cs[3]));
        *(LAS float*)(smem + SP_CS + (lane & 7) * 256 + (lane >> 3) * 32 + wv * 4) = (lane & 8) ? b : a;
      }
      if constexpr (SET == 0 && T == 14) { cr[0] = *(LAS f32x4*)(smem + SP_CS + lane * 32); cr[1] = *(LAS f32x4*)(smem + SP_CS + lane * 32 + 16); }
      if constexpr (SET == 0 && T == 16) {
        const float s = ((cr[0][0] + cr[0][1]) + (cr[0][2] + cr[0][3])) + ((cr[1][0] + cr[1][1]) + (cr[1][2] + cr[1][3]));
        __builtin_amdgcn_raw_buffer_store_b64(u32x2{pk2(s, s - bfr(s)), tag}, rw, (c == 0 && cu < 54) ? lane * 8u : OOB, WS_CS + g3 * 36864 + cg * 512, 16);
        cnt++;
      }
      if constexpr (T == (SET == 0 ? TGA : 41)) {
        gather();
        if (SET == 0) BLD("dword", bia, mkdesc(b1, 9216), gl * 4, 576 * nt, "");
        else BLD("dwordx2", fc, mkdesc(&ST.fc, 2304), r * 8, (ND ? 0 : nt) * 256, "");
        cnt += SET == 0 ? 5 : 2; mpf = cnt;
      }
      if constexpr (T == (SET == 0 ? 49 : 48)) {
        auto gwait = [&]() AI {
          if (SET == 0) asm volatile("s_waitcnt vmcnt(%5)" : "+v"(sg), "+v"(cq[0]), "+v"(cq[1]), "+v"(cq[2]), "+v"(bia) : "i"(cnt - mpf) : "memory");
          else asm volatile("s_waitcnt vmcnt(%2)" : "+v"(sg), "+v"(fc) : "i"(cnt - mpf) : "memory");
          unsigned m = sg[1] ^ tag;
          if (SET == 0) m |= (cq[0][1] ^ tag) | (cq[0][3] ^ tag) | (cq[1][1] ^ tag) | (cq[1][3] ^ tag) | (cq[2][1] ^ tag) | (cq[2][3] ^ tag);
          return __builtin_amdgcn_ballot_w64(m != 0) != 0;
        };
        for (int it = 0; gwait(); it++) {
          if (it >= SPIN) { put_flag(rw, true, WS_ERR, 2); break; }
          __builtin_amdgcn_s_sleep(4);
          gather();
          WAITV(0);
        }
        if (SET == 0) {
          *(LAS u32x4*)(smem + SP_T + gl * 32) = u32x4{cq[0][0], cq[1][0], cq[2][0], bia & 0xffffu};
          *(LAS u32x4*)(smem + SP_T + gl * 32 + 16) = u32x4{cq[0][2], cq[1][2], cq[2][2], bia >> 16};
        }
      }
      if constexpr (T == 51) {
        const unsigned nm = hh ? 0u : ((sg[0] & 0xffffu) ^ 0x8000u) * 0x10001u;
        sf = u32x4{nm, nm, nm, hh ? 0u : sg[0] >> 16};
        sdf = bhi(sg[0]);
        PIN(sf); PIN(sdf);
      }
      if constexpr (SET == 1 && !ND && T == 52) *(LAS float*)(smem + SP_RS + c * 128 + r * 4) = __builtin_amdgcn_rcpf(sdf);
      if constexpr (T == (SET == 0 ? 52 : 70)) {
        const float rstd = __builtin_amdgcn_rcpf(sdf), c0 = -2.0f * 0.7978845608028654f * 1.4426950408889634f, c1 = c0 * 0.044715f;
        k0 = c0 * rstd; k1 = c1 * rstd * rstd * rstd;
        PIN(k0); PIN(k1);
      }
      if (SET == 0 && T == 51) tf = *(LAS u32x4*)(smem + SP_T + (flh + r) * 16);
      if (SET == 0 && T == 53) acc[0] = mfma32(tf, sf, acc[0]);
      if constexpr (!isM(k)) {
        if (half(k) == 1 && st == (k == 9 ? 13 : 15)) tf = *(LAS u32x4*)(smem + SP_T + (fl0 + 64 * (t - 1) + 32 * SET + r) * 16);
        if (half(k) == 1 && st == 17) acc[t] = mfma32(tf, sf, acc[t]);
      } else if constexpr (SET == 1 && !ND && k == 2 && st == 17) {
        acc[0] = mfma32(sf, u32x4{fc[0], 0, 0, 0}, acc[0]);
        acc[5] = mfma32(sf, u32x4{fc[1], 0, 0, 0}, acc[5]);
      }
      constexpr int TW = TWS;
      if constexpr (!(ABL & 16) && SET == 0 && ((T >= TW && T < TW + 4) || (T >= TW + 17 && T < TW + 21))) {
        const int wset = 8 * (lid >> 2) + (lane & 7), wp = 5 * c + (lane >> 3), wcol = 72 * q + (q == 3) + 4 * wp;
        const bool on = lane < 40 && wp < 19 && wcol <= 285;
        if constexpr (T < TW + 8) {
          constexpr int i = T - TW;
          if (i == 0) { wvv = ((16 * (wset >> 7) + (wset & 15) + 256 * ((wset >> 4) & 7)) * 289 + wcol) * 2u; PIN(wvv); wvv = on ? wvv : OOB; }
          BLD("dwordx2", wl[2 * i], mkdesc(wr, 2048 * 578), wvv, i * 64 * 578, WNT);
          BLD("dwordx2", wl[2 * i + 1], mkdesc(wi, 2048 * 578), wvv, i * 64 * 578, WNT);
          cnt += 2; mw = cnt;
        } else {
          constexpr int j = T - TW - 17;
          if (j == 0) {
            asm volatile("s_waitcnt vmcnt(%8)" : "+v"(wl[0]), "+v"(wl[1]), "+v"(wl[2]), "+v"(wl[3]), "+v"(wl[4]), "+v"(wl[5]), "+v"(wl[6]), "+v"(wl[7]) : "i"(cnt - mw) : "memory");
            wvv = wcol * 8192u + wset * 16; PIN(wvv); wvv = on ? wvv : OOB;
          }
          u32x4 o;
#pragma unroll
          for (int i = 0; i < 4; i++) o[i] = __builtin_amdgcn_perm(wl[2 * i + 1][j >> 1], wl[2 * i][j >> 1], (j & 1) ? 0x07060302u : 0x05040100u);
          st16(rw, wvv, WS_WT + j * 8192, o);
          mw = ++cnt;
        }
      }
      if constexpr (SET == 0) {
        if constexpr (T == 161) { BLD("dwordx4", pf, dws, 0, WS_FA + (lid >> 2) * 16, " sc1"); mpf = ++cnt; }
        if constexpr (T == 168) {
          asm volatile("s_waitcnt vmcnt(%1)" : "+v"(pf) : "i"(cnt - mpf) : "memory");
          poll4(rw, dws, pf, 0, WS_FA + (lid >> 2) * 16, tag);
        }
        if constexpr (T >= 168 && (T - 168) % 3 == 0) hload((T - 168) / 3);
      }
      if constexpr (SET == 1) {
        if constexpr (T == TX0 - 9) { BLD("dwordx4", pf, dws, lane * 16, WS_F0 + xcd * 1024, " sc1"); mpf = ++cnt; }
        if constexpr (T == TX0) {
          asm volatile("s_waitcnt vmcnt(%1)" : "+v"(pf) : "i"(cnt - mpf) : "memory");
          poll4(rw, dws, pf, lane * 16, WS_F0 + xcd * 1024, tag);
        }
        if constexpr (T == TX0 || T == TX0 + 1) { GLDSP(dws, q4 * 256 + g * 64 + p4 * 16, kc[0] * 8192 + c * 2048, WS_XS + (T - TX0) * 1024, c * 2048, SP + (T - TX0) * 1024, " sc1"); mx = ++cnt; }
        if constexpr (T >= 168 && T < 176) { tload(T < 172 ? fa : f32, T < 172 ? c * 6144 : 49152, T & 3); cnt++; }
      }
      if constexpr (PREF == 1 && (SET == 0 ? (T == 77 || T == 113) : T == 107)) {
        const int u = wv * 32 + (rt & 7) * 4 + q;
        const u32x4 dp = u < 162 ? dw2 : u < 185 ? mkdesc(&GT, sizeof(GT)) : dst;
        GLDS1(dp, (c >> 1) == (T == 113) ? lane * 128u : OOB, u < 162 ? bb * 1327104 + u * 8192 : u < 185 ? bb * 184320 + (u - 162) * 8192 : (u - 185) * 8192, SP_PF);
        cnt++;
      }
      if constexpr (PREF == 4 && ND && (T == PFA || T == PFB)) {
        const int u = ((T == PFB) * 4 + c) * 112 + rt * 7 + nt - 9;
        const u32x4 dp = u < 7 ? dst : u < 655 ? dw2 : u < 745 ? mkdesc(&GT, sizeof(GT)) : mkdesc(b2, 1152);
        GLDS1(dp, lane * 128u, (u < 7 ? u : u < 655 ? u - 7 : u < 745 ? u - 655 : u - 745) * 8192, SP_PF);
        cnt++;
      }
      if constexpr (PREF == 3 && SET == 1 && (T == PFA || T == PFB || T == PFC)) {
        const int u = c * 32 + slot, xh = xcd >> 2;
        const u32x4 dp = u < 1 ? dst : u < 82 ? dw2 : u < 95 ? mkdesc(&GT, sizeof(GT)) : mkdesc(b2, 1152);
        GLDS1(dp, c == (T == PFA ? 0 : T == PFB ? 1 : 2) ? lane * 128u : OOB, u < 1 ? xcd * 8192 : u < 82 ? bb * 1327104 + (xh * 81 + u - 1) * 8192 : u < 95 ? bb * 184320 + (xh * 10 + u - 82) * 8192 : 0, SP_PF);
        cnt++;
      }
      if constexpr (FTCH && SET == 1 && T == FTCH) {
        asm volatile("s_mov_b32 m0, %0\n\tbuffer_load_dword %1, %2, %3 offen sc1 lds" ::"i"(SP_PF), "v"((c == 3 && id < 3) ? lane * 128u : OOB), "s"(dws), "s"(id ? 16384 + id * 8192 : 0) : "memory");
        cnt++;
      }
      if constexpr (SET == 0) {
        if constexpr (T >= 55 && T < 73) gstep(0, 0, T - 55, 2);
        if constexpr (T >= 73 && T < 109 && ((T - 73) & 1) == 0) gstep(0, 1, (T - 73) >> 1, 2);
      } else if constexpr (ND) {
      } else if constexpr (T >= 55 && T < 61) {
        constexpr int u = (T - 55) / 3, ph = (T - 55) % 3;
        const int bc = 32 * nt + r;
        const float b1c = bc == 144 ? 0.f : 1.f, b2c = bc < 144 ? -1.f : 1.f, a2c = -b1c * b2c;
        if (ph == 0) { cr[0] = *(LAS f32x4*)(smem + SP_RS + c * 128 + u * 64 + hh * 16); cr[1] = *(LAS f32x4*)(smem + SP_RS + c * 128 + u * 64 + 32 + hh * 16); }
        else {
          float o1[4], o2[4];
#pragma unroll
          for (int i = 0; i < 4; i++) {
            const float e = acc[0][8 * u + 4 * (ph - 1) + i] * cr[ph - 1][i], o = acc[5][8 * u + 4 * (ph - 1) + i] * cr[ph - 1][i];
            o1[i] = __builtin_fmaf(b1c, o, e); o2[i] = __builtin_fmaf(a2c, e, b2c * o);
          }
          LAS char* p = smem + r * 272 + (2 * u + hh) * 64 + c * 16 + (ph - 1) * 8;
          *(LAS u32x2*)p = u32x2{pk2(o1[0], o1[1]), pk2(o1[2], o1[3])};
          *(LAS u32x2*)(p + 32 * 272) = u32x2{pk2(o2[0], o2[1]), pk2(o2[2], o2[3])};
        }
      } else if constexpr (T == 65) {
#pragma unroll
        for (int i = 0; i < 4; i++) xq[i] = *(LAS u32x4*)(smem + (16 * c + 4 * i + g) * 272 + l15 * 16);
      } else if constexpr (T >= 67 && T < 71) {
        const int bc = 32 * nt + 16 * (c & 1) + 4 * (T - 67) + g;
        const int p1 = 2 * bc - (bc > 144 ? 287 : 0);
        int p2 = (bc < 144 ? 576 : 865) - 2 * bc; p2 = bc == 0 ? 1 : p2; p2 = bc == 144 ? 289 : p2;
        unsigned vo = (c < 2 ? p1 : p2) * 4096u + l15 * 16; PIN(vo);
        st16(rw, vo, WS_XS + bh * 2048 + aq * 256, xq[T - 67]);
        mx = ++cnt;
      }
      if constexpr (T >= 73 && (ABL & 4)) { if constexpr ((T - 73) % 36 == 0) PIN(acc[(T - 73) / 36 + 1]); } else if constexpr (T >= 73) {
        const int pt = (T - 73) / 36 + 1, s = (T - 73) % 36;
        if (s < 18) gstep(pt, 0, s, 0);
        if (s >= 16 && s < 34) gstep(pt, 1, s - 16, 1);
      }
      SB();
    });
    if (SET == 1 && PRIO) __builtin_amdgcn_s_setprio(1);
#pragma unroll
    for (int s = 0; s < 18; s++) {
      if (s % 4 == 1 && s < 16 && SET == 0) hload(4 + s / 4);
      if (s >= 10 && s < 12 && SET == 1) { xload(0, s - 2); mx = ++cnt; }
      if ((s == 0 || s == 5) && SET == 1 && has2) {
#pragma unroll
        for (int i = 0; i < 5; i++) xload(1, s + i);
      }
      gstep(4, 0, s, 0); gstep(4, 1, s, 1);
    }
    mst = cnt;
  }
  if (SET == 1 && PRIO == 2) __builtin_amdgcn_s_setprio(0);
  if constexpr (SET == 0) {
    constexpr int D = DIST2, R = RING2;
    const unsigned tb = (16 * (g >> 1) + 4 * (g & 1) + q4) * 288 + p4 * 8;
    const unsigned zb = 2 * SLOT + (8 * (g >> 1) + 2 * (g & 1) + (q4 >> 1)) * 512 + (2 * (p4 >> 1) + (q4 & 1)) * 64 + ((c ^ (2 * (g & 1) + (q4 >> 1))) * 16) + (p4 & 1) * 8;
    u32x2 ra[R], rb[R];
    f32x4 acc[9][2];
    LAS char* bs[11];
    const f32x4 z4 = {0, 0, 0, 0};
    constexpr auto ksof = [](int G) constexpr { return G < 288 ? G / 9 : G < 316 ? 32 + (G - 288) / 7 : G < 331 ? 36 + (G - 316) / 3 : G < 351 ? 36 + (G - 331) % 10 / 2 : 32 + (G - 351) / 2; };
    constexpr auto dtof = [](int G) constexpr { return G < 288 ? G % 9 : G < 316 ? (G - 288) % 7 : G < 331 ? (G - 316) % 3 : (G < 341 ? 3 : G < 351 ? 5 : 7) + (G - 331) % 2; };
    auto fad = [&](int F) AI { return bs[ksof(F) / 4] + (ksof(F) % 4) * 9216 + dtof(F) * 32; };
    auto pk4 = [&](f32x4 a, f32x4 b) AI { return u32x4{pk2(a[0], a[1]), pk2(a[2], a[3]), pk2(b[0], b[1]), pk2(b[2], b[3])}; };
    auto keep = [&](int t) AI {
      *(LAS f32x4*)(smem + 3 * SLOT + c * 6144 + (t - 6) * 2048 + lane * 16) = acc[t][0];
      *(LAS f32x4*)(smem + 3 * SLOT + c * 6144 + (t - 6) * 2048 + 1024 + lane * 16) = acc[t][1];
    };
    bs[0] = smem + tb; PIN(bs[0]);
#pragma unroll
    for (int F = 0; F < D; F++) { ra[F] = trd(fad(F)); rb[F] = trd(fad(F) + 2304); }
    SB();
    sfor<369>([&](auto GC_) AI {
      constexpr int G = decltype(GC_)::v, ks = ksof(G), dt = dtof(G), F = G + D, hi = ks < 36 ? ks % HW : ks - 32;
      if constexpr (F < 351 && ksof(F) % 4 == 0 && dtof(F) == 0) {
        constexpr int j = ksof(F) / 4;
        if (j == 1) { WAITV(cnt - mst); }
        if (j == 6 || j == 7) WAITL(0);
        if (j < 10) { BARX(); }
        if (j == 1) { put_flag(rw, tid == 0, WS_FH + lid * 4, tag); cnt++; }
        bs[j] = smem + (j < 10 ? (j & 3) * SLOT : SP) + tb; PIN(bs[j]);
      }
      if constexpr (dt == 0 && ks < 36) asm volatile("s_waitcnt vmcnt(%2)" : "+v"(hb[ks % HW][0]), "+v"(hb[ks % HW][1]) : "i"(cnt - mh[ks]) : "memory");
      const u32x4 a = {ra[G % R][0], ra[G % R][1], rb[G % R][0], rb[G % R][1]};
      acc[dt][0] = mfma16(a, hb[hi][0], ks == 0 ? z4 : acc[dt][0]);
      SB();
      if constexpr (F < 369) ra[F % R] = trd(fad(F));
      SB();
      acc[dt][1] = mfma16(a, hb[hi][1], ks == 0 ? z4 : acc[dt][1]);
      SB();
      if constexpr (F < 369) rb[F % R] = trd(fad(F) + 2304);
      if constexpr (G == 9 * (10 - HW) + 7 || G == 9 * (18 - HW) + 7) { BLD("dwordx4", pf, dws, 0, (ks + HW < 12 ? WS_F1 : WS_FH) + (lid >> 2) * 16, " sc1"); mpf = ++cnt; }
      if constexpr (dt == 8 && ks + HW < 36) {
        if (ks + HW == 12 || ks + HW == 20) {
          asm volatile("s_waitcnt vmcnt(%1)" : "+v"(pf) : "i"(cnt - mpf) : "memory");
          poll4(rw, dws, pf, 0, (ks + HW == 12 ? WS_F1 : WS_FH) + (lid >> 2) * 16, tag);
        }
        hload(ks + HW);
      }
      if constexpr (G >= 313 && G < 328 && (G - 313) % 3 < 2) {
        constexpr int sp = (G - 313) / 3, u = (G - 313) % 3;
        hb[4 + sp][u] = trd2(smem + zb + u * 256 + sp * 8192 - (sp == 4 ? (g >> 1) * 4096 : 0), 2048);
      }
      constexpr int t = G == 330 ? 0 : G == 333 ? 1 : G == 336 ? 2 : G == 341 ? 3 : G == 344 ? 4 : G == 351 ? 5 : G == 353 ? 6 : -1;
      if constexpr (t >= 0) {
        if (t == 6 && bb == 0) keep(6);
        else st16(rw, lane * 16, hso + t * 1024, pk4(acc[t][0], acc[t][1]));
      }
      SB();
    });
    keep(7); keep(8);
    WAITV(0);
    put_flag(rw, lane == 0, fio + bb * 4, tag);
    WAITL(0); BARX();
  } else {
    auto stage = [&](int j) AI {
      const unsigned so = w2off(j);
#pragma unroll
      for (int n = 0; n < 9; n++) w2stage(so, j, n);
      cnt += 9; mark[j] = cnt;
    };
    auto bar = [&](int j, int m) AI { WAITV(cnt - m); WAITL(0); if (j == 1) { } BARX(); };
    const u32x4 dg = mkdesc(&GT, sizeof(GT));
    f32x4 t1[4], t2[4];
    const f32x4 z4 = {0, 0, 0, 0};
    auto xpin = [&](int z) AI {
#pragma unroll
      for (int i = 0; i < 8; i++) PIN(xx[z][i]);
      PIN(wt[z][0]); PIN(wt[z][1]);
    };
    auto tpin = [&](u32x4 (&f)[2][3]) AI {
#pragma unroll
      for (int t = 0; t < 2; t++) { PIN(f[t][0]); PIN(f[t][1]); f[t][2] = f[t][1] ^ 0x80008000u; }
    };
    unsigned so;
    auto io = [&](int j, int n) AI {
      if (j < 0) return;
      SB();
      if (n == 0) so = w2off(j);
      w2stage(so, j, n); mark[j] = ++cnt;
      SB();
    };
#pragma unroll
    for (int i = 0; i < 8; i++) xx[0][i] = *(LAS u32x4*)(smem + SP + i * 1024 + lane * 16);
#pragma unroll
    for (int i = 0; i < 4; i++) tload(fb, 24576 + c * 6144, i);
    cnt += 4;
    const int mb = cnt;
    auto twid = [&](int w) AI {
      float f1 = (16 * c + l15) * (1.0f / 2048.0f), f2 = (16 * c + 4 * g) * (1.0f / 2048.0f);
      PIN(f1); PIN(f2);
#pragma unroll
      for (int at = 0; at < 2; at++)
#pragma unroll
        for (int i = 0; i < 4; i++) {
          const float a1 = (float)(16 * at + 4 * g + i) * f1, a2 = (float)(16 * at + l15) * (f2 + i * (1.0f / 2048.0f));
          if (w == 0) { t1[2 * at][i] = __builtin_amdgcn_cosf(a1); t1[2 * at + 1][i] = __builtin_amdgcn_sinf(a1); }
          else { t2[2 * at][i] = __builtin_amdgcn_cosf(a2) * (1.0f / 2048.0f); t2[2 * at + 1][i] = __builtin_amdgcn_sinf(a2) * (1.0f / 2048.0f); }
        }
#pragma unroll
      for (int i = 0; i < 4; i++) { if (w == 0) PIN(t1[i]); else PIN(t2[i]); }
    };
    twid(0);
#pragma unroll
    for (int i = 4; i < 8; i++) xx[0][i] &= lid == 0 ? 0u : ~0u;
    WAITV(cnt - mx);
    tpin(fa); tpin(f32); xpin(0);
    auto front = [&](int z, int j) AI {
      u32x4 br, bi, yr, yi;
#pragma unroll
      for (int at = 0; at < 2; at++) {
        f32x4 tr = z4, ti = z4;
#pragma unroll
        for (int t = 0; t < 2; t++) {
          const u32x4 xr = xx[z][2 * t + at], xi = xx[z][4 + 2 * t + at];
          tr = mfma16(xr, fa[t][0], tr); ti = mfma16(xi, fa[t][0], ti);
          tr = mfma16(xi, fa[t][1], tr); ti = mfma16(xr, fa[t][2], ti);
          io(j, 4 * at + 2 * t); io(j, 4 * at + 2 * t + 1);
        }
        const f32x4 co = t1[2 * at], si = t1[2 * at + 1];
        const f32x4 ur = tr * co + ti * si, ui = ti * co - tr * si;
        br[2 * at] = pk2(ur[0], ur[1]); br[2 * at + 1] = pk2(ur[2], ur[3]);
        bi[2 * at] = pk2(ui[0], ui[1]); bi[2 * at + 1] = pk2(ui[2], ui[3]);
      }
      if (j >= 0) { io(j, 8); PIN(br); PIN(bi); if (PRIO == 1) __builtin_amdgcn_s_setprio(0); bar(1, mst); j++; io(j, 0); twid(1); }
#pragma unroll
      for (int ct = 0; ct < 2; ct++) {
        f32x4 xr = mfma16(f32[ct][0], br, z4), xi = mfma16(f32[ct][0], bi, z4);
        xr = mfma16(f32[ct][1], bi, xr); xi = mfma16(f32[ct][2], br, xi);
        io(j, 2 * ct + 1); io(j, 2 * ct + 2);
        float vr[4], vi[4];
#pragma unroll
        for (int i = 0; i < 4; i++) { const unsigned w = wt[z][ct][i]; const float a = blo(w), b = bhi(w); vr[i] = xr[i] * a - xi[i] * b; vi[i] = xr[i] * b + xi[i] * a; }
        yr[2 * ct] = pk2(vr[0], vr[1]); yr[2 * ct + 1] = pk2(vr[2], vr[3]);
        yi[2 * ct] = pk2(vi[0], vi[1]); yi[2 * ct + 1] = pk2(vi[2], vi[3]);
      }
#pragma unroll
      for (int at = 0; at < 2; at++) {
        f32x4 pr = mfma16(yr, f32[at][0], z4), pi = mfma16(yi, f32[at][0], z4);
        pr = mfma16(yi, f32[at][2], pr); pi = mfma16(yr, f32[at][1], pi);
        io(j, 5 + 2 * at); io(j, 6 + 2 * at);
        const f32x4 co = t2[2 * at], si = t2[2 * at + 1];
        const f32x4 ur = pr * co - pi * si, ui = pi * co + pr * si;
        LAS char* p = smem + SP + (1 - z) * 8192 + (at * 4 + (c >> 1)) * 1024 + lane * 16 + (c & 1) * 8;
        *(LAS u32x2*)p = u32x2{pk2(ur[0], ur[1]), pk2(ur[2], ur[3])};
        *(LAS u32x2*)(p + 2048) = u32x2{pk2(ui[0], ui[1]), pk2(ui[2], ui[3])};
      }
    };
    front(0, 2);
    if (has2) { xpin(1); front(1, -1); }
    bar(2, mark[2]);
    tpin(fb);
    auto back = [&](int z, int j) AI {
      const int pair = kc[z] == 288 ? 0 : kc[z];
      const bool on = kc[z] == 0 ? (g & 1) == 0 : (kc[z] == 288 ? (g & 1) == 1 : true);
#pragma unroll
      for (int at = 0; at < 2; at++) {
        f32x4 zr = z4, zi = z4;
#pragma unroll
        for (int t = 0; t < 2; t++) {
          LAS char* p = smem + SP + (1 - z) * 8192 + (at * 4 + t) * 1024 + lane * 16;
          const u32x4 pr = *(LAS u32x4*)p, pi = *(LAS u32x4*)(p + 2048);
          io(j, 4 * at + 2 * t);
          zr = mfma16(fb[t][0], pr, zr); zi = mfma16(fb[t][0], pi, zi);
          zr = mfma16(fb[t][2], pi, zr); zi = mfma16(fb[t][1], pr, zi);
          io(j, 4 * at + 2 * t + 1);
        }
        const unsigned r0 = pk2(zr[0], zr[1]), r1 = pk2(zr[2], zr[3]);
        const unsigned i0 = kc[z] == 288 ? r0 : pk2(zi[0], zi[1]), i1 = kc[z] == 288 ? r1 : pk2(zi[2], zi[3]);
        const auto s0 = __builtin_amdgcn_permlane16_swap(r0, i0, false, false), s1 = __builtin_amdgcn_permlane16_swap(r1, i1, false, false);
        const unsigned a0 = s0[0], a1 = s1[0], a2 = s0[1], a3 = s1[1];
        st16(rw, on ? q4 * 1024u + g * 64 + ((p4 ^ (pair & 3)) * 16) : OOB, WS_Z + pair * 8192 + at * 4096 + c * 256, u32x4{a0, a1, a2, a3});
        if (z == 0) cnt++;
      }
    };
    if (has2) back(1, -1);
    back(0, 4);
    io(4, 8);
    bar(3, mark[3]);
    stage(5);
    bar(4, mark[4]);
    put_flag8(rw, tid - 256, WS_FZ + lid * 4, tag); cnt++;
    stage(6);
#pragma unroll
    for (int n = 0; n < 3; n++) { const int pc = n < 2 ? c + 4 * n : 8; GLDS(dg, lane * 16, (nt * 5 + 4) * 9216 + pc * 1024, 0, SP + pc * 1024, 0); }
    cnt += 3;
    bar(5, mark[5]);
    put_flag(rw, tid == 256, WS_F5 + lid * 4, tag); cnt++;
    stage(7);
    bar(6, mark[6]);
    const unsigned pvo = c == 0 ? lane * 16 : c == 1 ? 0u : OOB, pso = c == 0 ? WS_FZ + xcd * 1024 : WS_F5 + (lid >> 2) * 16;
#pragma unroll
    for (int n = 0; n < 9; n++) { io(8, n); if (n == 4) BLD("dwordx4", pf, dws, pvo, pso, " sc1"); }
#pragma unroll
    for (int n = 0; n < 3; n++) GLDS(dg, lane * 16, (nt * 5 + c) * 9216, 1024 * n, mlb, SLOT + 1024 * n);
    asm volatile("s_waitcnt vmcnt(7)" : "+v"(pf)::"memory");
    if (c < 2) poll4(rw, dws, pf, pvo, pso, tag);
    BARX();
#pragma unroll
    for (int n = 0; n < 9; n++) GLDSP(dws, hh * 8192 + r * 16, (72 * bb + 2 * c) * 8192 + rt * 512, WS_Z + 8 * n * 8192, c * 1024, 2 * SLOT + 4 * n * 1024, " sc1");
#pragma unroll
    for (int n = 3; n < 9; n++) GLDS(dg, lane * 16, (nt * 5 + c) * 9216, 1024 * n, mlb, SLOT + 1024 * n);
    put_flag(rw, lid == 0 && tid == 256, WS_E, tag);
    WAITV(16); BARX();
    const unsigned orow = (4 * aq + c + 32 * (32 * bh + l15)) * 1152u, ocol = 288 * q + 64 * bb, v0 = bb == 0 ? 0u : OOB;
    u32x2 xv[3][2], bv[3];
#pragma unroll
    for (int u = 0; u < 3; u++) {
      BLD("dwordx2", xv[u][0], dx, (orow + g * 8) | (u ? 0u : v0), ocol + 32 * u, "");
      BLD("dwordx2", xv[u][1], dx, (orow + g * 8) | (u ? 0u : v0), ocol + 32 * u + 589824, "");
      BLD("dwordx2", bv[u], mkdesc(b2, 1152), g * 8, ocol + 32 * u, "");
    }
    bar(9, cnt - 10);
    asm volatile("s_waitcnt vmcnt(0)" : "+v"(xv[0][0]), "+v"(xv[0][1]), "+v"(xv[1][0]), "+v"(xv[1][1]), "+v"(xv[2][0]), "+v"(xv[2][1]), "+v"(bv[0]), "+v"(bv[1]), "+v"(bv[2])::"memory");
    f32x4 own[3][2];
#pragma unroll
    for (int i = 0; i < 6; i++) {
      const u32x2 a = xv[i >> 1][i & 1], b = bv[i >> 1];
      own[i >> 1][i & 1] = f32x4{blo(a[0]) + blo(b[0]), bhi(a[0]) + bhi(b[0]), blo(a[1]) + blo(b[1]), bhi(a[1]) + bhi(b[1])};
      PIN(own[i >> 1][i & 1]);
    }
    __builtin_amdgcn_s_sleep(TSLEEP);
    poll4(rw, dws, u32x4{~tag, 0, 0, 0}, 0, fio, tag);
    u32x4 pp[3][3];
#pragma unroll
    for (int u = 2; u >= 0; u--)
#pragma unroll
      for (int s = 0; s < 3; s++) {
        const int b = (bb + 1 + s) & 3, d = bb - b;
        BLD("dwordx4", pp[s][u], dws, (lane * 16) | (u ? 0u : v0), hrow + 9 * (4 * b + q) * 2048 + ((d < 0 ? 2 * d + 6 : 2 * d - 3) + u) * 1024, " sc1");
      }
    BARX();
#pragma unroll
    for (int i = 0; i < 6; i++) { own[i >> 1][i & 1] += *(LAS f32x4*)(smem + 3 * SLOT + c * 6144 + i * 1024 + lane * 16); PIN(own[i >> 1][i & 1]); }
    asm volatile("s_waitcnt vmcnt(0)" : "+v"(pp[0][0]), "+v"(pp[0][1]), "+v"(pp[0][2]), "+v"(pp[1][0]), "+v"(pp[1][1]), "+v"(pp[1][2]), "+v"(pp[2][0]), "+v"(pp[2][1]), "+v"(pp[2][2])::"memory");
    const rsrc_t ro = mkrsrc(out, 2048 * 1152);
    auto fin = [&](int u, int h) AI {
      float v[4];
#pragma unroll
      for (int e = 0; e < 4; e++) {
        const unsigned w0 = pp[0][u][2 * h + (e >> 1)], w1 = pp[1][u][2 * h + (e >> 1)], w2_ = pp[2][u][2 * h + (e >> 1)];
        v[e] = (e & 1) ? (own[u][h][e] + bhi(w0)) + (bhi(w1) + bhi(w2_)) : (own[u][h][e] + blo(w0)) + (blo(w1) + blo(w2_));
      }
      return u32x2{pk2(v[0], v[1]), pk2(v[2], v[3])};
    };
#pragma unroll
    for (int h = 0; h < 2; h++) {
      const u32x2 o1 = fin(1, h), o2 = fin(2, h);
      const auto s0 = __builtin_amdgcn_permlane16_swap(o1[0], o2[0], false, false), s1 = __builtin_amdgcn_permlane16_swap(o1[1], o2[1], false, false);
      const unsigned a0 = s0[0], a1 = s1[0], a2 = s0[1], a3 = s1[1];
      st16x<OAUX>(ro, orow + (g & 1) * 32 + (g >> 1) * 16, ocol + 32 + h * 589824, u32x4{a0, a1, a2, a3});
    }
    if (bb == 0) {
      const u32x2 o1 = fin(0, 0), o2 = fin(0, 1);
      const auto s0 = __builtin_amdgcn_permlane16_swap(o1[0], o2[0], false, false), s1 = __builtin_amdgcn_permlane16_swap(o1[1], o2[1], false, false);
      const unsigned a0 = s0[0], a1 = s1[0], a2 = s0[1], a3 = s1[1];
      st16x<OAUX>(ro, orow + (g & 1) * 589824 + (g >> 1) * 16, ocol, u32x4{a0, a1, a2, a3});
    }
    if (STAMPW) { WAITV(0); }
  }
  asm volatile(".Lkend%0:" ::"i"(decltype(SETC)::v));
  };
  if (wv < 4) body(IC<0>{}); else if ((blockIdx.x & 3) * 4 + ((blockIdx.x >> 3) & 3) < 9) body(IC<1>{}); else body(IC<3>{});
}

extern "C" void kernel_launch(void* const* d_in, const int* in_sizes, int n_in, void* d_out, int out_size, void* d_ws, size_t ws_size, hipStream_t stream) {
  (void)hipFuncSetAttribute((const void*)kmain, hipFuncAttributeMaxDynamicSharedMemorySize, LDSZ);
  kmain<<<256, 512, LDSZ, stream>>>((const u16*)d_in[0], (const u16*)d_in[1], (const u16*)d_in[2], (const u16*)d_in[4], (const u16*)d_in[5],
                                    (const u16*)d_in[6], (const u16*)d_in[7], (u16*)d_out, (char*)d_ws);
}
```
